```python
import torch

def _softmax(x, axis=-1):
    return torch.softmax(x, dim=axis)

def _gelu(x):
    return torch.nn.functional.gelu(x, approximate='tanh')

def _silu(x):
    return torch.nn.functional.silu(x)

def _sigmoid(x):
    return torch.sigmoid(x)

def _layernorm(x, axis=-1, eps=1e-05):
    m = x.mean(dim=axis, keepdim=True)
    v = x.var(dim=axis, keepdim=True, unbiased=False)
    return (x - m) / torch.sqrt(v + eps)

def _scatter_add(tgt, idx, src, axis=0):
    return tgt.clone().index_add_(axis, idx, src)

def _segment_sum(src, seg, n):
    out = torch.zeros((n,) + src.shape[1:], dtype=src.dtype)
    return out.index_add_(0, seg, src)

def _pad(x, pw):
    flat = tuple((v for pair in reversed(pw) for v in pair))
    return torch.nn.functional.pad(x, flat)
kernel_name = 'adaLN_cond__ssm_conv_s4__glu_gated_glu__prompt'

def setup_inputs():

    _w = lambda a: (lambda t: t.to(torch.bfloat16) if t.is_floating_point() else t)(torch.as_tensor(a))

    def _A_setup():
        B, L, D, N = (1, 2048, 448, 256)
        A_re = -(lambda _x: torch.exp(torch.as_tensor(_x)))((lambda *s: torch.randn(*s, dtype=torch.bfloat16) * 3.2)(D, N))
        A_im = torch.pi * (lambda *s: torch.randn(*s, dtype=torch.bfloat16) * 0.2)(D, N)
        B_re = (lambda *s: torch.randn(*s, dtype=torch.bfloat16) * 0.2)(D, N)
        B_im = (lambda *s: torch.randn(*s, dtype=torch.bfloat16) * 0.2)(D, N)
        C_re = (lambda *s: torch.randn(*s, dtype=torch.bfloat16) * 0.2)(D, N)
        C_im = (lambda *s: torch.randn(*s, dtype=torch.bfloat16) * 0.2)(D, N)
        log_dt = (lambda *s: torch.randn(*s, dtype=torch.bfloat16) * 0.2)(D) * 0.5 - 4.0
        Dskip = (lambda *s: torch.randn(*s, dtype=torch.bfloat16) * 0.2)(D)
        return {'u': _w((lambda *s: torch.randn(*s, dtype=torch.bfloat16) * 0.05)(B, L, D)), 'A_re': _w(A_re), 'A_im': _w(A_im), 'B_re': _w(B_re), 'B_im': _w(B_im), 'C_re': _w(C_re), 'C_im': _w(C_im), 'log_dt': _w(log_dt), 'Dskip': _w(Dskip)}
    _A_in = _A_setup()

    def _B_setup():
        B, S, D, F = (1, 2048, 448, 1792)
        return {'x': _w((lambda *s: torch.randn(*s, dtype=torch.bfloat16) * 0.05)(B, S, D)), 'w_gate': _w((lambda *s: torch.randn(*s, dtype=torch.bfloat16) * 0.4)(D, F)), 'w_up': _w((lambda *s: torch.randn(*s, dtype=torch.bfloat16) * 0.2)(D, F)), 'w_down': _w((lambda *s: torch.randn(*s, dtype=torch.bfloat16) * 0.2)(F, D))}
    _B_in = _B_setup()
    return {'A_u': _A_in['u'], 'A_A_re': _A_in['A_re'], 'A_A_im': _A_in['A_im'], 'A_B_re': _A_in['B_re'], 'A_B_im': _A_in['B_im'], 'A_C_re': _A_in['C_re'], 'A_C_im': _A_in['C_im'], 'A_log_dt': _A_in['log_dt'], 'A_Dskip': _A_in['Dskip'], 'B_x': _B_in['x'], 'B_w_gate': _B_in['w_gate'], 'B_w_up': _B_in['w_up'], 'B_w_down': _B_in['w_down'], 'c': _w((lambda *s: torch.randn(*s, dtype=torch.bfloat16) * 0.4)(1, 448) * 8.0), 'w_mod': _w((lambda *s: torch.randn(*s, dtype=torch.bfloat16) * 0.4)(448, 6 * 448) * 448 ** (-0.5) * 8.0)}

def reference(A_u, A_A_re, A_A_im, A_B_re, A_B_im, A_C_re, A_C_im, A_log_dt, A_Dskip, B_x, B_w_gate, B_w_up, B_w_down, c, w_mod):

    def _A_ref(u, A_re, A_im, B_re, B_im, C_re, C_im, log_dt, Dskip):
        Bsz, L, D = u.shape
        dt = torch.exp(log_dt)[:, None]
        A = A_re + 1j * A_im
        Bc = B_re + 1j * B_im
        Cc = C_re + 1j * C_im
        logdA = dt * A
        dB = dt * Bc
        l = (lambda x, dt: x.to(dt))(torch.arange(L), A_re.dtype)
        P = torch.exp(logdA[:, :, None] * l[None, None, :])
        K = torch.einsum('dn,dnl,dn->dl', Cc, P, dB).real
        L2 = 2 * L
        Kp = _pad(K, ((0, 0), (0, L)))
        up = (lambda x, *d: x.permute(*d))(u, 0, 2, 1)
        up = _pad(up, ((0, 0), (0, 0), (0, L)))
        Kf = (lambda x, axes=(-2, -1): torch.fft.rfft2(x.float(), dim=axes))(Kp[:, None, :], axes=(-2, -1))
        Uf = (lambda x, axes=(-2, -1): torch.fft.rfft2(x.float(), dim=axes))(up[:, :, None, :], axes=(-2, -1))
        Yf = Uf * Kf[None, :, :, :]
        y = (lambda x, s, axes=(-2, -1): torch.fft.irfft2(x, s=s, dim=axes).to(torch.bfloat16))(Yf, (1, L2), axes=(-2, -1))[:, :, 0, :L]
        y = (lambda x, *d: x.permute(*d))(y, 0, 2, 1) + u * Dskip[None, None, :]
        return y

    def _B_ref(x, w_gate, w_up, w_down):
        g = torch.einsum('bsd,df->bsf', x, w_gate)
        u = torch.einsum('bsd,df->bsf', x, w_up)
        h = _sigmoid(g) * u
        return torch.einsum('bsf,fd->bsd', h, w_down)
    x = A_u
    mod = (_silu(c) @ w_mod).reshape(c.shape[0], 6, 1, -1)
    sa, ca, ga, sb, cb, gb = (mod[:, 0].clone(), mod[:, 1].clone(), mod[:, 2].clone(), mod[:, 3].clone(), mod[:, 4].clone(), mod[:, 5].clone())
    h = _layernorm(x, axis=-1) * (1 + ca) + sa
    x = x + ga * _A_ref(h, A_A_re, A_A_im, A_B_re, A_B_im, A_C_re, A_C_im, A_log_dt, A_Dskip)
    h = _layernorm(x, axis=-1) * (1 + cb) + sb
    x = x + gb * _B_ref(h, B_w_gate, B_w_up, B_w_down)
    return x
```

```cpp
#include <hip/hip_runtime.h>
#include <hip/hip_bf16.h>
#include <stdint.h>

typedef unsigned short u16;
typedef unsigned int u32;
typedef __attribute__((ext_vector_type(8))) short s16x8;
typedef __attribute__((ext_vector_type(4))) short s16x4;
typedef __attribute__((ext_vector_type(8))) _Float16 f16x8;
typedef __attribute__((ext_vector_type(2))) _Float16 f16x2;
typedef __attribute__((ext_vector_type(2))) __bf16 bf16x2;
typedef __attribute__((ext_vector_type(8))) __bf16 bf16x8;
typedef __attribute__((ext_vector_type(2))) float f32x2;
typedef __attribute__((ext_vector_type(4))) float f32x4;
typedef __attribute__((ext_vector_type(16))) float f32x16;
typedef __attribute__((ext_vector_type(2))) unsigned u32x2;
typedef __attribute__((ext_vector_type(4))) unsigned u32x4;
typedef __attribute__((address_space(3))) char* lds_ptr;
typedef __attribute__((address_space(3))) const char* lds_cptr;

constexpr int L = 2048, D = 448, NS = 256, F = 1792;
constexpr int RLEN = 2128;

constexpr size_t WS_MOD = 0;
constexpr size_t WS_KSC = 16384;
constexpr size_t WS_UV = 32768;
constexpr size_t WS_XT = 65536;
constexpr size_t WS_XNT = WS_XT + (size_t)D * L * 2;
constexpr size_t WS_KR = WS_XNT + (size_t)D * L * 2;
constexpr size_t WS_WDF = WS_KR + (size_t)D * 2 * RLEN * 2;
constexpr size_t WS_WF = WS_WDF + (size_t)F * D * 2;
constexpr size_t WS_X1T = WS_WF + (size_t)2 * F * D * 2;
constexpr size_t WS_X1R = WS_X1T + (size_t)D * L * 2;
constexpr size_t WS_HH = WS_X1R + (size_t)D * L * 2;

struct Params {
  const u16* u; const u16* A_re; const u16* A_im; const u16* B_re; const u16* B_im; const u16* C_re; const u16* C_im;
  const u16* log_dt; const u16* Dskip; const u16* w_gate; const u16* w_up; const u16* w_down; const u16* c; const u16* w_mod;
  char* ws; u16* out;
};

__device__ __forceinline__ float bf2f(u16 v) { return __uint_as_float((u32)v << 16); }
__device__ __forceinline__ float bflo(u32 v) { return __uint_as_float(v << 16); }
__device__ __forceinline__ float bfhi(u32 v) { return __uint_as_float(v & 0xffff0000u); }
__device__ __forceinline__ u32 pkbf(float lo, float hi) {
  f32x2 v = {lo, hi}; bf16x2 b = __builtin_convertvector(v, bf16x2); return __builtin_bit_cast(u32, b);
}
__device__ __forceinline__ float bfr(float x) { return bflo(pkbf(x, x)); }
__device__ __forceinline__ u32 pkh(float lo, float hi) {
  f32x2 v = {lo, hi}; f16x2 b = __builtin_convertvector(v, f16x2); return __builtin_bit_cast(u32, b);
}
template <int CTRL> __device__ __forceinline__ float dppf(float x) {
  return __builtin_bit_cast(float, __builtin_amdgcn_mov_dpp(__builtin_bit_cast(int, x), CTRL, 0xf, 0xf, true));
}
__device__ __forceinline__ float xrow16_sum(float x) {
  auto s = __builtin_amdgcn_permlane16_swap(__float_as_uint(x), __float_as_uint(x), false, false);
  x = __uint_as_float(s[0]) + __uint_as_float(s[1]);
  auto t = __builtin_amdgcn_permlane32_swap(__float_as_uint(x), __float_as_uint(x), false, false);
  return __uint_as_float(t[0]) + __uint_as_float(t[1]);
}
__device__ __forceinline__ float xrow16_max(float x) {
  auto s = __builtin_amdgcn_permlane16_swap(__float_as_uint(x), __float_as_uint(x), false, false);
  x = fmaxf(__uint_as_float(s[0]), __uint_as_float(s[1]));
  auto t = __builtin_amdgcn_permlane32_swap(__float_as_uint(x), __float_as_uint(x), false, false);
  return fmaxf(__uint_as_float(t[0]), __uint_as_float(t[1]));
}
__device__ __forceinline__ float wave_sum(float x) {
  x += dppf<0xB1>(x); x += dppf<0x4E>(x); x += dppf<0x141>(x); x += dppf<0x128>(x);
  return xrow16_sum(x);
}
__device__ __forceinline__ float wave_max(float x) {
  x = fmaxf(x, dppf<0xB1>(x)); x = fmaxf(x, dppf<0x4E>(x)); x = fmaxf(x, dppf<0x141>(x)); x = fmaxf(x, dppf<0x128>(x));
  return xrow16_max(x);
}
__device__ __forceinline__ float dot2bf(u32 a, u32 b, float acc) {
  return __builtin_amdgcn_fdot2_f32_bf16(__builtin_bit_cast(bf16x2, a), __builtin_bit_cast(bf16x2, b), acc, false);
}
typedef short v4i16_t __attribute__((ext_vector_type(4)));
__device__ __forceinline__ u32x2 tr_read(lds_cptr p) {
  return __builtin_bit_cast(u32x2, __builtin_amdgcn_ds_read_tr16_b64_v4i16((__attribute__((address_space(3))) v4i16_t*)p));
}
__device__ __forceinline__ void glds16(const void* g, unsigned lds_base) {
  unsigned sv;
  asm volatile("s_mov_b32 %0, m0\n\ts_mov_b32 m0, %2\n\ts_nop 0\n\tglobal_load_lds_dwordx4 %1, off\n\ts_mov_b32 m0, %0"
               : "=&s"(sv) : "v"(g), "s"(lds_base) : "memory");
}
__device__ __forceinline__ float pow2_scale(float x, int target) {
  int e = (int)((__float_as_uint(x) >> 23) & 255u);
  if (e == 0 || e == 255) return 1.0f;
  int s = target - (e - 127);
  s = s < -100 ? -100 : (s > 100 ? 100 : s);
  return __uint_as_float((u32)(127 + s) << 23);
}
struct cpx { float r, i; };
__device__ __forceinline__ cpx cmul(cpx a, cpx b) { return {a.r * b.r - a.i * b.i, a.r * b.i + a.i * b.r}; }
__device__ __forceinline__ cpx cexpm(float zr, float zi, float m) {
  float mag = __builtin_amdgcn_exp2f(zr * (m * 1.4426950408889634f));
  float rev = __builtin_amdgcn_fractf(zi * (m * 0.15915494309189535f));
  return {mag * __builtin_amdgcn_cosf(rev), mag * __builtin_amdgcn_sinf(rev)};
}

constexpr int A_NKG = 448, A_NLN = 128, A_NMOD = 42, A_NWD = 98;
constexpr int A_LDS = 4 * 32 * 272;

__device__ __forceinline__ void kgen_role(const Params& p, int d, char* smem) {
  const int tid = threadIdx.x, lane = tid & 63, w = tid >> 6;
  const int idx = d * NS + tid;
  const float are = bf2f(p.A_re[idx]), aim = bf2f(p.A_im[idx]);
  const float bre = bf2f(p.B_re[idx]), bim = bf2f(p.B_im[idx]);
  const float cre = bf2f(p.C_re[idx]), cim = bf2f(p.C_im[idx]);
  const float dt = bfr(expf(bf2f(p.log_dt[d])));
  const float zr = dt * are, zi = dt * aim;
  const float br = dt * bre, bi = dt * bim;
  cpx W = {cre * br - cim * bi, cre * bi + cim * br};
  const float wmax = wave_max(fmaxf(fabsf(W.r), fabsf(W.i)));
  const float wsum = wave_sum(fabsf(W.r) + fabsf(W.i));
  const float sw = pow2_scale(wmax, 13);
  W.r *= sw; W.i *= sw;

  char* tab = smem + w * (32 * 272);
  const cpx E1 = cexpm(zr, zi, 1.f), E8 = cexpm(zr, zi, 8.f), E256 = cexpm(zr, zi, 256.f);
  {
    const cpx E16 = cmul(E8, E8), E32 = cmul(E16, E16);
    cpx q0 = {1.f, 0.f}, q1 = E8, q2 = E16, q3 = cmul(E16, E8);
#pragma unroll
    for (int g = 0; g < 8; ++g) {
      *(u32*)(tab + (4 * g + 0) * 272 + lane * 4) = pkh(q0.r, q0.i);
      *(u32*)(tab + (4 * g + 1) * 272 + lane * 4) = pkh(q1.r, q1.i);
      *(u32*)(tab + (4 * g + 2) * 272 + lane * 4) = pkh(q2.r, q2.i);
      *(u32*)(tab + (4 * g + 3) * 272 + lane * 4) = pkh(q3.r, q3.i);
      if (g < 7) { q0 = cmul(q0, E32); q1 = cmul(q1, E32); q2 = cmul(q2, E32); q3 = cmul(q3, E32); }
    }
  }
  f16x8 bfr_[8];
  const int r32 = lane & 31, hh = lane >> 5;
#pragma unroll
  for (int s = 0; s < 8; ++s) bfr_[s] = *(const f16x8*)(tab + r32 * 272 + 32 * s + 16 * hh);
  {
    const cpx E2 = cmul(E1, E1), E4 = cmul(E2, E2);
    cpx t = W;
#define PUT(rr, vv) *(u32*)(tab + (rr) * 272 + lane * 4) = pkh((vv).r, -(vv).i)
    asm volatile("s_waitcnt lgkmcnt(0)" ::: "memory");
#pragma unroll
    for (int q = 0; q < 8; ++q) { PUT(q, t); if (q < 7) t = cmul(t, E1); }
    const cpx W1 = cmul(W, E256);
    t = W1;
#pragma unroll
    for (int q = 0; q < 4; ++q) { PUT(8 + q, t); if (q < 3) t = cmul(t, E2); }
    const cpx W2 = cmul(W1, E256);
    const cpx W3 = cmul(W2, E256);
    PUT(12, W2); t = cmul(W2, E4); PUT(13, t);
    PUT(14, W3); t = cmul(W3, E4); PUT(15, t);
    t = cmul(W3, E256);
#pragma unroll
    for (int q = 0; q < 5; ++q) { PUT(16 + q, t); if (q < 4) t = cmul(t, E256); }
#undef PUT
  }
  f32x16 acc = {};
#pragma unroll
  for (int s = 0; s < 8; ++s) {
    f16x8 a = *(const f16x8*)(tab + r32 * 272 + 32 * s + 16 * hh);
    acc = __builtin_amdgcn_mfma_f32_32x32x16_f16(a, bfr_[s], acc, 0, 0, 0);
  }
  __syncthreads();
  float* red = (float*)smem;
  const float isw = 1.0f / sw;
#pragma unroll
  for (int r = 0; r < 12; ++r) red[(w * 12 + r) * 64 + lane] = acc[r] * isw;
  float* wsums = (float*)(smem + 4 * 12 * 64 * 4);
  float* ktab = (float*)(smem + 4 * 12 * 64 * 4 + 64);
  if (lane == 0) wsums[w] = wsum;
  __syncthreads();
  const float S = wsums[0] + wsums[1] + wsums[2] + wsums[3];
  const float kt = pow2_scale(S, 14);
  for (int e = tid; e < 12 * 64; e += 256) {
    const int r = e >> 6, l = e & 63;
    const float v = red[e] + red[768 + e] + red[1536 + e] + red[2304 + e];
    const int row = (r & 3) + 8 * (r >> 2) + 4 * (l >> 5);
    ktab[row * 32 + (l & 31)] = v * kt;
  }
  if (tid == 0) ((float*)(p.ws + WS_KSC))[d] = 1.0f / kt;
  __syncthreads();
  u16* R0 = (u16*)(p.ws + WS_KR) + (size_t)d * 2 * RLEN;
  u16* R1 = R0 + RLEN;
  for (int t = tid; t < RLEN / 8; t += 256) {
    float v[9];
#pragma unroll
    for (int j = 0; j < 9; ++j) {
      const int i = 8 * t + j;
      const int l = 2048 - i;
      float val = 0.f;
      if (i >= 1 && l >= 0) {
        const int lb = (int)bfr((float)l);
        int row, col;
        if (lb < 256) { row = lb & 7; col = lb >> 3; }
        else if (lb < 512) { row = 8 + ((lb >> 1) & 3); col = (lb - 256) >> 3; }
        else if (lb < 1024) { row = 12 + 2 * ((lb - 512) >> 8) + ((lb >> 2) & 1); col = (lb & 255) >> 3; }
        else { row = 16 + ((lb - 1024) >> 8); col = (lb & 255) >> 3; }
        val = ktab[row * 32 + col];
      }
      v[j] = val;
    }
    u32x4 o0 = {pkh(v[0], v[1]), pkh(v[2], v[3]), pkh(v[4], v[5]), pkh(v[6], v[7])};
    u32x4 o1 = {pkh(v[1], v[2]), pkh(v[3], v[4]), pkh(v[5], v[6]), pkh(v[7], v[8])};
    *(u32x4*)(R0 + 8 * t) = o0;
    *(u32x4*)(R1 + 8 * t) = o1;
  }
}

__device__ __forceinline__ void ln1_role(const Params& p, int j, char* smem) {
  const int tid = threadIdx.x, lane = tid & 63, w = tid >> 6;
  const int rg = (j & 7) * 16 + (j >> 3);
  const int l0 = rg * 16;
  constexpr int RS = 960;
  char* xs = smem;
  char* xn = smem + 16 * RS;
  const bool act = lane < 56;
  u32x4 v[4];
#pragma unroll
  for (int r = 0; r < 4; ++r) {
    v[r] = u32x4{0, 0, 0, 0};
    if (act) v[r] = *(const u32x4*)(p.u + (size_t)(l0 + 4 * w + r) * D + lane * 8);
  }
#pragma unroll
  for (int r = 0; r < 4; ++r) {
    float x[8];
#pragma unroll
    for (int k = 0; k < 4; ++k) { x[2 * k] = bflo(v[r][k]); x[2 * k + 1] = bfhi(v[r][k]); }
    float s = 0.f;
#pragma unroll
    for (int k = 0; k < 8; ++k) s += x[k];
    s = wave_sum(s);
    const float mean = s * (1.0f / D);
    float q = 0.f;
#pragma unroll
    for (int k = 0; k < 8; ++k) { const float t = x[k] - mean; q += t * t; }
    if (!act) q = 0.f;
    q = wave_sum(q);
    const float m = bfr(mean);
    const float var = bfr(q * (1.0f / D));
    const float sd = bfr(sqrtf(bfr(var + 1e-5f)));
    const float rs = 1.0f / sd;
    u32x4 o;
#pragma unroll
    for (int k = 0; k < 4; ++k) {
      float a = bfr(x[2 * k] - m), b = bfr(x[2 * k + 1] - m);
      float qa = a * rs, qb = b * rs;
      qa = fmaf(fmaf(-qa, sd, a), rs, qa); qb = fmaf(fmaf(-qb, sd, b), rs, qb);
      o[k] = pkbf(qa, qb);
    }
    if (act) {
      *(u32x4*)(xs + (4 * w + r) * RS + lane * 16) = v[r];
      *(u32x4*)(xn + (4 * w + r) * RS + lane * 16) = o;
    }
  }
  __syncthreads();
  const int g = lane >> 4, q4 = (lane & 15) >> 2, pp = lane & 3;
  for (int task = w; task < 14; task += 4) {
    const int arr = task >= 7, ps = arr ? task - 7 : task;
    lds_cptr base = (lds_cptr)(arr ? xn : xs) + q4 * RS + (ps * 64 + g * 16 + pp * 4) * 2;
    u32x2 t0 = tr_read(base), t1 = tr_read(base + 4 * RS), t2 = tr_read(base + 8 * RS), t3 = tr_read(base + 12 * RS);
    const int d = ps * 64 + lane;
    u16* dst = (u16*)(p.ws + (arr ? WS_XNT : WS_XT)) + (size_t)d * L + l0;
    *(u32x4*)dst = u32x4{t0[0], t0[1], t1[0], t1[1]};
    *(u32x4*)(dst + 8) = u32x4{t2[0], t2[1], t3[0], t3[1]};
  }
}

__device__ __forceinline__ void mod_role(const Params& p, int j, char* smem) {
  const int tid = threadIdx.x;
  const int cg = tid & 7, rg = tid >> 3;
  const int c0 = j * 64 + cg * 8;
  u32x4 v[14]; float s[14];
#pragma unroll
  for (int i = 0; i < 14; ++i) v[i] = *(const u32x4*)(p.w_mod + (size_t)(rg + 32 * i) * (6 * D) + c0);
#pragma unroll
  for (int i = 0; i < 14; ++i) {
    const float cv = bf2f(p.c[rg + 32 * i]);
    s[i] = bfr(cv / (1.0f + expf(-cv)));
  }
  float acc[8] = {};
#pragma unroll
  for (int i = 0; i < 14; ++i)
#pragma unroll
    for (int k = 0; k < 4; ++k) { acc[2 * k] += s[i] * bflo(v[i][k]); acc[2 * k + 1] += s[i] * bfhi(v[i][k]); }
  float* red = (float*)smem;
#pragma unroll
  for (int k = 0; k < 8; ++k) red[rg * 64 + cg * 8 + k] = acc[k];
  __syncthreads();
  if (tid < 64) {
    float t = 0.f;
#pragma unroll
    for (int r = 0; r < 32; ++r) t += red[r * 64 + tid];
    ((float*)(p.ws + WS_MOD))[j * 64 + tid] = bfr(t);
  }
}

__device__ __forceinline__ void wd_role(const Params& p, int j, char* smem) {
  const int tid = threadIdx.x, lane = tid & 63, w = tid >> 6;
  const int fb = j / 7, db = j % 7;
  constexpr int RS = 144;
#pragma unroll
  for (int i = 0; i < 4; ++i) {
    const int r = (tid >> 3) + 32 * i;
    u32x4 v = *(const u32x4*)(p.w_down + (size_t)(fb * 128 + r) * D + db * 64 + (tid & 7) * 8);
    *(u32x4*)(smem + r * RS + (tid & 7) * 16) = v;
  }
  __syncthreads();
  const int g = lane >> 4, q4 = (lane & 15) >> 2, pp = lane & 3;
  u16* wdf = (u16*)(p.ws + WS_WDF);
#pragma unroll
  for (int i = 0; i < 4; ++i) {
    const int ks = w, dt = i;
    lds_cptr base = (lds_cptr)smem + (32 * ks + 8 * g + q4) * RS + (16 * dt + 4 * pp) * 2;
    u32x2 t0 = tr_read(base), t1 = tr_read(base + 4 * RS);
    *(u32x4*)(wdf + ((size_t)((db * 4 + dt) * 56 + fb * 4 + ks) * 64 + lane) * 8) = u32x4{t0[0], t0[1], t1[0], t1[1]};
  }
}

__global__ __launch_bounds__(256) void kA(Params p) {
  extern __shared__ __attribute__((aligned(16))) char smem[];
  int b = blockIdx.x;
  if (b < A_NLN) { ln1_role(p, b, smem); return; }
  b -= A_NLN;
  if (b < A_NKG) { kgen_role(p, b, smem); return; }
  b -= A_NKG;
  if (b < A_NMOD) { mod_role(p, b, smem); return; }
  b -= A_NMOD;
  wd_role(p, b, smem);
}

constexpr int B_NCONV = 224, B_NWP = 56;
constexpr int B_IMG = 33280, B_HOFF = 2 * B_IMG, B_HCH = 96 * 80, B_LDS = B_HOFF + 2 * B_HCH;

__device__ __forceinline__ void conv_role(const Params& p, int b, char* smem) {
  const int tid = threadIdx.x, lane = tid & 63;
  const int w = __builtin_amdgcn_readfirstlane(tid >> 6);
  const int ch = w >> 2, pq = w & 3;
  const int d = 2 * b + ch;
  const unsigned lds0 = (unsigned)(uintptr_t)smem;
  {
    const u16* R0 = (const u16*)(p.ws + WS_KR) + (size_t)d * 2 * RLEN;
    const int s = lane & 7;
    const u16* src = R0 + (s & 1) * RLEN + (s & ~1) + (lane >> 3) * 8;
#pragma unroll
    for (int i = 0; i < 8; ++i) {
      const int pc = pq + 4 * i;
      glds16(src + pc * 64, lds0 + ch * B_IMG + pc * 1024);
    }
    if (pq == 0 && lane < 32) glds16(src + 32 * 64, lds0 + ch * B_IMG + 32 * 1024);
  }
  const float* mod = (const float*)(p.ws + WS_MOD);
  const float sa = mod[d], ca1 = bfr(1.0f + mod[D + d]), ga = mod[2 * D + d];
  const float dsk = bf2f(p.Dskip[d]);
  const float ksc = ((const float*)(p.ws + WS_KSC))[d];
  const float hs = pow2_scale(22.0f * fabsf(ca1) + fabsf(sa), 14);
  const u16* xnT = (const u16*)(p.ws + WS_XNT) + (size_t)d * L;
  const u16* xT = (const u16*)(p.ws + WS_XT) + (size_t)d * L;
  {
    const int t = tid & 255;
    char* hb = smem + B_HOFF + ch * B_HCH;
    u32x4 v = *(const u32x4*)(xnT + t * 8);
    u32x4 o;
#pragma unroll
    for (int k = 0; k < 4; ++k) {
      const float h0 = bfr(bfr(bflo(v[k]) * ca1) + sa), h1 = bfr(bfr(bfhi(v[k]) * ca1) + sa);
      o[k] = pkh(h0 * hs, h1 * hs);
    }
    *(u32x4*)(hb + (32 + (t >> 2)) * 80 + (t & 3) * 16) = o;
    if (t < 160) *(u32x4*)(hb + t * 16) = u32x4{0, 0, 0, 0};
  }
  const int a = lane & 31, hh = lane >> 5;
  const int lout = 32 * (32 * (pq >> 1) + a) + 16 * (pq & 1) + 8 * hh;
  const u32x4 exn = *(const u32x4*)(xnT + lout);
  const u32x4 ex = *(const u32x4*)(xT + lout);

  asm volatile("s_waitcnt vmcnt(0)" ::: "memory");
  __syncthreads();

  const int cb = (a + 7) >> 3, s = (8 - (a & 7)) & 7;
  lds_cptr abase = (lds_cptr)smem + ch * B_IMG + 128 * (256 - 4 * (8 * pq) + hh - cb) + 16 * s;
  lds_cptr bbase = (lds_cptr)smem + B_HOFF + ch * B_HCH + (a + 32 - 8 * pq) * 80 + 16 * hh;
  f32x16 acc0 = {}, acc1 = {}, acc0s = {}, acc1s = {};
  const bool up = (lane & 16) != 0;
  u32x4 fprevL = *(const __attribute__((address_space(3))) u32x4*)(abase + 512);
  u32x4 fprevH = *(const __attribute__((address_space(3))) u32x4*)(abase + 512 - 16384);
#pragma unroll
  for (int i = 0; i < 8; ++i) {
    const u32x4 fL = *(const __attribute__((address_space(3))) u32x4*)(abase - 512 * i);
    const u32x4 fH = *(const __attribute__((address_space(3))) u32x4*)(abase - 512 * i - 16384);
    const u32x4 b00 = *(const __attribute__((address_space(3))) u32x4*)(bbase - 80 * i);
    const u32x4 b01 = *(const __attribute__((address_space(3))) u32x4*)(bbase - 80 * i + 32);
    const u32x4 b10 = *(const __attribute__((address_space(3))) u32x4*)(bbase - 80 * i + 2560);
    const u32x4 b11 = *(const __attribute__((address_space(3))) u32x4*)(bbase - 80 * i + 2560 + 32);
    u32x4 sL, sH;
#pragma unroll
    for (int k = 0; k < 4; ++k) { sL[k] = up ? fprevL[k] : fL[k]; sH[k] = up ? fprevH[k] : fH[k]; }
#define H8(x) __builtin_bit_cast(f16x8, x)
    acc0 = __builtin_amdgcn_mfma_f32_32x32x16_f16(H8(fL), H8(b00), acc0, 0, 0, 0);
    acc1 = __builtin_amdgcn_mfma_f32_32x32x16_f16(H8(fL), H8(b10), acc1, 0, 0, 0);
    acc1 = __builtin_amdgcn_mfma_f32_32x32x16_f16(H8(fH), H8(b00), acc1, 0, 0, 0);
    acc0s = __builtin_amdgcn_mfma_f32_32x32x16_f16(H8(sL), H8(b01), acc0s, 0, 0, 0);
    acc1s = __builtin_amdgcn_mfma_f32_32x32x16_f16(H8(sL), H8(b11), acc1s, 0, 0, 0);
    acc1s = __builtin_amdgcn_mfma_f32_32x32x16_f16(H8(sH), H8(b01), acc1s, 0, 0, 0);
#undef H8
    fprevL = fL; fprevH = fH;
  }
#pragma unroll
  for (int r = 0; r < 16; ++r) { acc0[r] += acc0s[r ^ 8]; acc1[r] += acc1s[r ^ 8]; }
  __syncthreads();
  {
    char* red = smem + ch * (4 * 4 * 2 * 1024);
#pragma unroll
    for (int q = 0; q < 4; ++q) {
      if (q != pq) {
        const f32x16& t = (q >> 1) ? acc1 : acc0;
        const int r0 = 8 * (q & 1);
        *(f32x4*)(red + ((pq * 4 + q) * 2 + 0) * 1024 + lane * 16) = f32x4{t[r0], t[r0 + 1], t[r0 + 2], t[r0 + 3]};
        *(f32x4*)(red + ((pq * 4 + q) * 2 + 1) * 1024 + lane * 16) = f32x4{t[r0 + 4], t[r0 + 5], t[r0 + 6], t[r0 + 7]};
      }
    }
    __syncthreads();
    f32x4 y0, y1;
    {
      if (pq == 0) { y0 = f32x4{acc0[0], acc0[1], acc0[2], acc0[3]}; y1 = f32x4{acc0[4], acc0[5], acc0[6], acc0[7]}; }
      else if (pq == 1) { y0 = f32x4{acc0[8], acc0[9], acc0[10], acc0[11]}; y1 = f32x4{acc0[12], acc0[13], acc0[14], acc0[15]}; }
      else if (pq == 2) { y0 = f32x4{acc1[0], acc1[1], acc1[2], acc1[3]}; y1 = f32x4{acc1[4], acc1[5], acc1[6], acc1[7]}; }
      else { y0 = f32x4{acc1[8], acc1[9], acc1[10], acc1[11]}; y1 = f32x4{acc1[12], acc1[13], acc1[14], acc1[15]}; }
    }
#pragma unroll
    for (int sw = 0; sw < 4; ++sw) {
      if (sw != pq) {
        y0 += *(const f32x4*)(red + ((sw * 4 + pq) * 2 + 0) * 1024 + lane * 16);
        y1 += *(const f32x4*)(red + ((sw * 4 + pq) * 2 + 1) * 1024 + lane * 16);
      }
    }
    float y[8];
#pragma unroll
    for (int k = 0; k < 4; ++k) {
      auto r = __builtin_amdgcn_permlane32_swap(__float_as_uint(y0[k]), __float_as_uint(y1[k]), false, false);
      y[k] = __uint_as_float(r[0]); y[4 + k] = __uint_as_float(r[1]);
    }
    const float ysc = ksc / hs;
    u32x4 o;
#pragma unroll
    for (int k = 0; k < 4; ++k) {
      float r2[2];
#pragma unroll
      for (int e = 0; e < 2; ++e) {
        const float xn = e ? bfhi(exn[k]) : bflo(exn[k]);
        const float xr = e ? bfhi(ex[k]) : bflo(ex[k]);
        const float h = bfr(bfr(xn * ca1) + sa);
        const float yy = bfr(y[2 * k + e] * ysc);
        const float t = bfr(yy + bfr(h * dsk));
        r2[e] = xr + bfr(ga * t);
      }
      o[k] = pkbf(r2[0], r2[1]);
    }
    *(u32x4*)((u16*)(p.ws + WS_X1T) + (size_t)d * L + lout) = o;
  }
}

__device__ __forceinline__ void wp_role(const Params& p, int j, char* smem) {
  const int tid = threadIdx.x, lane = tid & 63, w = tid >> 6;
  const int mat = j & 1, fb = j >> 1;
  const u16* W = mat ? p.w_up : p.w_gate;
  const float* mod = (const float*)(p.ws + WS_MOD);
  constexpr int RS = 144;
  const int cg = tid & 7, rg = tid >> 3;
  u32x4 v[7];
#pragma unroll
  for (int i = 0; i < 7; ++i) v[i] = *(const u32x4*)(W + (size_t)(rg + 64 * i) * F + fb * 64 + cg * 8);
  float us[8] = {}, vs[8] = {};
#pragma unroll
  for (int i = 0; i < 7; ++i) {
    const int r = rg + 64 * i;
    const float c1 = bfr(1.0f + mod[4 * D + r]), sb = mod[3 * D + r];
    u32x4 o;
#pragma unroll
    for (int k = 0; k < 4; ++k) {
      const float a = bflo(v[i][k]), b = bfhi(v[i][k]);
      o[k] = pkbf(a * c1, b * c1);
      us[2 * k] += bflo(o[k]); us[2 * k + 1] += bfhi(o[k]);
      vs[2 * k] += sb * a; vs[2 * k + 1] += sb * b;
    }
    *(u32x4*)(smem + r * RS + cg * 16) = o;
  }
  float* red = (float*)(smem + 448 * RS);
#pragma unroll
  for (int k = 0; k < 8; ++k) {
    us[k] += dppf<0x128>(us[k]); vs[k] += dppf<0x128>(vs[k]);
    us[k] = xrow16_sum(us[k]); vs[k] = xrow16_sum(vs[k]);
  }
  if (lane < 8) {
#pragma unroll
    for (int k = 0; k < 8; ++k) { red[(w * 2 + 0) * 64 + lane * 8 + k] = us[k]; red[(w * 2 + 1) * 64 + lane * 8 + k] = vs[k]; }
  }
  __syncthreads();
  if (tid < 128) {
    const int which = tid >> 6, cidx = tid & 63;
    float t = 0.f;
#pragma unroll
    for (int q = 0; q < 8; ++q) t += red[(q * 2 + which) * 64 + cidx];
    ((float*)(p.ws + WS_UV))[(mat * 2 + which) * F + fb * 64 + cidx] = t;
  }
  const int g = lane >> 4, q4 = (lane & 15) >> 2, pp = lane & 3;
  u16* wf = (u16*)(p.ws + WS_WF);
  for (int t = w; t < 56; t += 8) {
    const int ft = t / 14, ks = t % 14;
    lds_cptr base = (lds_cptr)smem + (32 * ks + 8 * g + q4) * RS + (16 * ft + 4 * pp) * 2;
    u32x2 t0 = tr_read(base), t1 = tr_read(base + 4 * RS);
    *(u32x4*)(wf + ((size_t)(((fb * 4 + ft) * 14 + ks) * 2 + mat) * 64 + lane) * 8) = u32x4{t0[0], t0[1], t1[0], t1[1]};
  }
}

__global__ __launch_bounds__(512) void kB(Params p) {
  extern __shared__ __attribute__((aligned(16))) char smem[];
  const int b = blockIdx.x;
  if (b < B_NCONV) conv_role(p, b, smem);
  else wp_role(p, b - B_NCONV, smem);
}

constexpr int C_XT = 448 * 256, C_LDS = C_XT + 1024;

__global__ __launch_bounds__(512) void kC(Params p) {
  extern __shared__ __attribute__((aligned(16))) char smem[];
  const int tid = threadIdx.x, lane = tid & 63;
  const int w = __builtin_amdgcn_readfirstlane(tid >> 6);
  const int xcd = blockIdx.x & 7, idx = blockIdx.x >> 3;
  const int Lt = 4 * (xcd >> 1) + idx / 7, Fi = 7 * (xcd & 1) + idx % 7;
  const unsigned lds0 = (unsigned)(uintptr_t)smem;
  const int f0 = 128 * Fi + 16 * w + 4 * (lane >> 4);
  const float* uv = (const float*)(p.ws + WS_UV);
  const f32x4 ug = *(const f32x4*)(uv + 0 * F + f0), vg = *(const f32x4*)(uv + 1 * F + f0);
  const f32x4 uu = *(const f32x4*)(uv + 2 * F + f0), vu = *(const f32x4*)(uv + 3 * F + f0);

  u32x4 wg[14], wu[14];
  {
    const int row = 4 * w + (lane >> 4);
    const int fsw = ((row & 3) << 2) | ((row >> 2) & 3);
    const u16* xsrc = (const u16*)(p.ws + WS_X1T) + (size_t)row * L + Lt * 128 + ((lane & 15) ^ fsw) * 8;
    const u16* wsrc = (const u16*)(p.ws + WS_WF) + ((size_t)(Fi * 8 + w) * 28 * 64 + lane) * 8;
#pragma unroll
    for (int ks = 0; ks < 14; ++ks) {
      glds16(xsrc + (size_t)ks * 32 * L, lds0 + ks * 8192 + w * 1024);
      asm volatile("global_load_dwordx4 %0, %1, off" : "=v"(wg[ks]) : "v"(wsrc + ks * 1024) : "memory");
      asm volatile("global_load_dwordx4 %0, %1, off" : "=v"(wu[ks]) : "v"(wsrc + ks * 1024 + 512) : "memory");
    }
  }
  f32x4 ag[8], au[8];
#pragma unroll
  for (int i = 0; i < 8; ++i) { ag[i] = f32x4{0, 0, 0, 0}; au[i] = f32x4{0, 0, 0, 0}; }
  float ssum = 0.f, ssq = 0.f;
  const int g = lane >> 4, q4 = (lane & 15) >> 2, pp = lane & 3;
  unsigned tb[2];
#pragma unroll
  for (int t = 0; t < 2; ++t) {
    const int row = 8 * g + 4 * t + q4;
    const int fsw = ((row & 3) << 2) | ((row >> 2) & 3);
    tb[t] = (unsigned)(256 * row + 16 * (((pp >> 1) ^ fsw) ^ (2 * w)) + 8 * (pp & 1));
  }
#define WAITV(n) asm volatile("s_waitcnt vmcnt(" #n ")\n\ts_barrier" : "+v"(wg[ks]), "+v"(wu[ks]) :: "memory")
#pragma unroll
  for (int ks = 0; ks < 14; ++ks) {
    switch (ks) {
      case 0: WAITV(39); break; case 1: WAITV(36); break; case 2: WAITV(33); break; case 3: WAITV(30); break;
      case 4: WAITV(27); break; case 5: WAITV(24); break; case 6: WAITV(21); break; case 7: WAITV(18); break;
      case 8: WAITV(15); break; case 9: WAITV(12); break; case 10: WAITV(9); break; case 11: WAITV(6); break;
      case 12: WAITV(3); break; default: WAITV(0); break;
    }
#pragma unroll
    for (int i = 0; i < 8; ++i) {
      const u32x2 t0 = tr_read((lds_cptr)smem + ks * 8192 + (tb[0] ^ (32 * i)));
      const u32x2 t1 = tr_read((lds_cptr)smem + ks * 8192 + (tb[1] ^ (32 * i)));
      const u32x4 xb = {t0[0], t0[1], t1[0], t1[1]};
      if (i == 0) {
#pragma unroll
        for (int k = 0; k < 4; ++k) { ssum = dot2bf(xb[k], 0x3f803f80u, ssum); ssq = dot2bf(xb[k], xb[k], ssq); }
      }
      ag[i] = __builtin_amdgcn_mfma_f32_16x16x32_bf16(__builtin_bit_cast(bf16x8, wg[ks]), __builtin_bit_cast(bf16x8, xb), ag[i], 0, 0, 0);
      au[i] = __builtin_amdgcn_mfma_f32_16x16x32_bf16(__builtin_bit_cast(bf16x8, wu[ks]), __builtin_bit_cast(bf16x8, xb), au[i], 0, 0, 0);
    }
  }
#undef WAITV
  ssum = xrow16_sum(ssum); ssq = xrow16_sum(ssq);
  float* stats = (float*)(smem + C_XT);
  {
    const float mean = ssum * (1.0f / D);
    const float var = fmaxf(ssq * (1.0f / D) - mean * mean, 0.f);
    const float m = bfr(mean);
    const float sd = bfr(sqrtf(bfr(bfr(var) + 1e-5f)));
    if (lane < 16) *(f32x2*)(stats + (16 * w + lane) * 2) = f32x2{m, 1.0f / sd};
  }
  {
    const u32x2 t0 = tr_read((lds_cptr)smem + Fi * 8192 + tb[0]);
    const u32x2 t1 = tr_read((lds_cptr)smem + Fi * 8192 + tb[1]);
    *(u32x4*)((u16*)(p.ws + WS_X1R) + (size_t)(Lt * 128 + 16 * w + (lane & 15)) * D + 32 * Fi + 8 * g) = u32x4{t0[0], t0[1], t1[0], t1[1]};
  }
  __syncthreads();
  u16* hh = (u16*)(p.ws + WS_HH);
  const int ks2 = 4 * Fi + (w >> 1), gp = 2 * (w & 1) + (g >> 1);
#pragma unroll
  for (int i = 0; i < 8; ++i) {
    const int c = i ^ w;
    const f32x2 st = *(const f32x2*)(stats + (16 * c + (lane & 15)) * 2);
    float h[4];
#pragma unroll
    for (int r = 0; r < 4; ++r) {
      const float gv = bfr((ag[i][r] - st[0] * ug[r]) * st[1] + vg[r]);
      const float uvv = bfr((au[i][r] - st[0] * uu[r]) * st[1] + vu[r]);
      const float sg = bfr(__builtin_amdgcn_rcpf(1.0f + __builtin_amdgcn_exp2f(-1.4426950408889634f * gv)));
      h[r] = sg * uvv;
    }
    const int lt16 = Lt * 8 + c;
    *(u32x2*)(hh + ((size_t)(lt16 * 56 + ks2) * 64 + 16 * gp + (lane & 15)) * 8 + 4 * (g & 1)) = u32x2{pkbf(h[0], h[1]), pkbf(h[2], h[3])};
  }
}

constexpr int D_LDS = 4 * 3 * 4 * 1024;

__global__ __launch_bounds__(256) void kD(Params p) {
  extern __shared__ __attribute__((aligned(16))) char smem[];
  const int tid = threadIdx.x, lane = tid & 63;
  const int w = __builtin_amdgcn_readfirstlane(tid >> 6);
  const int xcd = blockIdx.x & 7, idx = blockIdx.x >> 3;
  const int P = 4 * xcd + idx / 7, Q = idx % 7;
  const u16* asrc = (const u16*)(p.ws + WS_WDF) + ((size_t)(4 * Q * 56 + 14 * w) * 64 + lane) * 8;
  const u16* bsrc = (const u16*)(p.ws + WS_HH) + ((size_t)(4 * P * 56 + 14 * w) * 64 + lane) * 8;
  const int lrow = P * 64 + 16 * w + (lane & 15);
  const int dcol = Q * 64 + 4 * (lane >> 4);
  u32x2 x1v[4]; f32x4 gbv[4];
#pragma unroll
  for (int id = 0; id < 4; ++id) {
    x1v[id] = *(const u32x2*)((const u16*)(p.ws + WS_X1R) + (size_t)lrow * D + dcol + 16 * id);
    gbv[id] = *(const f32x4*)((const float*)(p.ws + WS_MOD) + 5 * D + dcol + 16 * id);
  }
  f32x4 acc[4][4];
#pragma unroll
  for (int i = 0; i < 4; ++i)
#pragma unroll
    for (int k = 0; k < 4; ++k) acc[i][k] = f32x4{0, 0, 0, 0};
  constexpr int PD = 4;
  u32x4 a[PD][4], bb[PD][4];
#pragma unroll
  for (int s = 0; s < PD - 1; ++s)
#pragma unroll
    for (int i = 0; i < 4; ++i) {
      a[s][i] = *(const u32x4*)(asrc + (size_t)(i * 56 + s) * 512);
      bb[s][i] = *(const u32x4*)(bsrc + (size_t)(i * 56 + s) * 512);
    }
#pragma unroll
  for (int ks = 0; ks < 14; ++ks) {
    if (ks + PD - 1 < 14) {
#pragma unroll
      for (int i = 0; i < 4; ++i) {
        a[(ks + PD - 1) % PD][i] = *(const u32x4*)(asrc + (size_t)(i * 56 + ks + PD - 1) * 512);
        bb[(ks + PD - 1) % PD][i] = *(const u32x4*)(bsrc + (size_t)(i * 56 + ks + PD - 1) * 512);
      }
    }
#pragma unroll
    for (int id = 0; id < 4; ++id)
#pragma unroll
      for (int il = 0; il < 4; ++il)
        acc[id][il] = __builtin_amdgcn_mfma_f32_16x16x32_bf16(__builtin_bit_cast(bf16x8, a[ks % PD][id]), __builtin_bit_cast(bf16x8, bb[ks % PD][il]), acc[id][il], 0, 0, 0);
  }
#pragma unroll
  for (int il = 0; il < 4; ++il) {
    if (il != w) {
      const int slot = il > w ? il - 1 : il;
#pragma unroll
      for (int id = 0; id < 4; ++id) *(f32x4*)(smem + (((il * 3 + (w > il ? w - 1 : w)) * 4 + id) * 1024) + lane * 16) = acc[id][il];
      (void)slot;
    }
  }
  __syncthreads();
  f32x4 o[4];
#pragma unroll
  for (int id = 0; id < 4; ++id) {
    o[id] = w == 0 ? acc[id][0] : (w == 1 ? acc[id][1] : (w == 2 ? acc[id][2] : acc[id][3]));
#pragma unroll
    for (int s = 0; s < 3; ++s) o[id] += *(const f32x4*)(smem + (((w * 3 + s) * 4 + id) * 1024) + lane * 16);
  }
#pragma unroll
  for (int id = 0; id < 4; ++id) {
    float r[4];
#pragma unroll
    for (int k = 0; k < 4; ++k) {
      const float x1 = (k & 1) ? bfhi(x1v[id][k >> 1]) : bflo(x1v[id][k >> 1]);
      r[k] = x1 + bfr(gbv[id][k] * bfr(o[id][k]));
    }
    *(u32x2*)(p.out + (size_t)lrow * D + dcol + 16 * id) = u32x2{pkbf(r[0], r[1]), pkbf(r[2], r[3])};
  }
}

extern "C" void kernel_launch(void* const* d_in, const int* in_sizes, int n_in, void* d_out, int out_size, void* d_ws,
                              size_t ws_size, hipStream_t stream) {
  Params p;
  p.u = (const u16*)d_in[0]; p.A_re = (const u16*)d_in[1]; p.A_im = (const u16*)d_in[2]; p.B_re = (const u16*)d_in[3];
  p.B_im = (const u16*)d_in[4]; p.C_re = (const u16*)d_in[5]; p.C_im = (const u16*)d_in[6]; p.log_dt = (const u16*)d_in[7];
  p.Dskip = (const u16*)d_in[8]; p.w_gate = (const u16*)d_in[10]; p.w_up = (const u16*)d_in[11]; p.w_down = (const u16*)d_in[12];
  p.c = (const u16*)d_in[13]; p.w_mod = (const u16*)d_in[14];
  p.ws = (char*)d_ws; p.out = (u16*)d_out;
  hipFuncSetAttribute((const void*)kB, hipFuncAttributeMaxDynamicSharedMemorySize, B_LDS);
  hipFuncSetAttribute((const void*)kC, hipFuncAttributeMaxDynamicSharedMemorySize, C_LDS);
  kA<<<A_NLN + A_NKG + A_NMOD + A_NWD, 256, A_LDS, stream>>>(p);
  kB<<<B_NCONV + B_NWP, 512, B_LDS, stream>>>(p);
  kC<<<224, 512, C_LDS, stream>>>(p);
  kD<<<224, 256, D_LDS, stream>>>(p);
}
```

```cpp
#include <hip/hip_runtime.h>
#include <hip/hip_bf16.h>
#include <stdint.h>

typedef unsigned short u16;
typedef unsigned int u32;
typedef __attribute__((ext_vector_type(8))) short s16x8;
typedef __attribute__((ext_vector_type(4))) short s16x4;
typedef __attribute__((ext_vector_type(8))) _Float16 f16x8;
typedef __attribute__((ext_vector_type(2))) _Float16 f16x2;
typedef __attribute__((ext_vector_type(2))) __bf16 bf16x2;
typedef __attribute__((ext_vector_type(8))) __bf16 bf16x8;
typedef __attribute__((ext_vector_type(2))) float f32x2;
typedef __attribute__((ext_vector_type(4))) float f32x4;
typedef __attribute__((ext_vector_type(16))) float f32x16;
typedef __attribute__((ext_vector_type(2))) unsigned u32x2;
typedef __attribute__((ext_vector_type(4))) unsigned u32x4;
typedef __attribute__((address_space(3))) char* lds_ptr;
typedef __attribute__((address_space(3))) const char* lds_cptr;

constexpr int L = 2048, D = 448, NS = 256, F = 1792;
constexpr int RLEN = 2128;

constexpr size_t WS_MOD = 0;
constexpr size_t WS_KSC = 16384;
constexpr size_t WS_UV = 32768;
constexpr size_t WS_FLG = 61440;
constexpr size_t WS_XT = 65536;
constexpr size_t WS_XNT = WS_XT + (size_t)D * L * 2;
constexpr size_t WS_KR = WS_XNT + (size_t)D * L * 2;
constexpr size_t WS_WDF = WS_KR + (size_t)D * 2 * RLEN * 2;
constexpr size_t WS_WF = WS_WDF + (size_t)F * D * 2;
constexpr size_t WS_X1T = WS_WF + (size_t)2 * F * D * 2;
constexpr size_t WS_X1R = WS_X1T + (size_t)D * L * 2;
constexpr size_t WS_HH = WS_X1R + (size_t)D * L * 2;
constexpr size_t WS_STG = WS_HH + (size_t)L * F * 2;

struct Params {
  const u16* u; const u16* A_re; const u16* A_im; const u16* B_re; const u16* B_im; const u16* C_re; const u16* C_im;
  const u16* log_dt; const u16* Dskip; const u16* w_gate; const u16* w_up; const u16* w_down; const u16* c; const u16* w_mod;
  char* ws; u16* out;
};

#define STAMP(kid, i) do {} while (0)
#define SDECL(n) do {} while (0)
#define SST(i) do {} while (0)
#define SEND(kid, n) do {} while (0)
__device__ __forceinline__ float bf2f(u16 v) { return __uint_as_float((u32)v << 16); }
__device__ __forceinline__ float bflo(u32 v) { return __uint_as_float(v << 16); }
__device__ __forceinline__ float bfhi(u32 v) { return __uint_as_float(v & 0xffff0000u); }
__device__ __forceinline__ u32 pkbf(float lo, float hi) {
  f32x2 v = {lo, hi}; bf16x2 b = __builtin_convertvector(v, bf16x2); return __builtin_bit_cast(u32, b);
}
__device__ __forceinline__ float bfr(float x) { return bflo(pkbf(x, x)); }
__device__ __forceinline__ u32 pkh(float lo, float hi) {
  f32x2 v = {lo, hi}; f16x2 b = __builtin_convertvector(v, f16x2); return __builtin_bit_cast(u32, b);
}
template <int CTRL> __device__ __forceinline__ float dppf(float x) {
  return __builtin_bit_cast(float, __builtin_amdgcn_mov_dpp(__builtin_bit_cast(int, x), CTRL, 0xf, 0xf, true));
}
__device__ __forceinline__ float xrow16_sum(float x) {
  auto s = __builtin_amdgcn_permlane16_swap(__float_as_uint(x), __float_as_uint(x), false, false);
  x = __uint_as_float(s[0]) + __uint_as_float(s[1]);
  auto t = __builtin_amdgcn_permlane32_swap(__float_as_uint(x), __float_as_uint(x), false, false);
  return __uint_as_float(t[0]) + __uint_as_float(t[1]);
}
__device__ __forceinline__ float xrow16_max(float x) {
  auto s = __builtin_amdgcn_permlane16_swap(__float_as_uint(x), __float_as_uint(x), false, false);
  x = fmaxf(__uint_as_float(s[0]), __uint_as_float(s[1]));
  auto t = __builtin_amdgcn_permlane32_swap(__float_as_uint(x), __float_as_uint(x), false, false);
  return fmaxf(__uint_as_float(t[0]), __uint_as_float(t[1]));
}
__device__ __forceinline__ float wave_sum(float x) {
  x += dppf<0xB1>(x); x += dppf<0x4E>(x); x += dppf<0x141>(x); x += dppf<0x128>(x);
  return xrow16_sum(x);
}
__device__ __forceinline__ float wave_max(float x) {
  x = fmaxf(x, dppf<0xB1>(x)); x = fmaxf(x, dppf<0x4E>(x)); x = fmaxf(x, dppf<0x141>(x)); x = fmaxf(x, dppf<0x128>(x));
  return xrow16_max(x);
}
__device__ __forceinline__ float dot2bf(u32 a, u32 b, float acc) {
  return __builtin_amdgcn_fdot2_f32_bf16(__builtin_bit_cast(bf16x2, a), __builtin_bit_cast(bf16x2, b), acc, false);
}
typedef short v4i16_t __attribute__((ext_vector_type(4)));
__device__ __forceinline__ u32x2 tr_read(lds_cptr p) {
  return __builtin_bit_cast(u32x2, __builtin_amdgcn_ds_read_tr16_b64_v4i16((__attribute__((address_space(3))) v4i16_t*)p));
}
__device__ __forceinline__ void glds16(const void* g, unsigned lds_base) {
  unsigned sv;
  asm volatile("s_mov_b32 %0, m0\n\ts_mov_b32 m0, %2\n\ts_nop 0\n\tglobal_load_lds_dwordx4 %1, off\n\ts_mov_b32 m0, %0"
               : "=&s"(sv) : "v"(g), "s"(lds_base) : "memory");
}
__device__ __forceinline__ float pow2_scale(float x, int target) {
  int e = (int)((__float_as_uint(x) >> 23) & 255u);
  if (e == 0 || e == 255) return 1.0f;
  int s = target - (e - 127);
  s = s < -100 ? -100 : (s > 100 ? 100 : s);
  return __uint_as_float((u32)(127 + s) << 23);
}
struct cpx { float r, i; };
__device__ __forceinline__ cpx cmul(cpx a, cpx b) { return {a.r * b.r - a.i * b.i, a.r * b.i + a.i * b.r}; }
__device__ __forceinline__ cpx cexpm(float zr, float zi, float m) {
  float mag = __builtin_amdgcn_exp2f(zr * (m * 1.4426950408889634f));
  float rev = __builtin_amdgcn_fractf(zi * (m * 0.15915494309189535f));
  return {mag * __builtin_amdgcn_cosf(rev), mag * __builtin_amdgcn_sinf(rev)};
}

constexpr int A_NKG = 448, A_NLN = 128, A_NMOD = 42, A_NWD = 98;
constexpr int A_LDS = 4 * 32 * 272;

__device__ __forceinline__ void kgen_role(const Params& p, int d, char* smem) {
  const int tid = threadIdx.x, lane = tid & 63, w = tid >> 6;
  const int idx = d * NS + tid;
  const float are = bf2f(p.A_re[idx]), aim = bf2f(p.A_im[idx]);
  const float bre = bf2f(p.B_re[idx]), bim = bf2f(p.B_im[idx]);
  const float cre = bf2f(p.C_re[idx]), cim = bf2f(p.C_im[idx]);
  const float dt = bfr(expf(bf2f(p.log_dt[d])));
  const float zr = dt * are, zi = dt * aim;
  const float br = dt * bre, bi = dt * bim;
  cpx W = {cre * br - cim * bi, cre * bi + cim * br};
  const float wmax = wave_max(fmaxf(fabsf(W.r), fabsf(W.i)));
  const float wsum = wave_sum(fabsf(W.r) + fabsf(W.i));
  STAMP(0, 1);
  const float sw = pow2_scale(wmax, 13);
  W.r *= sw; W.i *= sw;

  char* tab = smem + w * (32 * 272);
  const cpx E1 = cexpm(zr, zi, 1.f), E8 = cexpm(zr, zi, 8.f), E256 = cexpm(zr, zi, 256.f);
  {
    const cpx E16 = cmul(E8, E8), E32 = cmul(E16, E16);
    cpx q0 = {1.f, 0.f}, q1 = E8, q2 = E16, q3 = cmul(E16, E8);
#pragma unroll
    for (int g = 0; g < 8; ++g) {
      *(u32*)(tab + (4 * g + 0) * 272 + lane * 4) = pkh(q0.r, q0.i);
      *(u32*)(tab + (4 * g + 1) * 272 + lane * 4) = pkh(q1.r, q1.i);
      *(u32*)(tab + (4 * g + 2) * 272 + lane * 4) = pkh(q2.r, q2.i);
      *(u32*)(tab + (4 * g + 3) * 272 + lane * 4) = pkh(q3.r, q3.i);
      if (g < 7) { q0 = cmul(q0, E32); q1 = cmul(q1, E32); q2 = cmul(q2, E32); q3 = cmul(q3, E32); }
    }
  }
  f16x8 bfr_[8];
  const int r32 = lane & 31, hh = lane >> 5;
#pragma unroll
  for (int s = 0; s < 8; ++s) bfr_[s] = *(const f16x8*)(tab + r32 * 272 + 32 * s + 16 * hh);
  {
    const cpx E2 = cmul(E1, E1), E4 = cmul(E2, E2);
    cpx t = W;
#define PUT(rr, vv) *(u32*)(tab + (rr) * 272 + lane * 4) = pkh((vv).r, -(vv).i)
    asm volatile("s_waitcnt lgkmcnt(0)" ::: "memory");
#pragma unroll
    for (int q = 0; q < 8; ++q) { PUT(q, t); if (q < 7) t = cmul(t, E1); }
    const cpx W1 = cmul(W, E256);
    t = W1;
#pragma unroll
    for (int q = 0; q < 4; ++q) { PUT(8 + q, t); if (q < 3) t = cmul(t, E2); }
    const cpx W2 = cmul(W1, E256);
    const cpx W3 = cmul(W2, E256);
    PUT(12, W2); t = cmul(W2, E4); PUT(13, t);
    PUT(14, W3); t = cmul(W3, E4); PUT(15, t);
    t = cmul(W3, E256);
#pragma unroll
    for (int q = 0; q < 5; ++q) { PUT(16 + q, t); if (q < 4) t = cmul(t, E256); }
#undef PUT
  }
  STAMP(0, 2);
  f32x16 acc = {};
#pragma unroll
  for (int s = 0; s < 8; ++s) {
    f16x8 a = *(const f16x8*)(tab + r32 * 272 + 32 * s + 16 * hh);
    acc = __builtin_amdgcn_mfma_f32_32x32x16_f16(a, bfr_[s], acc, 0, 0, 0);
  }
  __syncthreads();
  STAMP(0, 3);
  float* red = (float*)smem;
  const float isw = 1.0f / sw;
#pragma unroll
  for (int r = 0; r < 12; ++r) red[(w * 12 + r) * 64 + lane] = acc[r] * isw;
  float* wsums = (float*)(smem + 4 * 12 * 64 * 4);
  float* ktab = (float*)(smem + 4 * 12 * 64 * 4 + 64);
  if (lane == 0) wsums[w] = wsum;
  __syncthreads();
  const float S = wsums[0] + wsums[1] + wsums[2] + wsums[3];
  const float kt = pow2_scale(S, 14);
  for (int e = tid; e < 12 * 64; e += 256) {
    const int r = e >> 6, l = e & 63;
    const float v = red[e] + red[768 + e] + red[1536 + e] + red[2304 + e];
    const int row = (r & 3) + 8 * (r >> 2) + 4 * (l >> 5);
    ktab[row * 32 + (l & 31)] = v * kt;
  }
  if (tid == 0) ((float*)(p.ws + WS_KSC))[d] = 1.0f / kt;
  __syncthreads();
  STAMP(0, 4);
  u16* R0 = (u16*)(p.ws + WS_KR) + (size_t)d * 2 * RLEN;
  u16* R1 = R0 + RLEN;
  for (int t = tid; t < RLEN / 8; t += 256) {
    float v[9];
#pragma unroll
    for (int j = 0; j < 9; ++j) {
      const int i = 8 * t + j;
      const int l = 2048 - i;
      float val = 0.f;
      if (i >= 1 && l >= 0) {
        const int lb = (int)bfr((float)l);
        int row, col;
        if (lb < 256) { row = lb & 7; col = lb >> 3; }
        else if (lb < 512) { row = 8 + ((lb >> 1) & 3); col = (lb - 256) >> 3; }
        else if (lb < 1024) { row = 12 + 2 * ((lb - 512) >> 8) + ((lb >> 2) & 1); col = (lb & 255) >> 3; }
        else { row = 16 + ((lb - 1024) >> 8); col = (lb & 255) >> 3; }
        val = ktab[row * 32 + col];
      }
      v[j] = val;
    }
    u32x4 o0 = {pkh(v[0], v[1]), pkh(v[2], v[3]), pkh(v[4], v[5]), pkh(v[6], v[7])};
    u32x4 o1 = {pkh(v[1], v[2]), pkh(v[3], v[4]), pkh(v[5], v[6]), pkh(v[7], v[8])};
    *(u32x4*)(R0 + 8 * t) = o0;
    *(u32x4*)(R1 + 8 * t) = o1;
  }
}

__device__ __forceinline__ void ln1_role(const Params& p, int j, char* smem) {
  const int tid = threadIdx.x, lane = tid & 63, w = tid >> 6;
  const int rg = (j & 7) * 16 + (j >> 3);
  const int l0 = rg * 16;
  constexpr int RS = 960;
  char* xs = smem;
  char* xn = smem + 16 * RS;
  const bool act = lane < 56;
  u32x4 v[4];
#pragma unroll
  for (int r = 0; r < 4; ++r) {
    v[r] = u32x4{0, 0, 0, 0};
    if (act) v[r] = *(const u32x4*)(p.u + (size_t)(l0 + 4 * w + r) * D + lane * 8);
  }
#pragma unroll
  for (int r = 0; r < 4; ++r) {
    float x[8];
#pragma unroll
    for (int k = 0; k < 4; ++k) { x[2 * k] = bflo(v[r][k]); x[2 * k + 1] = bfhi(v[r][k]); }
    float s = 0.f;
#pragma unroll
    for (int k = 0; k < 8; ++k) s += x[k];
    s = wave_sum(s);
    const float mean = s * (1.0f / D);
    float q = 0.f;
#pragma unroll
    for (int k = 0; k < 8; ++k) { const float t = x[k] - mean; q += t * t; }
    if (!act) q = 0.f;
    q = wave_sum(q);
    const float m = bfr(mean);
    const float var = bfr(q * (1.0f / D));
    const float sd = bfr(sqrtf(bfr(var + 1e-5f)));
    const float rs = 1.0f / sd;
    u32x4 o;
#pragma unroll
    for (int k = 0; k < 4; ++k) {
      float a = bfr(x[2 * k] - m), b = bfr(x[2 * k + 1] - m);
      float qa = a * rs, qb = b * rs;
      qa = fmaf(fmaf(-qa, sd, a), rs, qa); qb = fmaf(fmaf(-qb, sd, b), rs, qb);
      o[k] = pkbf(qa, qb);
    }
    if (act) {
      *(u32x4*)(xs + (4 * w + r) * RS + lane * 16) = v[r];
      *(u32x4*)(xn + (4 * w + r) * RS + lane * 16) = o;
    }
  }
  __syncthreads();
  STAMP(0, 1);
  const int g = lane >> 4, q4 = (lane & 15) >> 2, pp = lane & 3;
  for (int task = w; task < 14; task += 4) {
    const int arr = task >= 7, ps = arr ? task - 7 : task;
    lds_cptr base = (lds_cptr)(arr ? xn : xs) + q4 * RS + (ps * 64 + g * 16 + pp * 4) * 2;
    u32x2 t0 = tr_read(base), t1 = tr_read(base + 4 * RS), t2 = tr_read(base + 8 * RS), t3 = tr_read(base + 12 * RS);
    const int d = ps * 64 + lane;
    u16* dst = (u16*)(p.ws + (arr ? WS_XNT : WS_XT)) + (size_t)d * L + l0;
    *(u32x4*)dst = u32x4{t0[0], t0[1], t1[0], t1[1]};
    *(u32x4*)(dst + 8) = u32x4{t2[0], t2[1], t3[0], t3[1]};
  }
}

__device__ __forceinline__ void mod_role(const Params& p, int j, char* smem) {
  const int tid = threadIdx.x;
  const int cg = tid & 7, rg = tid >> 3;
  const int c0 = j * 64 + cg * 8;
  u32x4 v[14]; float s[14];
#pragma unroll
  for (int i = 0; i < 14; ++i) v[i] = *(const u32x4*)(p.w_mod + (size_t)(rg + 32 * i) * (6 * D) + c0);
#pragma unroll
  for (int i = 0; i < 14; ++i) {
    const float cv = bf2f(p.c[rg + 32 * i]);
    s[i] = bfr(cv / (1.0f + expf(-cv)));
  }
  float acc[8] = {};
#pragma unroll
  for (int i = 0; i < 14; ++i)
#pragma unroll
    for (int k = 0; k < 4; ++k) { acc[2 * k] += s[i] * bflo(v[i][k]); acc[2 * k + 1] += s[i] * bfhi(v[i][k]); }
  float* red = (float*)smem;
#pragma unroll
  for (int k = 0; k < 8; ++k) red[rg * 64 + cg * 8 + k] = acc[k];
  __syncthreads();
  if (tid < 64) {
    float t = 0.f;
#pragma unroll
    for (int r = 0; r < 32; ++r) t += red[r * 64 + tid];
    ((float*)(p.ws + WS_MOD))[j * 64 + tid] = bfr(t);
  }
}

__device__ __forceinline__ void wd_role(const Params& p, int j, char* smem) {
  const int tid = threadIdx.x, lane = tid & 63, w = tid >> 6;
  const int fb = j / 7, db = j % 7;
  constexpr int RS = 144;
#pragma unroll
  for (int i = 0; i < 4; ++i) {
    const int r = (tid >> 3) + 32 * i;
    u32x4 v = *(const u32x4*)(p.w_down + (size_t)(fb * 128 + r) * D + db * 64 + (tid & 7) * 8);
    *(u32x4*)(smem + r * RS + (tid & 7) * 16) = v;
  }
  __syncthreads();
  const int g = lane >> 4, q4 = (lane & 15) >> 2, pp = lane & 3;
  u16* wdf = (u16*)(p.ws + WS_WDF);
#pragma unroll
  for (int i = 0; i < 4; ++i) {
    const int ks = w, dt = i;
    lds_cptr base = (lds_cptr)smem + (32 * ks + 8 * g + q4) * RS + (16 * dt + 4 * pp) * 2;
    u32x2 t0 = tr_read(base), t1 = tr_read(base + 4 * RS);
    const int kk = fb * 4 + ks;
    *(u32x4*)(wdf + ((size_t)((db * 8 + kk / 7) * 28 + dt * 7 + kk % 7) * 64 + lane) * 8) = u32x4{t0[0], t0[1], t1[0], t1[1]};
  }
}

__global__ __launch_bounds__(256) void kA(Params p) {
  extern __shared__ __attribute__((aligned(16))) char smem[];
  int b = blockIdx.x;
  STAMP(0, 0);
  if (b < A_NLN) { ln1_role(p, b, smem); STAMP(0, 7); return; }
  b -= A_NLN;
  if (b < A_NKG) { kgen_role(p, b, smem); STAMP(0, 7); return; }
  b -= A_NKG;
  if (b < A_NMOD) { mod_role(p, b, smem); STAMP(0, 7); return; }
  b -= A_NMOD;
  if (b == 0) { ((u32x2*)(p.ws + WS_FLG))[threadIdx.x] = u32x2{0u, 0u}; }
  if (b == 1) { ((u32x4*)(p.ws + WS_STG))[threadIdx.x] = u32x4{0u, 0u, 0u, 0u}; ((u32x4*)(p.ws + WS_STG))[256 + threadIdx.x] = u32x4{0u, 0u, 0u, 0u}; }
  wd_role(p, b, smem);
  STAMP(0, 7);
}

constexpr int B_NCONV = 224, B_NWP = 56;
constexpr int B_IMG = 33280, B_HOFF = 2 * B_IMG, B_HCH = 96 * 80, B_LDS = B_HOFF + 2 * B_HCH;

__device__ __forceinline__ void conv_role(const Params& p, int b, char* smem) {
  const int tid = threadIdx.x, lane = tid & 63;
  const int w = __builtin_amdgcn_readfirstlane(tid >> 6);
  const int ch = w >> 2, pq = w & 3;
  const int d = 2 * b + ch;
  const unsigned lds0 = (unsigned)(uintptr_t)smem;
  {
    const u16* R0 = (const u16*)(p.ws + WS_KR) + (size_t)d * 2 * RLEN;
    const int s = lane & 7;
    const u16* src = R0 + (s & 1) * RLEN + (s & ~1) + (lane >> 3) * 8;
#pragma unroll
    for (int i = 0; i < 8; ++i) {
      const int pc = pq + 4 * i;
      glds16(src + pc * 64, lds0 + ch * B_IMG + pc * 1024);
    }
    if (pq == 0 && lane < 32) glds16(src + 32 * 64, lds0 + ch * B_IMG + 32 * 1024);
  }
  const float* mod = (const float*)(p.ws + WS_MOD);
  const float sa = mod[d], ca1 = bfr(1.0f + mod[D + d]), ga = mod[2 * D + d];
  const float dsk = bf2f(p.Dskip[d]);
  const float ksc = ((const float*)(p.ws + WS_KSC))[d];
  const float hs = pow2_scale(22.0f * fabsf(ca1) + fabsf(sa), 14);
  const u16* xnT = (const u16*)(p.ws + WS_XNT) + (size_t)d * L;
  const u16* xT = (const u16*)(p.ws + WS_XT) + (size_t)d * L;
  {
    const int t = tid & 255;
    char* hb = smem + B_HOFF + ch * B_HCH;
    u32x4 v = *(const u32x4*)(xnT + t * 8);
    u32x4 o;
#pragma unroll
    for (int k = 0; k < 4; ++k) {
      const float h0 = bfr(bfr(bflo(v[k]) * ca1) + sa), h1 = bfr(bfr(bfhi(v[k]) * ca1) + sa);
      o[k] = pkh(h0 * hs, h1 * hs);
    }
    *(u32x4*)(hb + (32 + (t >> 2)) * 80 + (t & 3) * 16) = o;
    if (t < 160) *(u32x4*)(hb + t * 16) = u32x4{0, 0, 0, 0};
  }
  const int a = lane & 31, hh = lane >> 5;
  const int lout = 32 * (32 * (pq >> 1) + a) + 16 * (pq & 1) + 8 * hh;
  const u32x4 exn = *(const u32x4*)(xnT + lout);
  const u32x4 ex = *(const u32x4*)(xT + lout);

  STAMP(1, 1);
  asm volatile("s_waitcnt vmcnt(0)" ::: "memory");
  __syncthreads();
  STAMP(1, 2);

  const int cb = (a + 7) >> 3, s = (8 - (a & 7)) & 7;
  lds_cptr abase = (lds_cptr)smem + ch * B_IMG + 128 * (256 - 4 * (8 * pq) + hh - cb) + 16 * s;
  lds_cptr bbase = (lds_cptr)smem + B_HOFF + ch * B_HCH + (a + 32 - 8 * pq) * 80 + 16 * hh;
  f32x16 acc0 = {}, acc1 = {}, acc0s = {}, acc1s = {};
  const bool up = (lane & 16) != 0;
  u32x4 fprevL = *(const __attribute__((address_space(3))) u32x4*)(abase + 512);
  u32x4 fprevH = *(const __attribute__((address_space(3))) u32x4*)(abase + 512 - 16384);
#pragma unroll
  for (int i = 0; i < 8; ++i) {
    const u32x4 fL = *(const __attribute__((address_space(3))) u32x4*)(abase - 512 * i);
    const u32x4 fH = *(const __attribute__((address_space(3))) u32x4*)(abase - 512 * i - 16384);
    const u32x4 b00 = *(const __attribute__((address_space(3))) u32x4*)(bbase - 80 * i);
    const u32x4 b01 = *(const __attribute__((address_space(3))) u32x4*)(bbase - 80 * i + 32);
    const u32x4 b10 = *(const __attribute__((address_space(3))) u32x4*)(bbase - 80 * i + 2560);
    const u32x4 b11 = *(const __attribute__((address_space(3))) u32x4*)(bbase - 80 * i + 2560 + 32);
    u32x4 sL, sH;
#pragma unroll
    for (int k = 0; k < 4; ++k) { sL[k] = up ? fprevL[k] : fL[k]; sH[k] = up ? fprevH[k] : fH[k]; }
#define H8(x) __builtin_bit_cast(f16x8, x)
    acc0 = __builtin_amdgcn_mfma_f32_32x32x16_f16(H8(fL), H8(b00), acc0, 0, 0, 0);
    acc1 = __builtin_amdgcn_mfma_f32_32x32x16_f16(H8(fL), H8(b10), acc1, 0, 0, 0);
    acc1 = __builtin_amdgcn_mfma_f32_32x32x16_f16(H8(fH), H8(b00), acc1, 0, 0, 0);
    acc0s = __builtin_amdgcn_mfma_f32_32x32x16_f16(H8(sL), H8(b01), acc0s, 0, 0, 0);
    acc1s = __builtin_amdgcn_mfma_f32_32x32x16_f16(H8(sL), H8(b11), acc1s, 0, 0, 0);
    acc1s = __builtin_amdgcn_mfma_f32_32x32x16_f16(H8(sH), H8(b01), acc1s, 0, 0, 0);
#undef H8
    fprevL = fL; fprevH = fH;
  }
#pragma unroll
  for (int r = 0; r < 16; ++r) { acc0[r] += acc0s[r ^ 8]; acc1[r] += acc1s[r ^ 8]; }
  STAMP(1, 3);
  __syncthreads();
  STAMP(1, 4);
  {
    char* red = smem + ch * (4 * 4 * 2 * 1024);
#pragma unroll
    for (int q = 0; q < 4; ++q) {
      if (q != pq) {
        const f32x16& t = (q >> 1) ? acc1 : acc0;
        const int r0 = 8 * (q & 1);
        *(f32x4*)(red + ((pq * 4 + q) * 2 + 0) * 1024 + lane * 16) = f32x4{t[r0], t[r0 + 1], t[r0 + 2], t[r0 + 3]};
        *(f32x4*)(red + ((pq * 4 + q) * 2 + 1) * 1024 + lane * 16) = f32x4{t[r0 + 4], t[r0 + 5], t[r0 + 6], t[r0 + 7]};
      }
    }
    __syncthreads();
    STAMP(1, 5);
    f32x4 y0, y1;
    {
      if (pq == 0) { y0 = f32x4{acc0[0], acc0[1], acc0[2], acc0[3]}; y1 = f32x4{acc0[4], acc0[5], acc0[6], acc0[7]}; }
      else if (pq == 1) { y0 = f32x4{acc0[8], acc0[9], acc0[10], acc0[11]}; y1 = f32x4{acc0[12], acc0[13], acc0[14], acc0[15]}; }
      else if (pq == 2) { y0 = f32x4{acc1[0], acc1[1], acc1[2], acc1[3]}; y1 = f32x4{acc1[4], acc1[5], acc1[6], acc1[7]}; }
      else { y0 = f32x4{acc1[8], acc1[9], acc1[10], acc1[11]}; y1 = f32x4{acc1[12], acc1[13], acc1[14], acc1[15]}; }
    }
#pragma unroll
    for (int sw = 0; sw < 4; ++sw) {
      if (sw != pq) {
        y0 += *(const f32x4*)(red + ((sw * 4 + pq) * 2 + 0) * 1024 + lane * 16);
        y1 += *(const f32x4*)(red + ((sw * 4 + pq) * 2 + 1) * 1024 + lane * 16);
      }
    }
    float y[8];
#pragma unroll
    for (int k = 0; k < 4; ++k) {
      auto r = __builtin_amdgcn_permlane32_swap(__float_as_uint(y0[k]), __float_as_uint(y1[k]), false, false);
      y[k] = __uint_as_float(r[0]); y[4 + k] = __uint_as_float(r[1]);
    }
    const float ysc = ksc / hs;
    u32x4 o;
#pragma unroll
    for (int k = 0; k < 4; ++k) {
      float r2[2];
#pragma unroll
      for (int e = 0; e < 2; ++e) {
        const float xn = e ? bfhi(exn[k]) : bflo(exn[k]);
        const float xr = e ? bfhi(ex[k]) : bflo(ex[k]);
        const float h = bfr(bfr(xn * ca1) + sa);
        const float yy = bfr(y[2 * k + e] * ysc);
        const float t = bfr(yy + bfr(h * dsk));
        r2[e] = xr + bfr(ga * t);
      }
      o[k] = pkbf(r2[0], r2[1]);
    }
    *(u32x4*)((u16*)(p.ws + WS_X1T) + ((size_t)(lout >> 7) * D + d) * 128 + (lout & 127)) = o;
  }
}

__device__ __forceinline__ void wp_role(const Params& p, int j, char* smem) {
  const int tid = threadIdx.x, lane = tid & 63, w = tid >> 6;
  const int mat = j & 1, fb = j >> 1;
  const u16* W = mat ? p.w_up : p.w_gate;
  const float* mod = (const float*)(p.ws + WS_MOD);
  constexpr int RS = 144;
  const int cg = tid & 7, rg = tid >> 3;
  u32x4 v[7];
#pragma unroll
  for (int i = 0; i < 7; ++i) v[i] = *(const u32x4*)(W + (size_t)(rg + 64 * i) * F + fb * 64 + cg * 8);
  float us[8] = {}, vs[8] = {};
#pragma unroll
  for (int i = 0; i < 7; ++i) {
    const int r = rg + 64 * i;
    const float c1 = bfr(1.0f + mod[4 * D + r]), sb = mod[3 * D + r];
    u32x4 o;
#pragma unroll
    for (int k = 0; k < 4; ++k) {
      const float a = bflo(v[i][k]), b = bfhi(v[i][k]);
      o[k] = pkbf(a * c1, b * c1);
      us[2 * k] += bflo(o[k]); us[2 * k + 1] += bfhi(o[k]);
      vs[2 * k] += sb * a; vs[2 * k + 1] += sb * b;
    }
    *(u32x4*)(smem + r * RS + cg * 16) = o;
  }
  float* red = (float*)(smem + 448 * RS);
#pragma unroll
  for (int k = 0; k < 8; ++k) {
    us[k] += dppf<0x128>(us[k]); vs[k] += dppf<0x128>(vs[k]);
    us[k] = xrow16_sum(us[k]); vs[k] = xrow16_sum(vs[k]);
  }
  if (lane < 8) {
#pragma unroll
    for (int k = 0; k < 8; ++k) { red[(w * 2 + 0) * 64 + lane * 8 + k] = us[k]; red[(w * 2 + 1) * 64 + lane * 8 + k] = vs[k]; }
  }
  __syncthreads();
  if (tid < 128) {
    const int which = tid >> 6, cidx = tid & 63;
    float t = 0.f;
#pragma unroll
    for (int q = 0; q < 8; ++q) t += red[(q * 2 + which) * 64 + cidx];
    ((float*)(p.ws + WS_UV))[(mat * 2 + which) * F + fb * 64 + cidx] = t;
  }
  const int g = lane >> 4, q4 = (lane & 15) >> 2, pp = lane & 3;
  u16* wf = (u16*)(p.ws + WS_WF);
  for (int t = w; t < 56; t += 8) {
    const int ft = t / 14, ks = t % 14;
    lds_cptr base = (lds_cptr)smem + (32 * ks + 8 * g + q4) * RS + (16 * ft + 4 * pp) * 2;
    u32x2 t0 = tr_read(base), t1 = tr_read(base + 4 * RS);
    *(u32x4*)(wf + ((size_t)(((fb * 4 + ft) * 14 + ks) * 2 + mat) * 64 + lane) * 8) = u32x4{t0[0], t0[1], t1[0], t1[1]};
  }
}

__global__ __launch_bounds__(512) void kB(Params p) {
  extern __shared__ __attribute__((aligned(16))) char smem[];
  const int b = blockIdx.x;
  STAMP(1, 0);
  if (b < B_NCONV) conv_role(p, b, smem);
  else wp_role(p, b - B_NCONV, smem);
  STAMP(1, 7);
}

constexpr int C_XT = 448 * 256, C_UV = C_XT, C_ST = C_UV + 2048, CD_LDS = 131072;

__device__ __forceinline__ void gload16(u32x4& dst, const void* sbase, u32 voff, int imm) {
  asm volatile("global_load_dwordx4 %0, %1, %2 offset:%3" : "=v"(dst) : "v"(voff), "s"(sbase), "i"(imm) : "memory");
}
__device__ __forceinline__ void gload16_sc1(u32x4& dst, const void* sbase, u32 voff, int imm) {
  asm volatile("global_load_dwordx4 %0, %1, %2 offset:%3 sc1" : "=v"(dst) : "v"(voff), "s"(sbase), "i"(imm) : "memory");
}
__device__ __forceinline__ void glds16s(const void* sbase, u32 voff, unsigned lds_base) {
  asm volatile("s_mov_b32 m0, %2\n\ts_nop 0\n\tglobal_load_lds_dwordx4 %0, %1" :: "v"(voff), "s"(sbase), "s"(lds_base) : "memory");
}
#define MFMA16(a, b, c) __builtin_amdgcn_mfma_f32_16x16x32_bf16(__builtin_bit_cast(bf16x8, a), __builtin_bit_cast(bf16x8, b), c, 0, 0, 0)
__device__ __forceinline__ void mfma_acc(f32x4& c, const u32x4& a, const u32x4& b) {
  asm volatile("v_mfma_f32_16x16x32_bf16 %0, %1, %2, %0" : "+v"(c) : "v"(a), "v"(b));
}
__device__ __forceinline__ void mfma_first(f32x4& c, const u32x4& a, const u32x4& b) {
  asm volatile("v_mfma_f32_16x16x32_bf16 %0, %1, %2, 0" : "=&v"(c) : "v"(a), "v"(b));
}
__device__ __forceinline__ void mfma_fence() { asm volatile("s_nop 7\n\ts_nop 7" ::: "memory"); }
#define VMWAIT(n) case n: asm volatile("s_waitcnt vmcnt(" #n ")" ::: "memory"); break
__device__ __forceinline__ void vmwait(int n) {
  switch (n) {
    VMWAIT(0); VMWAIT(1); VMWAIT(2); VMWAIT(3); VMWAIT(4); VMWAIT(5); VMWAIT(6); VMWAIT(7); VMWAIT(8); VMWAIT(9);
    VMWAIT(10); VMWAIT(11); VMWAIT(12); VMWAIT(13); VMWAIT(14); VMWAIT(15); VMWAIT(16); VMWAIT(17); VMWAIT(18); VMWAIT(19);
    VMWAIT(20); VMWAIT(21); VMWAIT(22); VMWAIT(23); VMWAIT(24); VMWAIT(25); VMWAIT(26); VMWAIT(27); VMWAIT(28); VMWAIT(29);
    VMWAIT(30); VMWAIT(31); VMWAIT(32); VMWAIT(33); VMWAIT(34); VMWAIT(35); VMWAIT(36); VMWAIT(37); VMWAIT(38); VMWAIT(39);
    default: asm volatile("s_waitcnt vmcnt(0)" ::: "memory"); break;
  }
}
#define PIN4(x) asm volatile("" : "+v"(x))

__device__ __forceinline__ void stats_role(const Params& p, int j, char* smem) {
  const int tid = threadIdx.x, lane = tid & 63, w = tid >> 6;
  const int lc = tid & 7, dr = tid >> 3;
  const char* src = p.ws + WS_X1T + (size_t)(j >> 1) * D * 256 + (j & 1) * 128 + lc * 16;
  u32x4 v[7];
#pragma unroll
  for (int i = 0; i < 7; ++i) v[i] = *(const u32x4*)(src + (dr + 64 * i) * 256);
  float s[8] = {}, q[8] = {};
#pragma unroll
  for (int i = 0; i < 7; ++i)
#pragma unroll
    for (int k = 0; k < 4; ++k) {
      const float a = bflo(v[i][k]), b = bfhi(v[i][k]);
      s[2 * k] += a; q[2 * k] = fmaf(a, a, q[2 * k]); s[2 * k + 1] += b; q[2 * k + 1] = fmaf(b, b, q[2 * k + 1]);
    }
#pragma unroll
  for (int k = 0; k < 8; ++k) {
    s[k] += dppf<0x128>(s[k]); q[k] += dppf<0x128>(q[k]);
    s[k] = xrow16_sum(s[k]); q[k] = xrow16_sum(q[k]);
  }
  float* red = (float*)smem;
  if (lane < 8) {
    *(f32x4*)(red + (w * 2 + 0) * 64 + lane * 8) = f32x4{s[0], s[1], s[2], s[3]};
    *(f32x4*)(red + (w * 2 + 0) * 64 + lane * 8 + 4) = f32x4{s[4], s[5], s[6], s[7]};
    *(f32x4*)(red + (w * 2 + 1) * 64 + lane * 8) = f32x4{q[0], q[1], q[2], q[3]};
    *(f32x4*)(red + (w * 2 + 1) * 64 + lane * 8 + 4) = f32x4{q[4], q[5], q[6], q[7]};
  }
  __syncthreads();
  if (tid < 64) {
    float ss = 0.f, qq = 0.f;
#pragma unroll
    for (int u = 0; u < 8; ++u) { ss += red[(u * 2) * 64 + tid]; qq += red[(u * 2 + 1) * 64 + tid]; }
    const float mean = ss * (1.0f / D);
    const float var = fmaxf(qq * (1.0f / D) - mean * mean, 0.f);
    const float sd = sqrtf(bfr(bfr(var) + 1e-5f));
    const u32 word = pkbf(mean, sd);
    typedef __attribute__((address_space(1))) unsigned gu32;
    __hip_atomic_store((gu32*)(p.ws + WS_STG) + 64 * j + tid, word, __ATOMIC_RELAXED, __HIP_MEMORY_SCOPE_AGENT);
  }
}

constexpr int C_LA = 3;
constexpr int C_WD0 = 8;
struct COp { int kind, arg; };
constexpr COp c_opA(int ks, int g) {
  if (g < 2 && ks + C_LA <= 13) return {1, 2 * (ks + C_LA) + g};
  if (ks == 0 && (g == 2 || g == 4 || g == 6)) return {2, 4 + (g - 2) / 2};
  if (ks == 1 && (g == 2 || g == 4)) return {2, 7 + (g - 2) / 2};
  if (ks >= 2 && ks <= 6 && g == 2) return {2, ks + 7};
  if (ks >= C_WD0 && (g == 4 || g == 6)) return {4, 2 * (ks - C_WD0) + (g - 4) / 2};
  return {0, 0};
}
constexpr COp c_opB(int ks, int g) { return ks == 7 ? COp{3, g} : COp{0, 0}; }
struct CSched { int wait_w[14], wait_x3, wait_x8, wait_st; };
constexpr CSched c_make_sched() {
  CSched r{};
  int seqW[28] = {}, seqX[14] = {}, seqS = 0, n = 1;
  for (int k = 0; k < 4; ++k) seqX[k] = n++;
  for (int k = 0; k < 2 * C_LA; ++k) seqW[k] = n++;
  for (int ks = 0; ks < 14; ++ks) {
    r.wait_w[ks] = n - 1 - seqW[2 * ks + 1];
    for (int g = 0; g < 8; ++g) {
      if (g == 4 && ks == 3) r.wait_x3 = n - 1 - seqX[8];
      if (g == 4 && ks == 8) r.wait_x8 = n - 1 - seqX[13];
      for (int u = 0; u < 2; ++u) {
        const COp o = u ? c_opB(ks, g) : c_opA(ks, g);
        if (o.kind == 1) seqW[o.arg] = n;
        if (o.kind == 2) seqX[o.arg] = n;
        if (o.kind == 3) seqS = n;
        if (o.kind) ++n;
      }
    }
  }
  r.wait_st = n - 1 - seqS;
  return r;
}
constexpr CSched C_SCHED = c_make_sched();
constexpr int C_WDM = 2 * (14 - C_WD0);
constexpr int C_WDE = 8;

__global__ __launch_bounds__(512) void kCD(Params p) {
  extern __shared__ __attribute__((aligned(16))) char smem[];
  if (blockIdx.x >= 224) { stats_role(p, blockIdx.x - 224, smem); return; }
  const int tid = threadIdx.x, lane = tid & 63;
  const int w = __builtin_amdgcn_readfirstlane(tid >> 6);
  const int xcd = blockIdx.x & 7, idx = blockIdx.x >> 3;
  const int Lt = 4 * (xcd >> 1) + idx / 7, Fi = 7 * (xcd & 1) + idx % 7;
  const int P = 2 * Lt + (xcd & 1), Q = idx % 7;
  const unsigned lds0 = (unsigned)(uintptr_t)smem;
  const int g = lane >> 4, q4 = (lane & 15) >> 2, pp = lane & 3;
  const u32 l16 = lane * 16;

  SDECL(10); SST(0);
  u32x4 wf[28];
  const int xrow = 4 * w + g;
  const u32 xoff = (u32)(xrow * 128 + ((lane & 15) ^ (((xrow & 3) << 2) | (((xrow >> 3) & 1) << 1))) * 8) * 2;
  const char* xsrc = p.ws + WS_X1T + (size_t)Lt * D * 256;
  const char* wsrc = p.ws + WS_WF + (size_t)(Fi * 8 + w) * 28 * 1024;
  const char* wdsrc = p.ws + WS_WDF + (size_t)(Q * 8 + w) * 28 * 1024;
  const char* stsrc = p.ws + WS_STG + Lt * 512;
  u32 stw[8];
  auto ld_x = [&](int ks) { glds16s(xsrc + ks * 8192, xoff, lds0 + ks * 8192 + w * 1024); };
  auto ld_w = [&](int n) { gload16(wf[n], wsrc + (n >> 2) * 4096, l16, (n & 3) * 1024); };
  auto ld_wd = [&](int n) { gload16(wf[n], wdsrc + (n >> 2) * 4096, l16, (n & 3) * 1024); };
  auto ld_st = [&](int i) {
    asm volatile("global_load_dword %0, %1, %2 sc1" : "=v"(stw[i]) : "v"((u32)(lane & 15) * 4), "s"(stsrc + ((i ^ w) << 6)) : "memory");
  };
  auto issue = [&](COp o) {
    if (o.kind == 1) ld_w(o.arg);
    if (o.kind == 2) ld_x(o.arg);
    if (o.kind == 3) ld_st(o.arg);
    if (o.kind == 4) ld_wd(o.arg);
  };
  if (lane < 16) glds16s(p.ws + WS_UV + ((size_t)(w >> 1) * F + 128 * Fi + 64 * (w & 1)) * 4, l16, lds0 + C_UV + w * 256);
#pragma unroll
  for (int ks = 0; ks < 4; ++ks) ld_x(ks);
#pragma unroll
  for (int n = 0; n < 2 * C_LA; ++n) ld_w(n);
  SST(1);
  f32x4 ag[8], au[8];
  unsigned ta[8];
  {
    const unsigned base = (unsigned)(256 * (8 * g + q4) + 32 * ((q4 << 1) | (g & 1)) + 8 * pp);
#pragma unroll
    for (int i = 0; i < 8; ++i) ta[i] = base ^ (32u * (unsigned)(i ^ w));
  }
  u32x4 xb[2][4];
  auto rd_tile = [&](int ks, int i8, u32x4& dst) {
    lds_cptr a = (lds_cptr)smem + ta[i8] + ks * 8192;
    const u32x2 t0 = tr_read(a), t1 = tr_read(a + 1024);
    dst = u32x4{t0[0], t0[1], t1[0], t1[1]};
  };
  vmwait(2 * C_LA);
  asm volatile("s_barrier" ::: "memory");
#pragma unroll
  for (int i = 0; i < 4; ++i) rd_tile(0, i, xb[0][i]);
#pragma unroll
  for (int ks = 0; ks < 14; ++ks) {
    vmwait(C_SCHED.wait_w[ks]);
    PIN4(wf[2 * ks]); PIN4(wf[2 * ks + 1]);
#pragma unroll
    for (int gi = 0; gi < 8; ++gi) {
      const int h = gi >> 2, i = gi & 3;
      if (gi == 4 && (ks == 3 || ks == 8)) {
        vmwait(ks == 3 ? C_SCHED.wait_x3 : C_SCHED.wait_x8);
        asm volatile("s_barrier" ::: "memory");
      }
      if (ks == 0) { mfma_first(ag[gi], wf[0], xb[h][i]); mfma_first(au[gi], wf[1], xb[h][i]); }
      else { mfma_acc(ag[gi], wf[2 * ks], xb[h][i]); mfma_acc(au[gi], wf[2 * ks + 1], xb[h][i]); }
      if (h == 0) rd_tile(ks, 4 + i, xb[1][i]);
      else if (ks < 13) rd_tile(ks + 1, i, xb[0][i]);
      issue(c_opA(ks, gi)); issue(c_opB(ks, gi));
      __builtin_amdgcn_sched_barrier(0);
    }
  }
  mfma_fence();
  SST(2);
  u32x4 xres;
  {
    const unsigned c = 4 * (P & 1) + (w & 3);
    lds_cptr a = (lds_cptr)smem + (ta[0] ^ (32u * (c ^ (unsigned)w))) + (2 * Q + (w >> 2)) * 8192;
    const u32x2 t0 = tr_read(a), t1 = tr_read(a + 1024);
    xres = u32x4{t0[0], t0[1], t1[0], t1[1]};
  }
  vmwait(C_SCHED.wait_st);
  for (unsigned spins = 0; spins < (1u << 16); ++spins) {
    u32 mn = 0xffffffffu;
#pragma unroll
    for (int i = 0; i < 8; ++i) { asm volatile("" : "+v"(stw[i])); mn = min(mn, stw[i]); }
    if (__all(mn >= 0x10000u)) break;
#pragma unroll
    for (int i = 0; i < 8; ++i) ld_st(i);
    vmwait(0);
  }
  SST(3);
  {
    const float* uvl = (const float*)(smem + C_UV) + 16 * w + 4 * g;
    const f32x4 ug = *(const f32x4*)(uvl), uu = *(const f32x4*)(uvl + 256), vu = *(const f32x4*)(uvl + 384);
    const f32x4 vg = *(const f32x4*)(uvl + 128) * -1.4426950408889634f;
    const char* hh = p.ws + WS_HH + (size_t)(Lt * 8 * 56 + 4 * Fi + (w >> 1)) * 1024;
    const u32 hoff = (u32)((16 * (2 * (w & 1) + (g >> 1)) + (lane & 15)) * 16 + 8 * (g & 1));
#pragma unroll
    for (int i = 0; i < 8; ++i) {
      const int c = i ^ w;
      const float m = bflo(stw[i]), rs = __builtin_amdgcn_rcpf(bfhi(stw[i]));
      const float rsg = rs * -1.4426950408889634f;
      f32x4 gv = (ag[i] - m * ug) * rsg + vg;
      f32x4 uv = (au[i] - m * uu) * rs + vu;
      f32x4 h;
#pragma unroll
      for (int r = 0; r < 4; ++r) h[r] = __builtin_amdgcn_rcpf(1.0f + __builtin_amdgcn_exp2f(gv[r])) * uv[r];
      const u32x2 o = {pkbf(h[0], h[1]), pkbf(h[2], h[3])};
      asm volatile("global_store_dwordx2 %0, %1, %2 sc1" :: "v"(hoff), "v"(o), "s"(hh + (size_t)c * 56 * 1024) : "memory");
      if (2 * i < C_WDE) { ld_wd(C_WDM + 2 * i); ld_wd(C_WDM + 2 * i + 1); }
    }
  }
#pragma unroll
  for (int n = C_WDM + C_WDE; n < 28; ++n) ld_wd(n);
  SST(4);
  vmwait(28 - C_WDM - C_WDE);
  asm volatile("s_barrier" ::: "memory");
  SST(5);
  typedef __attribute__((address_space(1))) unsigned gu32;
  gu32* cnt = (gu32*)(p.ws + WS_FLG) + Lt * 32;
  if (tid == 0) __hip_atomic_fetch_add(cnt, 1u, __ATOMIC_RELAXED, __HIP_MEMORY_SCOPE_AGENT);
  const int dcol = 64 * Q + 32 * (w >> 2) + 8 * g;
  u32x4 gb0, gb1;
  gload16(gb0, p.ws + WS_MOD + 5 * D * 4, (u32)dcol * 4, 0);
  gload16(gb1, p.ws + WS_MOD + 5 * D * 4, (u32)dcol * 4, 16);
  if (w == 0) {
    for (unsigned spins = 0; spins < (1u << 20); ++spins) {
      if (__hip_atomic_load(cnt, __ATOMIC_RELAXED, __HIP_MEMORY_SCOPE_AGENT) >= 14u) break;
      __builtin_amdgcn_s_sleep(1);
    }
  }
  asm volatile("s_barrier" ::: "memory");
  SST(6);
  u32x4 hb[3][7];
  const char* hsrc = p.ws + WS_HH + (size_t)(4 * P * 56 + 7 * w) * 1024;
  auto ld_il = [&](int il, u32x4 (&dst)[7]) {
#pragma unroll
    for (int j = 0; j < 7; ++j) gload16_sc1(dst[j], hsrc + (size_t)il * 56 * 1024 + (j >> 2) * 4096, l16, (j & 3) * 1024);
  };
  ld_il(0, hb[0]); ld_il(1, hb[1]); ld_il(2, hb[2]);
#pragma unroll
  for (int n = 0; n < 28; ++n) PIN4(wf[n]);
#pragma unroll
  for (int il = 0; il < 4; ++il) {
    f32x4 acc[4];
#pragma unroll
    for (int j = 0; j < 7; ++j) {
      vmwait((il == 0 ? 14 : (il == 1 ? 14 : (il == 2 ? 7 : 0))) + 6 - j);
      PIN4(hb[il % 3][j]);
#pragma unroll
      for (int id = 0; id < 4; ++id) { if (j == 0) mfma_first(acc[id], wf[id * 7], hb[il % 3][0]); else mfma_acc(acc[id], wf[id * 7 + j], hb[il % 3][j]); }
      __builtin_amdgcn_sched_barrier(0);
    }
    if (il == 0) ld_il(3, hb[0]);
    mfma_fence();
#pragma unroll
    for (int id = 0; id < 4; ++id) *(f32x4*)(smem + ((w * 4 + il) * 4 + id) * 1024 + l16) = acc[id];
    __builtin_amdgcn_sched_barrier(0);
  }
  SST(7);
  __syncthreads();
  {
    f32x4 o0 = {0, 0, 0, 0}, o1 = {0, 0, 0, 0};
    const char* rb = smem + ((w & 3) * 4 + 2 * (w >> 2) + (g >> 1)) * 1024 + (32 * (g & 1) + (lane & 15)) * 16;
#pragma unroll
    for (int s = 0; s < 8; ++s) { o0 += *(const f32x4*)(rb + s * 16384); o1 += *(const f32x4*)(rb + s * 16384 + 256); }
    PIN4(gb0); PIN4(gb1);
    const f32x4 g0 = __builtin_bit_cast(f32x4, gb0), g1 = __builtin_bit_cast(f32x4, gb1);
    float r[8];
#pragma unroll
    for (int k = 0; k < 4; ++k) { r[k] = bfr(g0[k] * bfr(o0[k])); r[4 + k] = bfr(g1[k] * bfr(o1[k])); }
    u32x4 o;
#pragma unroll
    for (int k = 0; k < 4; ++k) o[k] = pkbf(bflo(xres[k]) + r[2 * k], bfhi(xres[k]) + r[2 * k + 1]);
    const int lrow = 64 * P + 16 * (w & 3) + (lane & 15);
    *(u32x4*)(p.out + (size_t)lrow * D + dcol) = o;
  }
  SST(8); SEND(2, 10);
}

extern "C" void kernel_launch(void* const* d_in, const int* in_sizes, int n_in, void* d_out, int out_size, void* d_ws,
                              size_t ws_size, hipStream_t stream) {
  Params p;
  p.u = (const u16*)d_in[0]; p.A_re = (const u16*)d_in[1]; p.A_im = (const u16*)d_in[2]; p.B_re = (const u16*)d_in[3];
  p.B_im = (const u16*)d_in[4]; p.C_re = (const u16*)d_in[5]; p.C_im = (const u16*)d_in[6]; p.log_dt = (const u16*)d_in[7];
  p.Dskip = (const u16*)d_in[8]; p.w_gate = (const u16*)d_in[10]; p.w_up = (const u16*)d_in[11]; p.w_down = (const u16*)d_in[12];
  p.c = (const u16*)d_in[13]; p.w_mod = (const u16*)d_in[14];
  p.ws = (char*)d_ws; p.out = (u16*)d_out;
  hipFuncSetAttribute((const void*)kB, hipFuncAttributeMaxDynamicSharedMemorySize, B_LDS);
  hipFuncSetAttribute((const void*)kCD, hipFuncAttributeMaxDynamicSharedMemorySize, CD_LDS);
  kA<<<A_NLN + A_NKG + A_NMOD + A_NWD, 256, A_LDS, stream>>>(p);
  kB<<<B_NCONV + B_NWP, 512, B_LDS, stream>>>(p);
  kCD<<<256, 512, CD_LDS, stream>>>(p);
}
```

```cpp
#include <hip/hip_runtime.h>
#include <hip/hip_bf16.h>
#include <stdint.h>

typedef unsigned short u16;
typedef unsigned int u32;
typedef __attribute__((ext_vector_type(8))) short s16x8;
typedef __attribute__((ext_vector_type(4))) short s16x4;
typedef __attribute__((ext_vector_type(8))) _Float16 f16x8;
typedef __attribute__((ext_vector_type(2))) _Float16 f16x2;
typedef __attribute__((ext_vector_type(2))) __bf16 bf16x2;
typedef __attribute__((ext_vector_type(8))) __bf16 bf16x8;
typedef __attribute__((ext_vector_type(2))) float f32x2;
typedef __attribute__((ext_vector_type(4))) float f32x4;
typedef __attribute__((ext_vector_type(16))) float f32x16;
typedef __attribute__((ext_vector_type(2))) unsigned u32x2;
typedef __attribute__((ext_vector_type(4))) unsigned u32x4;
typedef __attribute__((address_space(3))) char* lds_ptr;
typedef __attribute__((address_space(3))) const char* lds_cptr;

constexpr int L = 2048, D = 448, NS = 256, F = 1792;

constexpr size_t WS_EP = 0;
constexpr size_t WS_RD = 2048;
constexpr size_t WS_MG = 4096;
constexpr size_t WS_SG = 32768;
constexpr size_t WS_MOD = 49152;
constexpr size_t WS_FLG = 61440;
constexpr size_t WS_UV = 65536;
constexpr size_t WS_STG = 98304;
constexpr size_t WS_WDF = 131072;
constexpr size_t WS_WF = WS_WDF + (size_t)F * D * 2;
constexpr size_t WS_X1T = WS_WF + (size_t)2 * F * D * 2;
constexpr size_t WS_X1C = WS_X1T + (size_t)D * L * 2;
constexpr size_t WS_HH = WS_X1C + (size_t)D * L * 2;

struct Params {
  const u16* u; const u16* A_re; const u16* A_im; const u16* B_re; const u16* B_im; const u16* C_re; const u16* C_im;
  const u16* log_dt; const u16* Dskip; const u16* w_gate; const u16* w_up; const u16* w_down; const u16* c; const u16* w_mod;
  char* ws; u16* out;
};

#define SDECL(n) do {} while (0)
#define SST(i) do {} while (0)
#define SSTE(i) do {} while (0)
#define SEND(kid, n) do {} while (0)
__device__ __forceinline__ float bf2f(u16 v) { return __uint_as_float((u32)v << 16); }
__device__ __forceinline__ float bflo(u32 v) { return __uint_as_float(v << 16); }
__device__ __forceinline__ float bfhi(u32 v) { return __uint_as_float(v & 0xffff0000u); }
__device__ __forceinline__ u32 pkbf(float lo, float hi) {
  f32x2 v = {lo, hi}; bf16x2 b = __builtin_convertvector(v, bf16x2); return __builtin_bit_cast(u32, b);
}
__device__ __forceinline__ float bfr(float x) { return bflo(pkbf(x, x)); }
__device__ __forceinline__ u32 pkh(float lo, float hi) {
  f32x2 v = {lo, hi}; f16x2 b = __builtin_convertvector(v, f16x2); return __builtin_bit_cast(u32, b);
}
template <int CTRL> __device__ __forceinline__ float dppf(float x) {
  return __builtin_bit_cast(float, __builtin_amdgcn_mov_dpp(__builtin_bit_cast(int, x), CTRL, 0xf, 0xf, true));
}
__device__ __forceinline__ float xrow16_sum(float x) {
  auto s = __builtin_amdgcn_permlane16_swap(__float_as_uint(x), __float_as_uint(x), false, false);
  x = __uint_as_float(s[0]) + __uint_as_float(s[1]);
  auto t = __builtin_amdgcn_permlane32_swap(__float_as_uint(x), __float_as_uint(x), false, false);
  return __uint_as_float(t[0]) + __uint_as_float(t[1]);
}
__device__ __forceinline__ float xrow16_max(float x) {
  auto s = __builtin_amdgcn_permlane16_swap(__float_as_uint(x), __float_as_uint(x), false, false);
  x = fmaxf(__uint_as_float(s[0]), __uint_as_float(s[1]));
  auto t = __builtin_amdgcn_permlane32_swap(__float_as_uint(x), __float_as_uint(x), false, false);
  return fmaxf(__uint_as_float(t[0]), __uint_as_float(t[1]));
}
__device__ __forceinline__ float wave_sum(float x) {
  x += dppf<0xB1>(x); x += dppf<0x4E>(x); x += dppf<0x141>(x); x += dppf<0x128>(x);
  return xrow16_sum(x);
}
__device__ __forceinline__ float wave_max(float x) {
  x = fmaxf(x, dppf<0xB1>(x)); x = fmaxf(x, dppf<0x4E>(x)); x = fmaxf(x, dppf<0x141>(x)); x = fmaxf(x, dppf<0x128>(x));
  return xrow16_max(x);
}
__device__ __forceinline__ float dot2bf(u32 a, u32 b, float acc) {
  return __builtin_amdgcn_fdot2_f32_bf16(__builtin_bit_cast(bf16x2, a), __builtin_bit_cast(bf16x2, b), acc, false);
}
typedef short v4i16_t __attribute__((ext_vector_type(4)));
__device__ __forceinline__ u32x2 tr_read(lds_cptr p) {
  return __builtin_bit_cast(u32x2, __builtin_amdgcn_ds_read_tr16_b64_v4i16((__attribute__((address_space(3))) v4i16_t*)p));
}
__device__ __forceinline__ void glds16(const void* g, unsigned lds_base) {
  unsigned sv;
  asm volatile("s_mov_b32 %0, m0\n\ts_mov_b32 m0, %2\n\ts_nop 0\n\tglobal_load_lds_dwordx4 %1, off\n\ts_mov_b32 m0, %0"
               : "=&s"(sv) : "v"(g), "s"(lds_base) : "memory");
}
__device__ __forceinline__ float pow2_scale(float x, int target) {
  const int e = (int)((__float_as_uint(x) >> 23) & 255u);
  const int s = min(max(target + 127 - e, -100), 100);
  return __uint_as_float(((u32)(e - 1) >= 254u) ? 0x3F800000u : (u32)(127 + s) << 23);
}
#define WT_AUX 16
__device__ __forceinline__ void gload16(u32x4& dst, const void* sbase, u32 voff, int imm) {
  asm volatile("global_load_dwordx4 %0, %1, %2 offset:%3" : "=v"(dst) : "v"(voff), "s"(sbase), "i"(imm) : "memory");
}
__device__ __forceinline__ void gload16_sc1(u32x4& dst, const void* sbase, u32 voff, int imm) {
  asm volatile("global_load_dwordx4 %0, %1, %2 offset:%3 sc1" : "=v"(dst) : "v"(voff), "s"(sbase), "i"(imm) : "memory");
}
__device__ __forceinline__ void glds16s(const void* sbase, u32 voff, unsigned lds_base) {
  asm volatile("s_mov_b32 m0, %2\n\ts_nop 0\n\tglobal_load_lds_dwordx4 %0, %1" :: "v"(voff), "s"(sbase), "s"(lds_base) : "memory");
}
#define MFMA16(a, b, c) __builtin_amdgcn_mfma_f32_16x16x32_bf16(__builtin_bit_cast(bf16x8, a), __builtin_bit_cast(bf16x8, b), c, 0, 0, 0)
__device__ __forceinline__ void mfma_acc(f32x4& c, const u32x4& a, const u32x4& b) {
  asm volatile("v_mfma_f32_16x16x32_bf16 %0, %1, %2, %0" : "+v"(c) : "v"(a), "v"(b));
}
__device__ __forceinline__ void mfma_first(f32x4& c, const u32x4& a, const u32x4& b) {
  asm volatile("v_mfma_f32_16x16x32_bf16 %0, %1, %2, 0" : "=&v"(c) : "v"(a), "v"(b));
}
__device__ __forceinline__ void mfma_fence() { asm volatile("s_nop 7\n\ts_nop 7" ::: "memory"); }
#define VMWAIT(n) case n: asm volatile("s_waitcnt vmcnt(" #n ")" ::: "memory"); break
__device__ __forceinline__ void vmwait(int n) {
  switch (n) {
    VMWAIT(0); VMWAIT(1); VMWAIT(2); VMWAIT(3); VMWAIT(4); VMWAIT(5); VMWAIT(6); VMWAIT(7); VMWAIT(8); VMWAIT(9);
    VMWAIT(10); VMWAIT(11); VMWAIT(12); VMWAIT(13); VMWAIT(14); VMWAIT(15); VMWAIT(16); VMWAIT(17); VMWAIT(18); VMWAIT(19);
    VMWAIT(20); VMWAIT(21); VMWAIT(22); VMWAIT(23); VMWAIT(24); VMWAIT(25); VMWAIT(26); VMWAIT(27); VMWAIT(28); VMWAIT(29);
    VMWAIT(30); VMWAIT(31); VMWAIT(32); VMWAIT(33); VMWAIT(34); VMWAIT(35); VMWAIT(36); VMWAIT(37); VMWAIT(38); VMWAIT(39);
    default: asm volatile("s_waitcnt vmcnt(0)" ::: "memory"); break;
  }
}
#define PIN4(x) asm volatile("" : "+v"(x))

typedef __attribute__((address_space(1))) unsigned gu32;
typedef __attribute__((address_space(1))) unsigned long long gu64;
constexpr u32 TAGC = 0x9E3779B9u;
constexpr int AB_MAIN = 224;
constexpr int T_RS = 1056, T_P = 32 * T_RS, T_CH = 53 * T_RS;
constexpr int L_R = 65536, R_CP = 4160, R_CH = 4 * R_CP;
constexpr int L_W = 2 * T_CH;
constexpr int L_KT = L_W + 24576, KT_CH = 2832, KT_Z = 1410;
constexpr int L_H = L_KT + 2 * KT_CH, H_CH = 4608;
constexpr int L_SC = L_H + 2 * H_CH, AB_MAIN_LDS = L_SC + 64;
constexpr u32 KT_B0 = 0x3F7F;

__device__ __forceinline__ float rnd(float x) { return __uint_as_float(pkbf(0.f, x)); }
__device__ __forceinline__ f32x2 rnd2(f32x2 v) { return f32x2{rnd(v.x), rnd(v.y)}; }
__device__ __forceinline__ u32 pkh2(f32x2 v) { return __builtin_bit_cast(u32, __builtin_convertvector(v, f16x2)); }
struct cm { f32x2 v, n; };
__device__ __forceinline__ cm mkcm(f32x2 b) { return {b, f32x2{-b.y, b.x}}; }
__device__ __forceinline__ f32x2 cmul(f32x2 a, const cm& b) { return __builtin_elementwise_fma(a.yy, b.n, a.xx * b.v); }
__device__ __forceinline__ f32x2 cexpm(float zr, float zi, float m) {
  const float mag = __builtin_amdgcn_exp2f(zr * (m * 1.4426950408889634f));
  const float rev = __builtin_amdgcn_fractf(zi * (m * 0.15915494309189535f));
  return f32x2{mag * __builtin_amdgcn_cosf(rev), mag * __builtin_amdgcn_sinf(rev)};
}
__device__ __forceinline__ void st_gran(char* base, int idx, u32 val, u32 tag) {
  __hip_atomic_store((gu64*)base + idx, ((unsigned long long)tag << 32) | val, __ATOMIC_RELAXED, __HIP_MEMORY_SCOPE_AGENT);
}
#define M0SET "s_mov_b32 m0, %0\n\ts_nop 0\n\t"
template <int O0, int O1, int O2, int O3>
__device__ __forceinline__ void wr_rows4(u32 m0v, u32 a, u32 b, u32 c, u32 d) {
  asm volatile(M0SET "ds_write_addtid_b32 %1 offset:%5\n\tds_write_addtid_b32 %2 offset:%6\n\t"
               "ds_write_addtid_b32 %3 offset:%7\n\tds_write_addtid_b32 %4 offset:%8"
               :: "s"(m0v), "v"(a), "v"(b), "v"(c), "v"(d), "i"(O0), "i"(O1), "i"(O2), "i"(O3) : "memory");
}
template <int O0>
__device__ __forceinline__ void wr_rows1(u32 m0v, u32 a) {
  asm volatile(M0SET "ds_write_addtid_b32 %1 offset:%2" :: "s"(m0v), "v"(a), "i"(O0) : "memory");
}
__device__ __forceinline__ void lds_barrier() { asm volatile("s_waitcnt lgkmcnt(0)\n\ts_barrier" ::: "memory"); }
#define LDSP(T, off) ((__attribute__((address_space(3))) T*)((lds_ptr)smem + (off)))
#define MFMA16H(a, b, c) __builtin_amdgcn_mfma_f32_16x16x32_f16(__builtin_bit_cast(f16x8, a), __builtin_bit_cast(f16x8, b), c, 0, 0, 0)
__device__ __forceinline__ u32 kt_off(float l) {
  u32 x = pkbf(0.f, l);
  x = min(max(x, KT_B0 << 16), (KT_B0 + KT_Z) << 16);
  return (x >> 15) - 2 * KT_B0;
}

__device__ __forceinline__ void glds16v(const void* sbase, u32 voff, unsigned lds_base) {
  asm volatile("s_mov_b32 m0, %2\n\ts_nop 0\n\tglobal_load_lds_dwordx4 %0, %1" :: "v"(voff), "s"(sbase), "s"(lds_base) : "memory");
}
__device__ __forceinline__ void reload16_sc1(u32x4& dst, const void* sbase, u32 voff, int imm) {
  asm volatile("global_load_dwordx4 %0, %1, %2 offset:%3 sc1" : "+v"(dst) : "v"(voff), "s"(sbase), "i"(imm) : "memory");
}
__device__ __forceinline__ void vm0() { asm volatile("s_waitcnt vmcnt(0)" ::: "memory"); }
template <int N> __device__ __forceinline__ void vmw() { asm volatile("s_waitcnt vmcnt(%0)" :: "i"(N) : "memory"); }
#define PIN(x) asm volatile("" : "+v"(x))
#define SB() __builtin_amdgcn_sched_barrier(0)
__device__ __forceinline__ u32x4 frag128(const char* smem, int base, int lbase, int phi, int ks, int t) {
  lds_cptr a = (lds_cptr)smem + base + lbase + 32 * (t ^ phi) + ks * 4096;
  const u32x2 t0 = tr_read(a), t1 = tr_read(a + 512);
  return u32x4{t0[0], t0[1], t1[0], t1[1]};
}
__device__ __forceinline__ void st16_wt(const __amdgpu_buffer_rsrc_t& r, int off, u32x4 v) { __builtin_amdgcn_raw_buffer_store_b128(v, r, off, 0, WT_AUX); }
__device__ __forceinline__ u32 cld32(const void* q) { return *(const __attribute__((address_space(4))) u32*)(uintptr_t)q; }
__device__ __forceinline__ float pow2_inv(float s) { return __uint_as_float(0x7F000000u - __float_as_uint(s)); }
__device__ __forceinline__ float rlane(u32 v, int l) { return __uint_as_float(__builtin_amdgcn_readlane(v, l)); }
#define LDS4(adr) (*(const __attribute__((address_space(3))) u32x4*)(adr))
#define LDS2(adr) (*(const __attribute__((address_space(3))) u32x2*)(adr))

__device__ __forceinline__ void ln_rows(const Params& p, u32x4 (&xr)[14], int row16, int lane, u32 tag) {
  const int col = lane & 15, g = lane >> 4;
  f32x4 ag = {0, 0, 0, 0}, as = {0, 0, 0, 0};
  const u32x4 ones = {0x3F803F80u, 0x3F803F80u, 0x3F803F80u, 0x3F803F80u};
#pragma unroll
  for (int s = 0; s < 14; ++s) { PIN4(xr[s]); ag = MFMA16(xr[s], xr[s], ag); as = MFMA16(xr[s], ones, as); }
  const int r = col & 3;
  const float qq = r == 0 ? ag[0] : (r == 1 ? ag[1] : (r == 2 ? ag[2] : ag[3]));
  const float ss = r == 0 ? as[0] : (r == 1 ? as[1] : (r == 2 ? as[2] : as[3]));
  const float mean = ss * (1.0f / D);
  const float var = fmaxf(qq * (1.0f / D) - mean * mean, 0.f);
  const float sd = sqrtf(rnd(rnd(var) + 1e-5f));
  if (g == (col >> 2)) st_gran(p.ws + WS_SG, row16 + col, pkbf(mean, sd), tag);
}

__device__ __forceinline__ void conv_main(const Params& p, int b, char* smem) {
  const int tid = threadIdx.x, lane = tid & 63;
  const int w = __builtin_amdgcn_readfirstlane(tid >> 6), ch = w >> 2, pq = w & 3;
  const int cp = (b & 7) * 28 + (b >> 3), d0 = 2 * cp, d = d0 + ch;
  const int col = lane & 15, g = lane >> 4;
  const unsigned lds0 = (unsigned)(uintptr_t)smem;
  SDECL(14); SST(0);
  const bool ln = w == 0 && b < 128;
  u32x4 xr[14];
  if (ln) {
    const char* xs = (const char*)(p.u + (size_t)(16 * b) * D);
    const u32 xo = (u32)(col * D + 8 * g) * 2;
#pragma unroll
    for (int s = 0; s < 14; ++s) gload16(xr[s], xs, xo, s * 64);
  }
  u32 pa[6];
  {
    const u32 po = (u32)(d * NS + pq * 64 + lane) * 2;
    const u16* const ps[6] = {p.A_re, p.A_im, p.B_re, p.B_im, p.C_re, p.C_im};
#pragma unroll
    for (int i = 0; i < 6; ++i) asm volatile("global_load_short_d16_hi %0, %1, %2" : "=v"(pa[i]) : "v"(po), "s"(ps[i]) : "memory");
  }
  asm volatile("s_barrier" ::: "memory");
  const int row0 = 256 * w + 16 * col + 4 * g;
  const u32 xo4 = (u32)row0 * (D * 2) + cp * 4;
  u32 xg[4];
  asm volatile("global_load_dword %0, %1, %2" : "=v"(xg[0]) : "v"(xo4), "s"(p.u) : "memory");
  asm volatile("global_load_dword %0, %1, %2 offset:896" : "=v"(xg[1]) : "v"(xo4), "s"(p.u) : "memory");
  const u32 ldt2 = cld32(p.log_dt + d0), dsk2 = cld32(p.Dskip + d0), tag = cld32(p.ws + WS_EP) + TAGC;
  SB();
  if (tid < 60) *LDSP(u32x4, L_H + (tid >= 30 ? H_CH - 480 : 0) + tid * 16) = u32x4{0, 0, 0, 0};
  if (tid >= 128 && tid < 130) *LDSP(u16, L_KT + (tid & 1) * KT_CH + 2 * KT_Z) = 0;
  const u32 ktb = L_KT + ch * KT_CH;
  u32 gofs[11];
  {
    const float l0 = (float)(2047 - 8 * (tid & 255));
#pragma unroll
    for (int j = 0; j < 11; ++j) { gofs[j] = ktb + kt_off(l0 - (float)j); PIN(gofs[j]); }
  }
  u32 kso[4];
  const int rt = pq & 1, ct = pq >> 1;
  {
    const unsigned long long tb = rt ? 0x7000700008000400ull : 0x0200010000040000ull;
    const unsigned long long t1 = rt ? 0x0000000070000100ull : 0x0004000200010001ull;
    const unsigned long long t2 = rt ? 0x0000000070000200ull : 0x0100000400020002ull;
    const int b0 = (int)(tb >> (16 * g)) & 0xffff, s1 = (int)(t1 >> (16 * g)) & 0xffff, s2 = (int)(t2 >> (16 * g)) & 0xffff;
    const int qc = 8 * (16 * ct + col);
#pragma unroll
    for (int r = 0; r < 4; ++r) {
      const int lb = b0 + (r & 1) * s1 + (r >> 1) * s2 + qc;
      kso[r] = ktb + (lb <= 2048 ? kt_off((float)lb) : 2 * KT_Z + 2);
      PIN(kso[r]);
    }
  }
  const float dt = rnd(expf(ch ? bfhi(ldt2) : bflo(ldt2)));
  SB();
  SST(1);
  if (ln) { vmw<8>(); ln_rows(p, xr, 16 * b, lane, tag); vmw<3>(); }
  else vmw<2>();
#pragma unroll
  for (int i = 0; i < 6; ++i) PIN(pa[i]);
  if (tid == 448) __hip_atomic_store((gu32*)(p.ws + WS_RD) + b, tag, __ATOMIC_RELAXED, __HIP_MEMORY_SCOPE_AGENT);
  SST(2);
  const u32 m0v = lds0 + ch * T_CH + pq * 256;
  const float zr = dt * bfhi(pa[0]), zi = dt * bfhi(pa[1]);
  {
    const f32x2 E8 = cexpm(zr, zi, 8.f);
    const cm m8 = mkcm(E8);
    const f32x2 E16 = cmul(E8, m8);
    const cm m16 = mkcm(E16);
    const cm m32 = mkcm(cmul(E16, m16));
    f32x2 q0 = {1.f, 0.f}, q1 = E8, q2 = E16, q3 = cmul(E16, m8);
#define QW(gq) wr_rows4<(4 * gq) * T_RS, (4 * gq + 1) * T_RS, (4 * gq + 2) * T_RS, (4 * gq + 3) * T_RS>(m0v, pkh2(q0), pkh2(q1), pkh2(q2), pkh2(q3))
#define QS() do { q0 = cmul(q0, m32); q1 = cmul(q1, m32); q2 = cmul(q2, m32); q3 = cmul(q3, m32); } while (0)
    QW(0); q0 = m32.v; q1 = cmul(q1, m32); q2 = cmul(q2, m32); q3 = cmul(q3, m32);
    QW(1); QS(); QW(2); QS(); QW(3); QS(); QW(4); QS(); QW(5); QS(); QW(6); QS(); QW(7);
#undef QW
#undef QS
  }
  asm volatile("global_load_dword %0, %1, %2 offset:1792" : "=v"(xg[2]) : "v"(xo4), "s"(p.u) : "memory");
  asm volatile("global_load_dword %0, %1, %2 offset:2688" : "=v"(xg[3]) : "v"(xo4), "s"(p.u) : "memory");
  SST(3);
  lds_barrier();
  u32x4 fq[16];
  {
    lds_cptr pb = (lds_cptr)smem + ch * T_CH + (16 * ct + col) * T_RS + 16 * g;
#pragma unroll
    for (int s = 0; s < 16; ++s) fq[s] = LDS4(pb + 64 * s);
  }
  SB();
  {
    const float br = dt * bfhi(pa[2]), bi = dt * bfhi(pa[3]), cr = bfhi(pa[4]), ci = bfhi(pa[5]);
    f32x2 W = {cr * br - ci * bi, -(cr * bi + ci * br)};
    const float S = wave_sum(fabsf(W.x) + fabsf(W.y));
    const float sw = pow2_scale(S, 14);
    W *= sw;
    if (lane == 0) *LDSP(f32x2, L_SC + w * 8) = f32x2{pow2_inv(sw), S};
    const f32x2 E1 = cexpm(zr, -zi, 1.f), E256 = cexpm(zr, -zi, 256.f);
    const cm m1 = mkcm(E1), m256 = mkcm(E256);
    const f32x2 E2 = cmul(E1, m1);
    const cm m2 = mkcm(E2);
    const f32x2 E4 = cmul(E2, m2);
    const cm m4 = mkcm(E4);
    const f32x2 a1 = cmul(W, m1), a2 = cmul(W, m2), a3 = cmul(a1, m2);
    wr_rows4<T_P, T_P + T_RS, T_P + 2 * T_RS, T_P + 3 * T_RS>(m0v, pkh2(W), pkh2(a1), pkh2(a2), pkh2(a3));
    wr_rows4<T_P + 4 * T_RS, T_P + 5 * T_RS, T_P + 6 * T_RS, T_P + 7 * T_RS>(m0v, pkh2(cmul(W, m4)), pkh2(cmul(a1, m4)), pkh2(cmul(a2, m4)), pkh2(cmul(a3, m4)));
    const f32x2 W1 = cmul(W, m256), b1 = cmul(W1, m2), b2 = cmul(W1, m4), b3 = cmul(b1, m4);
    wr_rows4<T_P + 8 * T_RS, T_P + 9 * T_RS, T_P + 10 * T_RS, T_P + 11 * T_RS>(m0v, pkh2(W1), pkh2(b1), pkh2(b2), pkh2(b3));
    const f32x2 W2 = cmul(W1, m256), W3 = cmul(W2, m256);
    wr_rows4<T_P + 12 * T_RS, T_P + 13 * T_RS, T_P + 14 * T_RS, T_P + 15 * T_RS>(m0v, pkh2(W2), pkh2(cmul(W2, m4)), pkh2(W3), pkh2(cmul(W3, m4)));
    const f32x2 W4 = cmul(W3, m256), W5 = cmul(W4, m256), W6 = cmul(W5, m256), W7 = cmul(W6, m256);
    wr_rows4<T_P + 16 * T_RS, T_P + 17 * T_RS, T_P + 18 * T_RS, T_P + 19 * T_RS>(m0v, pkh2(W4), pkh2(W5), pkh2(W6), pkh2(W7));
    wr_rows1<T_P + 20 * T_RS>(m0v, pkh2(cmul(W7, m256)));
  }
  SST(4);
  lds_barrier();
  float kt0, kt1;
  {
    lds_cptr pa_ = (lds_cptr)smem + ch * T_CH + T_P + (16 * rt + col) * T_RS + 16 * g;
    u32x4 fp[16];
#pragma unroll
    for (int s = 0; s < 4; ++s)
#pragma unroll
      for (int c = 0; c < 4; ++c) fp[4 * c + s] = LDS4(pa_ + 256 * c + 64 * s);
    const f32x4 s0 = *LDSP(f32x4, L_SC), s1 = *LDSP(f32x4, L_SC + 16), s2 = *LDSP(f32x4, L_SC + 32), s3 = *LDSP(f32x4, L_SC + 48);
    SB();
    f32x4 acc[4];
#pragma unroll
    for (int s = 0; s < 4; ++s)
#pragma unroll
      for (int c = 0; c < 4; ++c) acc[c] = MFMA16H(fp[4 * c + s], fq[4 * c + s], (s == 0 ? f32x4{0, 0, 0, 0} : acc[c]));
    kt0 = pow2_scale(s0[1] + s0[3] + s1[1] + s1[3], 14);
    kt1 = pow2_scale(s2[1] + s2[3] + s3[1] + s3[3], 14);
    const float kt = ch ? kt1 : kt0;
    const f32x4 sa = ch ? s2 : s0, sb = ch ? s3 : s1;
    const f32x4 kv = acc[0] * (sa[0] * kt) + acc[1] * (sa[2] * kt) + acc[2] * (sb[0] * kt) + acc[3] * (sb[2] * kt);
#pragma unroll
    for (int r = 0; r < 4; ++r) *LDSP(_Float16, kso[r]) = (_Float16)kv[r];
  }
  u32x4 st0, st1, mg;
  const u32 sgo = (u32)WS_SG + row0 * 8, mgo = (u32)WS_MG + (((lane & 3) == 3 ? 4 : (lane & 3)) * D + d0) * 8;
  gload16_sc1(st0, p.ws, sgo, 0); gload16_sc1(st1, p.ws, sgo, 16); gload16_sc1(mg, p.ws, mgo, 0);
  const int wt = b < 196 ? b : b - 196, wta = wt / 7, wtb = wt - 7 * wta;
  const int q4 = col >> 2, pp = lane & 3;
  {
    const int dchunk = (lane & 7) ^ ((((lane >> 4) & 1) | ((w & 1) << 1)) << 1);
    const u32 vf = (u32)((lane >> 3) * F + dchunk * 8) * 2, vd = (u32)((lane >> 3) * D + dchunk * 8) * 2;
    glds16v(p.w_gate + (size_t)(64 * wtb + 8 * w) * F + 64 * wta, vf, lds0 + L_W + w * 1024);
    glds16v(p.w_up + (size_t)(64 * wtb + 8 * w) * F + 64 * wta, vf, lds0 + L_W + 8192 + w * 1024);
    glds16v(p.w_down + (size_t)(64 * wta + 8 * w) * D + 64 * wtb, vd, lds0 + L_W + 16384 + w * 1024);
  }
  SST(5);
  lds_barrier();
  {
    u32 v[11];
#pragma unroll
    for (int j = 0; j < 11; ++j) v[j] = *LDSP(u16, gofs[j]);
    u32 e[5], o[5];
#pragma unroll
    for (int j = 0; j < 5; ++j) e[j] = v[2 * j] | (v[2 * j + 1] << 16);
#pragma unroll
    for (int j = 0; j < 4; ++j) o[j] = __builtin_amdgcn_alignbit(e[j + 1], e[j], 16);
    o[4] = (e[4] >> 16) | (v[10] << 16);
    const int rb = L_R + ch * R_CH + (tid & 255) * 16;
    *LDSP(u32x4, rb) = u32x4{e[0], e[1], e[2], e[3]};
    *LDSP(u32x4, rb + R_CP) = u32x4{o[0], o[1], o[2], o[3]};
    *LDSP(u32x4, rb + 2 * R_CP) = u32x4{e[1], e[2], e[3], e[4]};
    *LDSP(u32x4, rb + 3 * R_CP) = u32x4{o[1], o[2], o[3], o[4]};
    if ((tid & 255) < 8) *LDSP(u32x4, L_R + ch * R_CH + ((tid & 7) >> 1) * R_CP + 4096 + (tid & 1) * 16) = u32x4{0, 0, 0, 0};
  }
  SST(6);
  lds_barrier();
  u32x4 A[2][8];
  {
    const int e0 = 2031 - col + 8 * g, c4 = e0 & 3;
    lds_cptr ab = (lds_cptr)smem + L_R + ch * R_CH + c4 * R_CP + 2 * (e0 - c4) - 64 * 63 - 64 * pq;
#pragma unroll
    for (int k = 0; k < 8; ++k)
#pragma unroll
      for (int u = 0; u < 2; ++u) {
        const u32x2 lo = LDS2(ab + 64 * (63 - 4 * u - 8 * k)), hi = LDS2(ab + 64 * (63 - 4 * u - 8 * k) + 8);
        A[u][k] = u32x4{lo[0], lo[1], hi[0], hi[1]};
      }
  }
  SB();
  vmw<3>();
  for (unsigned spins = 0; spins < (1u << 20); ++spins) {
    PIN4(st0); PIN4(st1); PIN4(mg);
    const bool ok = st0[1] == tag && st0[3] == tag && st1[1] == tag && st1[3] == tag && (((lane & 3) >= 2) || (mg[1] == tag && mg[3] == tag));
    if (__all(ok)) break;
    __builtin_amdgcn_s_sleep(1);
    reload16_sc1(st0, p.ws, sgo, 0); reload16_sc1(st1, p.ws, sgo, 16); reload16_sc1(mg, p.ws, mgo, 0);
    vm0();
  }
#pragma unroll
  for (int r = 0; r < 4; ++r) PIN(xg[r]);
  SST(7);
  const f32x2 sa2 = {rlane(mg[0], 0), rlane(mg[2], 0)};
  const f32x2 ca2 = rnd2(f32x2{rlane(mg[0], 1), rlane(mg[2], 1)} + 1.0f);
  const f32x2 hs2 = {pow2_scale(22.0f * fabsf(ca2.x) + fabsf(sa2.x), 14), pow2_scale(22.0f * fabsf(ca2.y) + fabsf(sa2.y), 14)};
  const f32x2 ihs = {pow2_inv(hs2.x), pow2_inv(hs2.y)};
  f32x2 xf[4], hd[4];
  {
    const f32x2 cas = ca2 * hs2, sas = sa2 * hs2;
    const f32x2 dks = f32x2{bflo(dsk2), bfhi(dsk2)} * ihs;
    const u32 sw_[4] = {st0[0], st0[2], st1[0], st1[2]};
    f32x2 h[4];
#pragma unroll
    for (int r = 0; r < 4; ++r) {
      xf[r] = f32x2{bflo(xg[r]), bfhi(xg[r])};
      const float m = bflo(sw_[r]), rs = __builtin_amdgcn_rcpf(bfhi(sw_[r]));
      const f32x2 xn = rnd2(rnd2(xf[r] - m) * rs);
      h[r] = rnd2(rnd2(xn * cas) + sas);
    }
    const int hb = L_H + 480 + row0 * 2;
    *LDSP(u32x2, hb) = u32x2{pkh(h[0].x, h[1].x), pkh(h[2].x, h[3].x)};
    *LDSP(u32x2, hb + H_CH) = u32x2{pkh(h[0].y, h[1].y), pkh(h[2].y, h[3].y)};
#pragma unroll
    for (int r = 0; r < 4; ++r) hd[r] = rnd2(h[r] * dks);
  }
  vm0();
  SST(8);
  lds_barrier();
  f32x4 acc[8];
  u32x4 wfr[3];
  {
    lds_cptr bb = (lds_cptr)smem + L_H + ch * H_CH + 448 - 256 - 64 * pq + 32 * col + 16 * g;
    u32x4 B[2][8];
#pragma unroll
    for (int u = 0; u < 2; ++u)
#pragma unroll
      for (int k = 0; k < 8; ++k) B[u][k] = LDS4(bb + 512 * k + 256 * (1 - u));
    const int lbase = (8 * g + q4) * 128 + 8 * pp, phi = (q4 >> 1) | ((g & 1) << 1);
#pragma unroll
    for (int i = 0; i < 3; ++i) wfr[i] = frag128(smem, L_W + i * 8192, lbase, phi, w >> 2, w & 3);
    bool mgok = (lane & 3) < 2 || (mg[1] == tag && mg[3] == tag);
    if (!__all(mgok)) reload16_sc1(mg, p.ws, mgo, 0);
    SB();
#pragma unroll
    for (int u = 0; u < 2; ++u)
#pragma unroll
      for (int jj = 0; jj < 8; ++jj)
#pragma unroll
        for (int k = 0; k + jj < 8; ++k)
          acc[k + jj] = MFMA16H(A[u][k], B[u][jj], ((u == 0 && jj == 0) ? f32x4{0, 0, 0, 0} : acc[k + jj]));
  }
  SST(9);
#pragma unroll
  for (int c = 0; c < 8; ++c) *LDSP(f32x4, (w * 8 + c) * 1024 + lane * 16) = acc[c];
  for (unsigned spins = 0; spins < (1u << 20); ++spins) {
    vm0();
    PIN4(mg);
    if (__all((lane & 3) < 2 || (mg[1] == tag && mg[3] == tag))) break;
    reload16_sc1(mg, p.ws, mgo, 0);
  }
  if (b < 196) {
    const __amdgpu_buffer_rsrc_t wr = __builtin_amdgcn_make_buffer_rsrc(p.ws, 0, (int)WS_X1T, 0x00020000);
    const int ks = 2 * wtb + (w >> 2), ft = 4 * wta + (w & 3);
    st16_wt(wr, (int)WS_WF + ((ft * 14 + ks) * 2) * 1024 + lane * 16, wfr[0]);
    st16_wt(wr, (int)WS_WF + ((ft * 14 + ks) * 2 + 1) * 1024 + lane * 16, wfr[1]);
    const int kk = 2 * wta + (w >> 2), w7 = kk / 7;
    st16_wt(wr, (int)WS_WDF + (((wtb * 8 + w7) * 4 + (w & 3)) * 7 + (kk - 7 * w7)) * 1024 + lane * 16, wfr[2]);
  }
  const f32x2 ga2 = {rlane(mg[0], 2), rlane(mg[2], 2)};
  const f32x2 c12 = rnd2(f32x2{rlane(mg[0], 3), rlane(mg[2], 3)} + 1.0f);
  const f32x2 ysc = f32x2{pow2_inv(kt0), pow2_inv(kt1)} * ihs;
  lds_barrier();
  SST(10);
  {
    f32x4 y[2];
#pragma unroll
    for (int c = 0; c < 2; ++c) {
      const int rb = (c * 32 + w) * 1024 + lane * 16;
      y[c] = (*LDSP(f32x4, rb) + *LDSP(f32x4, rb + 8192)) + (*LDSP(f32x4, rb + 16384) + *LDSP(f32x4, rb + 24576));
    }
    const int lt = 2 * w + (col >> 3), lin = 16 * (col & 7) + 4 * g;
#pragma unroll
    for (int c = 0; c < 2; ++c) {
      u32x2 o1, oc;
#pragma unroll
      for (int h = 0; h < 2; ++h) {
        const f32x2 yy = rnd2(f32x2{y[c][2 * h], y[c][2 * h + 1]} * ysc[c]);
        const f32x2 t = rnd2(yy + f32x2{hd[2 * h][c], hd[2 * h + 1][c]});
        const f32x2 x1 = rnd2(f32x2{xf[2 * h][c], xf[2 * h + 1][c]} + rnd2(t * ga2[c]));
        o1[h] = pkbf(x1.x, x1.y);
        const f32x2 xc = x1 * c12[c];
        oc[h] = pkbf(xc.x, xc.y);
      }
      const size_t off = ((size_t)(lt * D + d0 + c) * 128 + lin) * 2;
      *(u32x2*)(p.ws + WS_X1T + off) = o1;
      *(u32x2*)(p.ws + WS_X1C + off) = oc;
    }
  }
  SSTE(11); SEND(0, 14);
}

constexpr int H_S = 0, H_CS = 1024, H_CNT = 2944, H_PART = 3072, H_G2 = 4096, H_WG = H_G2 + 57344, H_G1 = H_WG + 57344, H_END = H_G1 + 28672;
constexpr int AB_LDS = AB_MAIN_LDS > H_END ? AB_MAIN_LDS : H_END;

__device__ __forceinline__ void helper(const Params& p, int j, char* smem) {
  const int tid = threadIdx.x, lane = tid & 63;
  const int w = __builtin_amdgcn_readfirstlane(tid >> 6);
  const int col = lane & 15, g = lane >> 4, q4 = col >> 2, pp = lane & 3;
  const unsigned lds0 = (unsigned)(uintptr_t)smem;
  const u32 tag = cld32(p.ws + WS_EP) + TAGC;
  if (tid == 448) __hip_atomic_store((gu32*)(p.ws + WS_RD) + AB_MAIN + j, tag, __ATOMIC_RELAXED, __HIP_MEMORY_SCOPE_AGENT);
  if (j >= 28) {
    if (j == 28) ((u32*)(p.ws + WS_FLG))[tid] = 0;
    if (j == 29) *(u32x4*)(p.ws + WS_STG + tid * 16) = u32x4{0, 0, 0, 0};
    if (j == 30) {
      if (tid < 256) {
        for (unsigned spins = 0; spins < (1u << 20); ++spins) {
          if (__hip_atomic_load((gu32*)(p.ws + WS_RD) + tid, __ATOMIC_RELAXED, __HIP_MEMORY_SCOPE_AGENT) == tag) break;
          __builtin_amdgcn_s_sleep(4);
        }
      }
      __syncthreads();
      if (tid == 0) *(u32*)(p.ws + WS_EP) = tag;
    }
    return;
  }
  SDECL(12); SST(0);
  const int lbase = (8 * g + q4) * 128 + 8 * pp, phi = (q4 >> 1) | ((g & 1) << 1);
  const int dchunk = (lane & 7) ^ ((((lane >> 4) & 1) | ((w & 1) << 1)) << 1);
  const int ks0 = w < 6 ? 2 * w : w + 6, nks = w < 6 ? 2 : 1;
  u32 cw;
  asm volatile("global_load_short_d16_hi %0, %1, %2" : "=v"(cw) : "v"((u32)min(32 * ks0 + lane, D - 1) * 2), "s"(p.c) : "memory");
  {
    const u32 go = (u32)((lane >> 2) * (6 * D) + ((lane & 3) ^ (((lane >> 5) & 1) << 1)) * 8) * 2;
    for (int i = 0; i < 2 * nks; ++i)
      glds16v(p.w_mod + (size_t)(32 * ks0 + 16 * i) * (6 * D) + 32 * j, go, lds0 + H_G1 + (32 * ks0 + 16 * i) * 64);
  }
  {
    const u32 go = (u32)((lane >> 3) * (6 * D) + dchunk * 8) * 2;
#pragma unroll
    for (int i = 0; i < 7; ++i) glds16v(p.w_mod + (size_t)(8 * (w + 8 * i)) * (6 * D) + 2 * D + 64 * j, go, lds0 + H_G2 + (w + 8 * i) * 1024);
  }
  if (tid == 0) *LDSP(u32, H_CNT) = 0;
  lds_barrier();
  {
    vmw<7>();
    PIN(cw);
    const float c0 = bfhi(cw);
    if (lane < 32 * nks) *LDSP(u16, H_S + (32 * ks0 + lane) * 2) = (u16)pkbf(c0 / (1.0f + expf(-c0)), 0.f);
    f32x4 a0 = {0, 0, 0, 0}, a1 = {0, 0, 0, 0};
    const int lb64 = (8 * g + q4) * 64 + 8 * pp;
    for (int s = 0; s < nks; ++s) {
      const int ks = ks0 + s;
      const u32x4 sf = *LDSP(u32x4, H_S + (32 * ks + 8 * g) * 2);
      lds_cptr a = (lds_cptr)smem + H_G1 + lb64 + ks * 2048;
      const u32x2 t0 = tr_read(a + 32 * (g & 1)), t1 = tr_read(a + 32 * (g & 1) + 256), t2 = tr_read(a + 32 * ((g & 1) ^ 1)), t3 = tr_read(a + 32 * ((g & 1) ^ 1) + 256);
      a0 = MFMA16(sf, (u32x4{t0[0], t0[1], t1[0], t1[1]}), a0);
      a1 = MFMA16(sf, (u32x4{t2[0], t2[1], t3[0], t3[1]}), a1);
    }
    if (lane < 16) { *LDSP(float, H_PART + (w * 32 + lane) * 4) = a0[0]; *LDSP(float, H_PART + (w * 32 + 16 + lane) * 4) = a1[0]; }
    asm volatile("s_waitcnt lgkmcnt(0)" ::: "memory");
    u32 old = 0;
    if (lane == 0) old = __hip_atomic_fetch_add(LDSP(u32, H_CNT), 1u, __ATOMIC_RELAXED, __HIP_MEMORY_SCOPE_WORKGROUP);
    old = __builtin_amdgcn_readfirstlane(old);
    if (old == 7 && lane < 32) {
      float t = 0.f;
#pragma unroll
      for (int u = 0; u < 8; ++u) t += *LDSP(float, H_PART + (u * 32 + lane) * 4);
      st_gran(p.ws + WS_MG, 32 * j + lane, __float_as_uint(rnd(t)), tag);
    }
  }
  SST(1);
  const u32 vofff = (u32)((lane >> 3) * F + dchunk * 8) * 2;
#pragma unroll
  for (int i = 0; i < 7; ++i) glds16v(p.w_gate + (size_t)(8 * (w + 8 * i)) * F + 64 * j, vofff, lds0 + H_WG + (w + 8 * i) * 1024);
  vmw<7>();
  lds_barrier();
  SST(2);
  if (w < 4) {
    f32x4 a = {0, 0, 0, 0};
#pragma unroll
    for (int ks = 0; ks < 14; ++ks) a = MFMA16(*LDSP(u32x4, H_S + (32 * ks + 8 * g) * 2), frag128(smem, H_G2, lbase, phi, ks, w), a);
    if (lane < 16) {
      const float t = rnd(a[0]);
      const int mi = 2 * D + 64 * j + 16 * w + lane;
      st_gran(p.ws + WS_MG, mi, __float_as_uint(t), tag);
      ((float*)(p.ws + WS_MOD))[mi] = t;
    }
  }
  lds_barrier();
  SST(3);
#pragma unroll
  for (int i = 0; i < 7; ++i) glds16v(p.w_up + (size_t)(8 * (w + 8 * i)) * F + 64 * j, vofff, lds0 + H_G2 + (w + 8 * i) * 1024);
  if (tid < D) {
    unsigned long long a, c;
    for (unsigned spins = 0; spins < (1u << 20); ++spins) {
      a = __hip_atomic_load((gu64*)(p.ws + WS_MG) + 3 * D + tid, __ATOMIC_RELAXED, __HIP_MEMORY_SCOPE_AGENT);
      c = __hip_atomic_load((gu64*)(p.ws + WS_MG) + 4 * D + tid, __ATOMIC_RELAXED, __HIP_MEMORY_SCOPE_AGENT);
      if ((u32)(a >> 32) == tag && (u32)(c >> 32) == tag) break;
      __builtin_amdgcn_s_sleep(1);
    }
    *LDSP(u16, H_CS + tid * 2) = (u16)(pkbf(1.0f + __uint_as_float((u32)c), 0.f));
    *LDSP(u16, H_CS + 896 + tid * 2) = (u16)((u32)a >> 16);
  }
  vm0();
  lds_barrier();
  SST(4);
  {
    f32x4 a = {0, 0, 0, 0};
#pragma unroll
    for (int ks = 0; ks < 14; ++ks)
      a = MFMA16(*LDSP(u32x4, H_CS + (col & 1) * 896 + (32 * ks + 8 * g) * 2), frag128(smem, w < 4 ? H_WG : H_G2, lbase, phi, ks, w & 3), a);
    if (lane < 16) {
      float* uv = (float*)(p.ws + WS_UV) + (size_t)(w >> 2) * 2 * F + 64 * j + 16 * (w & 3) + lane;
      uv[0] = a[0]; uv[F] = a[1];
    }
  }
  SSTE(5); SEND(1, 12);
}

__global__ __launch_bounds__(512) void kAB(Params p) {
  extern __shared__ __attribute__((aligned(16))) char smem[];
  if (blockIdx.x < AB_MAIN) conv_main(p, blockIdx.x, smem);
  else helper(p, blockIdx.x - AB_MAIN, smem);
}

constexpr int C_XT = 448 * 256, C_UV = C_XT, C_RES = C_UV + 2048, CD_LDS = 131072;

__device__ __forceinline__ void stats_role(const Params& p, int j, char* smem) {
  const int tid = threadIdx.x, lane = tid & 63, w = tid >> 6;
  const int lc = tid & 7, dr = tid >> 3;
  const char* src = p.ws + WS_X1T + (size_t)(j >> 1) * D * 256 + (j & 1) * 128 + lc * 16;
  u32x4 v[7];
#pragma unroll
  for (int i = 0; i < 7; ++i) v[i] = *(const u32x4*)(src + (dr + 64 * i) * 256);
  float s[8] = {}, q[8] = {};
#pragma unroll
  for (int i = 0; i < 7; ++i)
#pragma unroll
    for (int k = 0; k < 4; ++k) {
      const float a = bflo(v[i][k]), b = bfhi(v[i][k]);
      s[2 * k] += a; q[2 * k] = fmaf(a, a, q[2 * k]); s[2 * k + 1] += b; q[2 * k + 1] = fmaf(b, b, q[2 * k + 1]);
    }
#pragma unroll
  for (int k = 0; k < 8; ++k) {
    s[k] += dppf<0x128>(s[k]); q[k] += dppf<0x128>(q[k]);
    s[k] = xrow16_sum(s[k]); q[k] = xrow16_sum(q[k]);
  }
  float* red = (float*)smem;
  if (lane < 8) {
    *(f32x4*)(red + (w * 2 + 0) * 64 + lane * 8) = f32x4{s[0], s[1], s[2], s[3]};
    *(f32x4*)(red + (w * 2 + 0) * 64 + lane * 8 + 4) = f32x4{s[4], s[5], s[6], s[7]};
    *(f32x4*)(red + (w * 2 + 1) * 64 + lane * 8) = f32x4{q[0], q[1], q[2], q[3]};
    *(f32x4*)(red + (w * 2 + 1) * 64 + lane * 8 + 4) = f32x4{q[4], q[5], q[6], q[7]};
  }
  __syncthreads();
  if (tid < 64) {
    float ss = 0.f, qq = 0.f;
#pragma unroll
    for (int u = 0; u < 8; ++u) { ss += red[(u * 2) * 64 + tid]; qq += red[(u * 2 + 1) * 64 + tid]; }
    const float mean = ss * (1.0f / D);
    const float var = fmaxf(qq * (1.0f / D) - mean * mean, 0.f);
    const float sd = sqrtf(bfr(bfr(var) + 1e-5f));
    const u32 word = pkbf(mean, sd);
    __hip_atomic_store((gu32*)(p.ws + WS_STG) + 64 * j + tid, word, __ATOMIC_RELAXED, __HIP_MEMORY_SCOPE_AGENT);
  }
}

constexpr int C_LA = 3;
constexpr int C_WD0 = 8;
struct COp { int kind, arg; };
constexpr COp c_opA(int ks, int g) {
  if (g < 2 && ks + C_LA <= 13) return {1, 2 * (ks + C_LA) + g};
  if (ks == 0 && (g == 2 || g == 4 || g == 6)) return {2, 4 + (g - 2) / 2};
  if (ks == 1 && (g == 2 || g == 4)) return {2, 7 + (g - 2) / 2};
  if (ks == 1 && g == 6) return {5, 0};
  if (ks >= 2 && ks <= 6 && g == 2) return {2, ks + 7};
  if (ks >= C_WD0 && (g == 4 || g == 6)) return {4, 2 * (ks - C_WD0) + (g - 4) / 2};
  return {0, 0};
}
constexpr COp c_opB(int ks, int g) { return ks == 7 ? COp{3, g} : COp{0, 0}; }
struct CSched { int wait_w[14], wait_x3, wait_x8, wait_st; };
constexpr CSched c_make_sched() {
  CSched r{};
  int seqW[28] = {}, seqX[14] = {}, seqS = 0, n = 1;
  for (int k = 0; k < 4; ++k) seqX[k] = n++;
  for (int k = 0; k < 2 * C_LA; ++k) seqW[k] = n++;
  for (int ks = 0; ks < 14; ++ks) {
    r.wait_w[ks] = n - 1 - seqW[2 * ks + 1];
    for (int g = 0; g < 8; ++g) {
      if (g == 4 && ks == 3) r.wait_x3 = n - 1 - seqX[8];
      if (g == 4 && ks == 8) r.wait_x8 = n - 1 - seqX[13];
      for (int u = 0; u < 2; ++u) {
        const COp o = u ? c_opB(ks, g) : c_opA(ks, g);
        if (o.kind == 1) seqW[o.arg] = n;
        if (o.kind == 2) seqX[o.arg] = n;
        if (o.kind == 3) seqS = n;
        if (o.kind) ++n;
      }
    }
  }
  r.wait_st = n - 1 - seqS;
  return r;
}
constexpr CSched C_SCHED = c_make_sched();
constexpr int C_WDM = 2 * (14 - C_WD0);
constexpr int C_WDE = 8;

__global__ __launch_bounds__(512) void kCD(Params p) {
  extern __shared__ __attribute__((aligned(16))) char smem[];
  if (blockIdx.x >= 224) { stats_role(p, blockIdx.x - 224, smem); return; }
  const int tid = threadIdx.x, lane = tid & 63;
  const int w = __builtin_amdgcn_readfirstlane(tid >> 6);
  const int xcd = blockIdx.x & 7, idx = blockIdx.x >> 3;
  const int Lt = 4 * (xcd >> 1) + idx / 7, Fi = 7 * (xcd & 1) + idx % 7;
  const int P = 2 * Lt + (xcd & 1), Q = idx % 7;
  const unsigned lds0 = (unsigned)(uintptr_t)smem;
  const int g = lane >> 4, q4 = (lane & 15) >> 2, pp = lane & 3;
  const u32 l16 = lane * 16;

  SDECL(10); SST(0);
  u32x4 wf[28];
  const int xrow = 4 * w + g;
  const u32 xoff = (u32)(xrow * 128 + ((lane & 15) ^ (((xrow & 3) << 2) | (((xrow >> 3) & 1) << 1))) * 8) * 2;
  const char* xsrc = p.ws + WS_X1C + (size_t)Lt * D * 256;
  const char* wsrc = p.ws + WS_WF + (size_t)(Fi * 8 + w) * 28 * 1024;
  const char* wdsrc = p.ws + WS_WDF + (size_t)(Q * 8 + w) * 28 * 1024;
  const char* stsrc = p.ws + WS_STG + Lt * 512;
  u32 stw[8];
  auto ld_x = [&](int ks) { glds16s(xsrc + ks * 8192, xoff, lds0 + ks * 8192 + w * 1024); };
  auto ld_w = [&](int n) { gload16(wf[n], wsrc + (n >> 2) * 4096, l16, (n & 3) * 1024); };
  auto ld_wd = [&](int n) { gload16(wf[n], wdsrc + (n >> 2) * 4096, l16, (n & 3) * 1024); };
  auto ld_st = [&](int i) {
    asm volatile("global_load_dword %0, %1, %2 sc1" : "=v"(stw[i]) : "v"((u32)(lane & 15) * 4), "s"(stsrc + ((i ^ w) << 6)) : "memory");
  };
  auto issue = [&](COp o) {
    if (o.kind == 1) ld_w(o.arg);
    if (o.kind == 2) ld_x(o.arg);
    if (o.kind == 3) ld_st(o.arg);
    if (o.kind == 4) ld_wd(o.arg);
    if (o.kind == 5)
      glds16s(p.ws + WS_X1T + ((size_t)(Lt * D + 64 * Q + 8 * w) * 128 + 64 * (P & 1)) * 2,
              (u32)((lane >> 3) * 128 + ((lane & 7) ^ ((((lane >> 4) & 1) | ((w & 1) << 1)) << 1)) * 8) * 2, lds0 + C_RES + w * 1024);
  };
  if (lane < 16) glds16s(p.ws + WS_UV + ((size_t)(w >> 1) * F + 128 * Fi + 64 * (w & 1)) * 4, l16, lds0 + C_UV + w * 256);
#pragma unroll
  for (int ks = 0; ks < 4; ++ks) ld_x(ks);
#pragma unroll
  for (int n = 0; n < 2 * C_LA; ++n) ld_w(n);
  SST(1);
  f32x4 ag[8], au[8];
  unsigned ta[8];
  {
    const unsigned base = (unsigned)(256 * (8 * g + q4) + 32 * ((q4 << 1) | (g & 1)) + 8 * pp);
#pragma unroll
    for (int i = 0; i < 8; ++i) ta[i] = base ^ (32u * (unsigned)(i ^ w));
  }
  u32x4 xb[2][4];
  auto rd_tile = [&](int ks, int i8, u32x4& dst) {
    lds_cptr a = (lds_cptr)smem + ta[i8] + ks * 8192;
    const u32x2 t0 = tr_read(a), t1 = tr_read(a + 1024);
    dst = u32x4{t0[0], t0[1], t1[0], t1[1]};
  };
  vmwait(2 * C_LA);
  asm volatile("s_barrier" ::: "memory");
#pragma unroll
  for (int i = 0; i < 4; ++i) rd_tile(0, i, xb[0][i]);
#pragma unroll
  for (int ks = 0; ks < 14; ++ks) {
    vmwait(C_SCHED.wait_w[ks]);
    PIN4(wf[2 * ks]); PIN4(wf[2 * ks + 1]);
#pragma unroll
    for (int gi = 0; gi < 8; ++gi) {
      const int h = gi >> 2, i = gi & 3;
      if (gi == 4 && (ks == 3 || ks == 8)) {
        vmwait(ks == 3 ? C_SCHED.wait_x3 : C_SCHED.wait_x8);
        asm volatile("s_barrier" ::: "memory");
      }
      if (ks == 0) { mfma_first(ag[gi], wf[0], xb[h][i]); mfma_first(au[gi], wf[1], xb[h][i]); }
      else { mfma_acc(ag[gi], wf[2 * ks], xb[h][i]); mfma_acc(au[gi], wf[2 * ks + 1], xb[h][i]); }
      if (h == 0) rd_tile(ks, 4 + i, xb[1][i]);
      else if (ks < 13) rd_tile(ks + 1, i, xb[0][i]);
      issue(c_opA(ks, gi)); issue(c_opB(ks, gi));
      __builtin_amdgcn_sched_barrier(0);
    }
  }
  mfma_fence();
  SST(2);
  u32x4 xres;
  {
    xres = frag128(smem, C_RES, (8 * g + q4) * 128 + 8 * pp, (q4 >> 1) | ((g & 1) << 1), w >> 2, w & 3);
  }
  vmwait(C_SCHED.wait_st);
  for (unsigned spins = 0; spins < (1u << 16); ++spins) {
    u32 mn = 0xffffffffu;
#pragma unroll
    for (int i = 0; i < 8; ++i) { asm volatile("" : "+v"(stw[i])); mn = min(mn, stw[i]); }
    if (__all(mn >= 0x10000u)) break;
#pragma unroll
    for (int i = 0; i < 8; ++i) ld_st(i);
    vmwait(0);
  }
  SST(3);
  {
    const float* uvl = (const float*)(smem + C_UV) + 16 * w + 4 * g;
    const f32x4 ug = *(const f32x4*)(uvl), uu = *(const f32x4*)(uvl + 256), vu = *(const f32x4*)(uvl + 384);
    const f32x4 vg = *(const f32x4*)(uvl + 128) * -1.4426950408889634f;
    const char* hh = p.ws + WS_HH + (size_t)(Lt * 8 * 56 + 4 * Fi + (w >> 1)) * 1024;
    const u32 hoff = (u32)((16 * (2 * (w & 1) + (g >> 1)) + (lane & 15)) * 16 + 8 * (g & 1));
#pragma unroll
    for (int i = 0; i < 8; ++i) {
      const int c = i ^ w;
      const float m = bflo(stw[i]), rs = __builtin_amdgcn_rcpf(bfhi(stw[i]));
      const float rsg = rs * -1.4426950408889634f;
      f32x4 gv = (ag[i] - m * ug) * rsg + vg;
      f32x4 uv = (au[i] - m * uu) * rs + vu;
      f32x4 h;
#pragma unroll
      for (int r = 0; r < 4; ++r) h[r] = __builtin_amdgcn_rcpf(1.0f + __builtin_amdgcn_exp2f(gv[r])) * uv[r];
      const u32x2 o = {pkbf(h[0], h[1]), pkbf(h[2], h[3])};
      asm volatile("global_store_dwordx2 %0, %1, %2 sc1" :: "v"(hoff), "v"(o), "s"(hh + (size_t)c * 56 * 1024) : "memory");
      if (2 * i < C_WDE) { ld_wd(C_WDM + 2 * i); ld_wd(C_WDM + 2 * i + 1); }
    }
  }
#pragma unroll
  for (int n = C_WDM + C_WDE; n < 28; ++n) ld_wd(n);
  SST(4);
  vmwait(28 - C_WDM - C_WDE);
  asm volatile("s_barrier" ::: "memory");
  SST(5);
  gu32* cnt = (gu32*)(p.ws + WS_FLG) + Lt * 32;
  if (tid == 0) __hip_atomic_fetch_add(cnt, 1u, __ATOMIC_RELAXED, __HIP_MEMORY_SCOPE_AGENT);
  const int dcol = 64 * Q + 32 * (w >> 2) + 8 * g;
  u32x4 gb0, gb1;
  gload16(gb0, p.ws + WS_MOD + 5 * D * 4, (u32)dcol * 4, 0);
  gload16(gb1, p.ws + WS_MOD + 5 * D * 4, (u32)dcol * 4, 16);
  if (w == 0) {
    for (unsigned spins = 0; spins < (1u << 20); ++spins) {
      if (__hip_atomic_load(cnt, __ATOMIC_RELAXED, __HIP_MEMORY_SCOPE_AGENT) >= 14u) break;
      __builtin_amdgcn_s_sleep(1);
    }
  }
  asm volatile("s_barrier" ::: "memory");
  SST(6);
  u32x4 hb[3][7];
  const char* hsrc = p.ws + WS_HH + (size_t)(4 * P * 56 + 7 * w) * 1024;
  auto ld_il = [&](int il, u32x4 (&dst)[7]) {
#pragma unroll
    for (int j = 0; j < 7; ++j) gload16_sc1(dst[j], hsrc + (size_t)il * 56 * 1024 + (j >> 2) * 4096, l16, (j & 3) * 1024);
  };
  ld_il(0, hb[0]); ld_il(1, hb[1]); ld_il(2, hb[2]);
#pragma unroll
  for (int n = 0; n < 28; ++n) PIN4(wf[n]);
#pragma unroll
  for (int il = 0; il < 4; ++il) {
    f32x4 acc[4];
#pragma unroll
    for (int j = 0; j < 7; ++j) {
      vmwait((il == 0 ? 14 : (il == 1 ? 14 : (il == 2 ? 7 : 0))) + 6 - j);
      PIN4(hb[il % 3][j]);
#pragma unroll
      for (int id = 0; id < 4; ++id) { if (j == 0) mfma_first(acc[id], wf[id * 7], hb[il % 3][0]); else mfma_acc(acc[id], wf[id * 7 + j], hb[il % 3][j]); }
      __builtin_amdgcn_sched_barrier(0);
    }
    if (il == 0) ld_il(3, hb[0]);
    mfma_fence();
#pragma unroll
    for (int id = 0; id < 4; ++id) *(f32x4*)(smem + ((w * 4 + il) * 4 + id) * 1024 + l16) = acc[id];
    __builtin_amdgcn_sched_barrier(0);
  }
  SST(7);
  __syncthreads();
  {
    f32x4 o0 = {0, 0, 0, 0}, o1 = {0, 0, 0, 0};
    const char* rb = smem + ((w & 3) * 4 + 2 * (w >> 2) + (g >> 1)) * 1024 + (32 * (g & 1) + (lane & 15)) * 16;
#pragma unroll
    for (int s = 0; s < 8; ++s) { o0 += *(const f32x4*)(rb + s * 16384); o1 += *(const f32x4*)(rb + s * 16384 + 256); }
    PIN4(gb0); PIN4(gb1);
    const f32x4 g0 = __builtin_bit_cast(f32x4, gb0), g1 = __builtin_bit_cast(f32x4, gb1);
    float r[8];
#pragma unroll
    for (int k = 0; k < 4; ++k) { r[k] = bfr(g0[k] * bfr(o0[k])); r[4 + k] = bfr(g1[k] * bfr(o1[k])); }
    u32x4 o;
#pragma unroll
    for (int k = 0; k < 4; ++k) o[k] = pkbf(bflo(xres[k]) + r[2 * k], bfhi(xres[k]) + r[2 * k + 1]);
    const int lrow = 64 * P + 16 * (w & 3) + (lane & 15);
    *(u32x4*)(p.out + (size_t)lrow * D + dcol) = o;
  }
  SSTE(8); SEND(2, 10);
}

extern "C" void kernel_launch(void* const* d_in, const int* in_sizes, int n_in, void* d_out, int out_size, void* d_ws,
                              size_t ws_size, hipStream_t stream) {
  Params p;
  p.u = (const u16*)d_in[0]; p.A_re = (const u16*)d_in[1]; p.A_im = (const u16*)d_in[2]; p.B_re = (const u16*)d_in[3];
  p.B_im = (const u16*)d_in[4]; p.C_re = (const u16*)d_in[5]; p.C_im = (const u16*)d_in[6]; p.log_dt = (const u16*)d_in[7];
  p.Dskip = (const u16*)d_in[8]; p.w_gate = (const u16*)d_in[10]; p.w_up = (const u16*)d_in[11]; p.w_down = (const u16*)d_in[12];
  p.c = (const u16*)d_in[13]; p.w_mod = (const u16*)d_in[14];
  p.ws = (char*)d_ws; p.out = (u16*)d_out;
  hipFuncSetAttribute((const void*)kAB, hipFuncAttributeMaxDynamicSharedMemorySize, AB_LDS);
  hipFuncSetAttribute((const void*)kCD, hipFuncAttributeMaxDynamicSharedMemorySize, CD_LDS);
  kAB<<<256, 512, AB_LDS, stream>>>(p);
  kCD<<<256, 512, CD_LDS, stream>>>(p);
}
```

```cpp
#include <hip/hip_runtime.h>
#include <hip/hip_bf16.h>
#include <stdint.h>

typedef unsigned short u16;
typedef unsigned int u32;
typedef __attribute__((ext_vector_type(8))) short s16x8;
typedef __attribute__((ext_vector_type(4))) short s16x4;
typedef __attribute__((ext_vector_type(8))) _Float16 f16x8;
typedef __attribute__((ext_vector_type(2))) _Float16 f16x2;
typedef __attribute__((ext_vector_type(2))) __bf16 bf16x2;
typedef __attribute__((ext_vector_type(8))) __bf16 bf16x8;
typedef __attribute__((ext_vector_type(2))) float f32x2;
typedef __attribute__((ext_vector_type(4))) float f32x4;
typedef __attribute__((ext_vector_type(16))) float f32x16;
typedef __attribute__((ext_vector_type(2))) unsigned u32x2;
typedef __attribute__((ext_vector_type(4))) unsigned u32x4;
typedef __attribute__((address_space(3))) char* lds_ptr;
typedef __attribute__((address_space(3))) const char* lds_cptr;

constexpr int L = 2048, D = 448, NS = 256, F = 1792;

constexpr size_t WS_EP = 0;
constexpr size_t WS_RD = 2048;
constexpr size_t WS_MG = 4096;
constexpr size_t WS_SG = 32768;
constexpr size_t WS_MOD = 49152;
constexpr size_t WS_FLG = 61440;
constexpr size_t WS_UV = 65536;
constexpr size_t WS_STG = 98304;
constexpr size_t WS_WDF = 131072;
constexpr size_t WS_WF = WS_WDF + (size_t)F * D * 2;
constexpr size_t WS_X1T = WS_WF + (size_t)2 * F * D * 2;
constexpr size_t WS_X1C = WS_X1T + (size_t)D * L * 2;
constexpr size_t WS_HH = WS_X1C + (size_t)D * L * 2;

struct Params {
  const u16* u; const u16* A_re; const u16* A_im; const u16* B_re; const u16* B_im; const u16* C_re; const u16* C_im;
  const u16* log_dt; const u16* Dskip; const u16* w_gate; const u16* w_up; const u16* w_down; const u16* c; const u16* w_mod;
  char* ws; u16* out;
};

#define SDECL(n) do {} while (0)
#define SST(i) do {} while (0)
#define SSTE(i) do {} while (0)
#define SEND(kid, n) do {} while (0)
__device__ __forceinline__ float bf2f(u16 v) { return __uint_as_float((u32)v << 16); }
__device__ __forceinline__ float bflo(u32 v) { return __uint_as_float(v << 16); }
__device__ __forceinline__ float bfhi(u32 v) { return __uint_as_float(v & 0xffff0000u); }
__device__ __forceinline__ u32 pkbf(float lo, float hi) {
  f32x2 v = {lo, hi}; bf16x2 b = __builtin_convertvector(v, bf16x2); return __builtin_bit_cast(u32, b);
}
__device__ __forceinline__ float bfr(float x) { return bflo(pkbf(x, x)); }
__device__ __forceinline__ u32 pkh(float lo, float hi) {
  f32x2 v = {lo, hi}; f16x2 b = __builtin_convertvector(v, f16x2); return __builtin_bit_cast(u32, b);
}
template <int CTRL> __device__ __forceinline__ float dppf(float x) {
  return __builtin_bit_cast(float, __builtin_amdgcn_mov_dpp(__builtin_bit_cast(int, x), CTRL, 0xf, 0xf, true));
}
__device__ __forceinline__ float xrow16_sum(float x) {
  auto s = __builtin_amdgcn_permlane16_swap(__float_as_uint(x), __float_as_uint(x), false, false);
  x = __uint_as_float(s[0]) + __uint_as_float(s[1]);
  auto t = __builtin_amdgcn_permlane32_swap(__float_as_uint(x), __float_as_uint(x), false, false);
  return __uint_as_float(t[0]) + __uint_as_float(t[1]);
}
__device__ __forceinline__ float xrow16_max(float x) {
  auto s = __builtin_amdgcn_permlane16_swap(__float_as_uint(x), __float_as_uint(x), false, false);
  x = fmaxf(__uint_as_float(s[0]), __uint_as_float(s[1]));
  auto t = __builtin_amdgcn_permlane32_swap(__float_as_uint(x), __float_as_uint(x), false, false);
  return fmaxf(__uint_as_float(t[0]), __uint_as_float(t[1]));
}
__device__ __forceinline__ float wave_sum(float x) {
  x += dppf<0xB1>(x); x += dppf<0x4E>(x); x += dppf<0x141>(x); x += dppf<0x128>(x);
  return xrow16_sum(x);
}
__device__ __forceinline__ float wave_max(float x) {
  x = fmaxf(x, dppf<0xB1>(x)); x = fmaxf(x, dppf<0x4E>(x)); x = fmaxf(x, dppf<0x141>(x)); x = fmaxf(x, dppf<0x128>(x));
  return xrow16_max(x);
}
__device__ __forceinline__ float dot2bf(u32 a, u32 b, float acc) {
  return __builtin_amdgcn_fdot2_f32_bf16(__builtin_bit_cast(bf16x2, a), __builtin_bit_cast(bf16x2, b), acc, false);
}
typedef short v4i16_t __attribute__((ext_vector_type(4)));
__device__ __forceinline__ u32x2 tr_read(lds_cptr p) {
  return __builtin_bit_cast(u32x2, __builtin_amdgcn_ds_read_tr16_b64_v4i16((__attribute__((address_space(3))) v4i16_t*)p));
}
__device__ __forceinline__ void glds16(const void* g, unsigned lds_base) {
  unsigned sv;
  asm volatile("s_mov_b32 %0, m0\n\ts_mov_b32 m0, %2\n\ts_nop 0\n\tglobal_load_lds_dwordx4 %1, off\n\ts_mov_b32 m0, %0"
               : "=&s"(sv) : "v"(g), "s"(lds_base) : "memory");
}
__device__ __forceinline__ float pow2_scale(float x, int target) {
  const int e = (int)(__float_as_uint(x) >> 23);
  return __uint_as_float((u32)(127 + min(max(target + 127 - e, -100), 100)) << 23);
}
#define WT_AUX 16
__device__ __forceinline__ void gload16(u32x4& dst, const void* sbase, u32 voff, int imm) {
  asm volatile("global_load_dwordx4 %0, %1, %2 offset:%3" : "=v"(dst) : "v"(voff), "s"(sbase), "i"(imm) : "memory");
}
__device__ __forceinline__ void gload16_sc1(u32x4& dst, const void* sbase, u32 voff, int imm) {
  asm volatile("global_load_dwordx4 %0, %1, %2 offset:%3 sc1" : "=v"(dst) : "v"(voff), "s"(sbase), "i"(imm) : "memory");
}
__device__ __forceinline__ void glds16s(const void* sbase, u32 voff, unsigned lds_base) {
  asm volatile("s_mov_b32 m0, %2\n\ts_nop 0\n\tglobal_load_lds_dwordx4 %0, %1" :: "v"(voff), "s"(sbase), "s"(lds_base) : "memory");
}
#define MFMA16(a, b, c) __builtin_amdgcn_mfma_f32_16x16x32_bf16(__builtin_bit_cast(bf16x8, a), __builtin_bit_cast(bf16x8, b), c, 0, 0, 0)
__device__ __forceinline__ void mfma_acc(f32x4& c, const u32x4& a, const u32x4& b) {
  asm volatile("v_mfma_f32_16x16x32_bf16 %0, %1, %2, %0" : "+v"(c) : "v"(a), "v"(b));
}
__device__ __forceinline__ void mfma_first(f32x4& c, const u32x4& a, const u32x4& b) {
  asm volatile("v_mfma_f32_16x16x32_bf16 %0, %1, %2, 0" : "=&v"(c) : "v"(a), "v"(b));
}
__device__ __forceinline__ void mfma_fence() { asm volatile("s_nop 7\n\ts_nop 7" ::: "memory"); }
#define VMWAIT(n) case n: asm volatile("s_waitcnt vmcnt(" #n ")" ::: "memory"); break
__device__ __forceinline__ void vmwait(int n) {
  switch (n) {
    VMWAIT(0); VMWAIT(1); VMWAIT(2); VMWAIT(3); VMWAIT(4); VMWAIT(5); VMWAIT(6); VMWAIT(7); VMWAIT(8); VMWAIT(9);
    VMWAIT(10); VMWAIT(11); VMWAIT(12); VMWAIT(13); VMWAIT(14); VMWAIT(15); VMWAIT(16); VMWAIT(17); VMWAIT(18); VMWAIT(19);
    VMWAIT(20); VMWAIT(21); VMWAIT(22); VMWAIT(23); VMWAIT(24); VMWAIT(25); VMWAIT(26); VMWAIT(27); VMWAIT(28); VMWAIT(29);
    VMWAIT(30); VMWAIT(31); VMWAIT(32); VMWAIT(33); VMWAIT(34); VMWAIT(35); VMWAIT(36); VMWAIT(37); VMWAIT(38); VMWAIT(39);
    default: asm volatile("s_waitcnt vmcnt(0)" ::: "memory"); break;
  }
}
#define PIN4(x) asm volatile("" : "+v"(x))

typedef __attribute__((address_space(1))) unsigned gu32;
typedef __attribute__((address_space(1))) unsigned long long gu64;
constexpr u32 TAGC = 0x9E3779B9u;
constexpr int AB_MAIN = 224;
constexpr int T_RS = 1056, T_P = 32 * T_RS, T_CH = 53 * T_RS;
constexpr int L_R = 65536, R_CP = 4160, R_CH = 4 * R_CP;
constexpr int L_W = 2 * T_CH;
constexpr int L_KT = L_W + 24576, KT_Z = 2048, KT_CH = 2064;
constexpr int L_H = L_KT + 2 * KT_CH, H_CH = 4608;
constexpr int L_SC = L_H + 2 * H_CH, AB_MAIN_LDS = L_SC + 64;

constexpr u32 kt_pos(int l) {
  if (l < 0) return KT_Z;
  int v = l;
  if (l >= 256) {
    const int s = l >= 1024 ? 3 : (l >= 512 ? 2 : 1), rem = l & ((1 << s) - 1), half = 1 << (s - 1);
    int q = l >> s;
    if (rem > half || (rem == half && (q & 1))) ++q;
    v = q << s;
  }
  int rr = 20, q = 0;
  if (v < 256) { rr = v & 7; q = v >> 3; }
  else if (v < 512) { rr = 8 + ((v >> 1) & 3); q = (v - 256) >> 3; }
  else if (v < 1024) { rr = 12 + 2 * ((v - 512) >> 8) + ((v >> 2) & 1); q = (v & 255) >> 3; }
  else if (v < 2048) { rr = 16 + ((v - 1024) >> 8); q = (v & 255) >> 3; }
  return (u32)(((rr >> 4) + 2 * (q >> 4)) * 512 + ((q & 15) + 16 * ((rr & 15) >> 2)) * 8 + 2 * (rr & 3));
}
struct GTab { u32 v[2][3][256][4]; };
constexpr GTab make_gtab() {
  GTab t{};
  for (int c = 0; c < 2; ++c)
    for (int i = 0; i < 256; ++i)
      for (int j = 0; j < 12; ++j) t.v[c][j >> 2][i][j & 3] = (u32)(L_KT + c * KT_CH) + kt_pos(2047 - 8 * i - j);
  return t;
}
static __device__ const GTab gtab = make_gtab();

__device__ __forceinline__ float rnd(float x) { return __uint_as_float(pkbf(0.f, x)); }
__device__ __forceinline__ f32x2 rnd2(f32x2 v) { return f32x2{rnd(v.x), rnd(v.y)}; }
__device__ __forceinline__ u32 pkh2(f32x2 v) { return __builtin_bit_cast(u32, __builtin_convertvector(v, f16x2)); }
struct cm { f32x2 v, n; };
__device__ __forceinline__ cm mkcm(f32x2 b) { return {b, f32x2{-b.y, b.x}}; }
__device__ __forceinline__ f32x2 cmul(f32x2 a, const cm& b) { return __builtin_elementwise_fma(a.yy, b.n, a.xx * b.v); }
__device__ __forceinline__ void cmul4(f32x2& o0, f32x2& o1, f32x2& o2, f32x2& o3, f32x2 a0, const cm& m0, f32x2 a1, const cm& m1, f32x2 a2, const cm& m2,
                                      f32x2 a3, const cm& m3) {
  f32x2 t0 = a0.xx * m0.v, t1 = a1.xx * m1.v, t2 = a2.xx * m2.v, t3 = a3.xx * m3.v;
  asm volatile("" : "+v"(t0), "+v"(t1), "+v"(t2), "+v"(t3));
  t0 = __builtin_elementwise_fma(a0.yy, m0.n, t0); t1 = __builtin_elementwise_fma(a1.yy, m1.n, t1);
  t2 = __builtin_elementwise_fma(a2.yy, m2.n, t2); t3 = __builtin_elementwise_fma(a3.yy, m3.n, t3);
  asm volatile("" : "+v"(t0), "+v"(t1), "+v"(t2), "+v"(t3));
  o0 = t0; o1 = t1; o2 = t2; o3 = t3;
}
__device__ __forceinline__ f32x2 cexpm(float zr, float zi, float m) {
  const float mag = __builtin_amdgcn_exp2f(zr * (m * 1.4426950408889634f));
  const float rev = __builtin_amdgcn_fractf(zi * (m * 0.15915494309189535f));
  return f32x2{mag * __builtin_amdgcn_cosf(rev), mag * __builtin_amdgcn_sinf(rev)};
}
__device__ __forceinline__ void st_gran(char* base, int idx, u32 val, u32 tag) {
  __hip_atomic_store((gu64*)base + idx, ((unsigned long long)tag << 32) | val, __ATOMIC_RELAXED, __HIP_MEMORY_SCOPE_AGENT);
}
template <int O0, int O1, int O2, int O3>
__device__ __forceinline__ void wr_rows4(u32 a, u32 b, u32 c, u32 d) {
  asm volatile("ds_write_addtid_b32 %0 offset:%4\n\tds_write_addtid_b32 %1 offset:%5\n\t"
               "ds_write_addtid_b32 %2 offset:%6\n\tds_write_addtid_b32 %3 offset:%7"
               :: "v"(a), "v"(b), "v"(c), "v"(d), "i"(O0), "i"(O1), "i"(O2), "i"(O3) : "memory");
}
template <int O0>
__device__ __forceinline__ void wr_rows1(u32 a) {
  asm volatile("ds_write_addtid_b32 %0 offset:%1" :: "v"(a), "i"(O0) : "memory");
}
__device__ __forceinline__ void table_q(f32x2 E8) {
  asm volatile("v_pk_mul_f32 v[232:233], v[226:227], v[226:227] op_sel_hi:[0,1]\n\tv_mov_b32 v240, 0x3c00\n\tv_cvt_pk_f16_f32 v241, v226, v227\n\tv_pk_fma_f32 v[228:229], v[226:227], v[226:227], v[232:233] op_sel:[1,1,0] op_sel_hi:[1,0,1] neg_lo:[0,1,0]\n\tds_write_addtid_b32 v240 offset:0\n\tds_write_addtid_b32 v241 offset:1056\n\tv_pk_mul_f32 v[232:233], v[228:229], v[226:227] op_sel_hi:[0,1]\n\tv_pk_mul_f32 v[234:235], v[228:229], v[228:229] op_sel_hi:[0,1]\n\tv_cvt_pk_f16_f32 v242, v228, v229\n\tv_pk_fma_f32 v[230:231], v[228:229], v[226:227], v[232:233] op_sel:[1,1,0] op_sel_hi:[1,0,1] neg_lo:[0,1,0]\n\tv_pk_fma_f32 v[244:245], v[228:229], v[228:229], v[234:235] op_sel:[1,1,0] op_sel_hi:[1,0,1] neg_lo:[0,1,0]\n\tds_write_addtid_b32 v242 offset:2112\n\tv_cvt_pk_f16_f32 v243, v230, v231\n\tv_pk_mul_f32 v[234:235], v[226:227], v[244:245] op_sel_hi:[0,1]\n\tv_pk_mul_f32 v[236:237], v[228:229], v[244:245] op_sel_hi:[0,1]\n\tv_pk_mul_f32 v[238:239], v[230:231], v[244:245] op_sel_hi:[0,1]\n\tds_write_addtid_b32 v243 offset:3168\n\tv_mov_b64 v[224:225], v[244:245]\n\tv_pk_fma_f32 v[226:227], v[226:227], v[244:245], v[234:235] op_sel:[1,1,0] op_sel_hi:[1,0,1] neg_lo:[0,1,0]\n\tv_pk_fma_f32 v[228:229], v[228:229], v[244:245], v[236:237] op_sel:[1,1,0] op_sel_hi:[1,0,1] neg_lo:[0,1,0]\n\tv_pk_fma_f32 v[230:231], v[230:231], v[244:245], v[238:239] op_sel:[1,1,0] op_sel_hi:[1,0,1] neg_lo:[0,1,0]\n\tv_cvt_pk_f16_f32 v240, v224, v225\n\tv_cvt_pk_f16_f32 v241, v226, v227\n\tv_cvt_pk_f16_f32 v242, v228, v229\n\tv_cvt_pk_f16_f32 v243, v230, v231\n\tv_pk_mul_f32 v[232:233], v[224:225], v[244:245] op_sel_hi:[0,1]\n\tv_pk_mul_f32 v[234:235], v[226:227], v[244:245] op_sel_hi:[0,1]\n\tv_pk_mul_f32 v[236:237], v[228:229], v[244:245] op_sel_hi:[0,1]\n\tv_pk_mul_f32 v[238:239], v[230:231], v[244:245] op_sel_hi:[0,1]\n\tds_write_addtid_b32 v240 offset:4224\n\tds_write_addtid_b32 v241 offset:5280\n\tds_write_addtid_b32 v242 offset:6336\n\tds_write_addtid_b32 v243 offset:7392\n\tv_pk_fma_f32 v[224:225], v[224:225], v[244:245], v[232:233] op_sel:[1,1,0] op_sel_hi:[1,0,1] neg_lo:[0,1,0]\n\tv_pk_fma_f32 v[226:227], v[226:227], v[244:245], v[234:235] op_sel:[1,1,0] op_sel_hi:[1,0,1] neg_lo:[0,1,0]\n\tv_pk_fma_f32 v[228:229], v[228:229], v[244:245], v[236:237] op_sel:[1,1,0] op_sel_hi:[1,0,1] neg_lo:[0,1,0]\n\tv_pk_fma_f32 v[230:231], v[230:231], v[244:245], v[238:239] op_sel:[1,1,0] op_sel_hi:[1,0,1] neg_lo:[0,1,0]\n\tv_cvt_pk_f16_f32 v240, v224, v225\n\tv_cvt_pk_f16_f32 v241, v226, v227\n\tv_cvt_pk_f16_f32 v242, v228, v229\n\tv_cvt_pk_f16_f32 v243, v230, v231\n\tv_pk_mul_f32 v[232:233], v[224:225], v[244:245] op_sel_hi:[0,1]\n\tv_pk_mul_f32 v[234:235], v[226:227], v[244:245] op_sel_hi:[0,1]\n\tv_pk_mul_f32 v[236:237], v[228:229], v[244:245] op_sel_hi:[0,1]\n\tv_pk_mul_f32 v[238:239], v[230:231], v[244:245] op_sel_hi:[0,1]\n\tds_write_addtid_b32 v240 offset:8448\n\tds_write_addtid_b32 v241 offset:9504\n\tds_write_addtid_b32 v242 offset:10560\n\tds_write_addtid_b32 v243 offset:11616\n\tv_pk_fma_f32 v[224:225], v[224:225], v[244:245], v[232:233] op_sel:[1,1,0] op_sel_hi:[1,0,1] neg_lo:[0,1,0]\n\tv_pk_fma_f32 v[226:227], v[226:227], v[244:245], v[234:235] op_sel:[1,1,0] op_sel_hi:[1,0,1] neg_lo:[0,1,0]\n\tv_pk_fma_f32 v[228:229], v[228:229], v[244:245], v[236:237] op_sel:[1,1,0] op_sel_hi:[1,0,1] neg_lo:[0,1,0]\n\tv_pk_fma_f32 v[230:231], v[230:231], v[244:245], v[238:239] op_sel:[1,1,0] op_sel_hi:[1,0,1] neg_lo:[0,1,0]\n\tv_cvt_pk_f16_f32 v240, v224, v225\n\tv_cvt_pk_f16_f32 v241, v226, v227\n\tv_cvt_pk_f16_f32 v242, v228, v229\n\tv_cvt_pk_f16_f32 v243, v230, v231\n\tv_pk_mul_f32 v[232:233], v[224:225], v[244:245] op_sel_hi:[0,1]\n\tv_pk_mul_f32 v[234:235], v[226:227], v[244:245] op_sel_hi:[0,1]\n\tv_pk_mul_f32 v[236:237], v[228:229], v[244:245] op_sel_hi:[0,1]\n\tv_pk_mul_f32 v[238:239], v[230:231], v[244:245] op_sel_hi:[0,1]\n\tds_write_addtid_b32 v240 offset:12672\n\tds_write_addtid_b32 v241 offset:13728\n\tds_write_addtid_b32 v242 offset:14784\n\tds_write_addtid_b32 v243 offset:15840\n\tv_pk_fma_f32 v[224:225], v[224:225], v[244:245], v[232:233] op_sel:[1,1,0] op_sel_hi:[1,0,1] neg_lo:[0,1,0]\n\tv_pk_fma_f32 v[226:227], v[226:227], v[244:245], v[234:235] op_sel:[1,1,0] op_sel_hi:[1,0,1] neg_lo:[0,1,0]\n\tv_pk_fma_f32 v[228:229], v[228:229], v[244:245], v[236:237] op_sel:[1,1,0] op_sel_hi:[1,0,1] neg_lo:[0,1,0]\n\tv_pk_fma_f32 v[230:231], v[230:231], v[244:245], v[238:239] op_sel:[1,1,0] op_sel_hi:[1,0,1] neg_lo:[0,1,0]\n\tv_cvt_pk_f16_f32 v240, v224, v225\n\tv_cvt_pk_f16_f32 v241, v226, v227\n\tv_cvt_pk_f16_f32 v242, v228, v229\n\tv_cvt_pk_f16_f32 v243, v230, v231\n\tv_pk_mul_f32 v[232:233], v[224:225], v[244:245] op_sel_hi:[0,1]\n\tv_pk_mul_f32 v[234:235], v[226:227], v[244:245] op_sel_hi:[0,1]\n\tv_pk_mul_f32 v[236:237], v[228:229], v[244:245] op_sel_hi:[0,1]\n\tv_pk_mul_f32 v[238:239], v[230:231], v[244:245] op_sel_hi:[0,1]\n\tds_write_addtid_b32 v240 offset:16896\n\tds_write_addtid_b32 v241 offset:17952\n\tds_write_addtid_b32 v242 offset:19008\n\tds_write_addtid_b32 v243 offset:20064\n\tv_pk_fma_f32 v[224:225], v[224:225], v[244:245], v[232:233] op_sel:[1,1,0] op_sel_hi:[1,0,1] neg_lo:[0,1,0]\n\tv_pk_fma_f32 v[226:227], v[226:227], v[244:245], v[234:235] op_sel:[1,1,0] op_sel_hi:[1,0,1] neg_lo:[0,1,0]\n\tv_pk_fma_f32 v[228:229], v[228:229], v[244:245], v[236:237] op_sel:[1,1,0] op_sel_hi:[1,0,1] neg_lo:[0,1,0]\n\tv_pk_fma_f32 v[230:231], v[230:231], v[244:245], v[238:239] op_sel:[1,1,0] op_sel_hi:[1,0,1] neg_lo:[0,1,0]\n\tv_cvt_pk_f16_f32 v240, v224, v225\n\tv_cvt_pk_f16_f32 v241, v226, v227\n\tv_cvt_pk_f16_f32 v242, v228, v229\n\tv_cvt_pk_f16_f32 v243, v230, v231\n\tv_pk_mul_f32 v[232:233], v[224:225], v[244:245] op_sel_hi:[0,1]\n\tv_pk_mul_f32 v[234:235], v[226:227], v[244:245] op_sel_hi:[0,1]\n\tv_pk_mul_f32 v[236:237], v[228:229], v[244:245] op_sel_hi:[0,1]\n\tv_pk_mul_f32 v[238:239], v[230:231], v[244:245] op_sel_hi:[0,1]\n\tds_write_addtid_b32 v240 offset:21120\n\tds_write_addtid_b32 v241 offset:22176\n\tds_write_addtid_b32 v242 offset:23232\n\tds_write_addtid_b32 v243 offset:24288\n\tv_pk_fma_f32 v[224:225], v[224:225], v[244:245], v[232:233] op_sel:[1,1,0] op_sel_hi:[1,0,1] neg_lo:[0,1,0]\n\tv_pk_fma_f32 v[226:227], v[226:227], v[244:245], v[234:235] op_sel:[1,1,0] op_sel_hi:[1,0,1] neg_lo:[0,1,0]\n\tv_pk_fma_f32 v[228:229], v[228:229], v[244:245], v[236:237] op_sel:[1,1,0] op_sel_hi:[1,0,1] neg_lo:[0,1,0]\n\tv_pk_fma_f32 v[230:231], v[230:231], v[244:245], v[238:239] op_sel:[1,1,0] op_sel_hi:[1,0,1] neg_lo:[0,1,0]\n\tv_cvt_pk_f16_f32 v240, v224, v225\n\tv_cvt_pk_f16_f32 v241, v226, v227\n\tv_cvt_pk_f16_f32 v242, v228, v229\n\tv_cvt_pk_f16_f32 v243, v230, v231\n\tv_pk_mul_f32 v[232:233], v[224:225], v[244:245] op_sel_hi:[0,1]\n\tv_pk_mul_f32 v[234:235], v[226:227], v[244:245] op_sel_hi:[0,1]\n\tv_pk_mul_f32 v[236:237], v[228:229], v[244:245] op_sel_hi:[0,1]\n\tv_pk_mul_f32 v[238:239], v[230:231], v[244:245] op_sel_hi:[0,1]\n\tds_write_addtid_b32 v240 offset:25344\n\tds_write_addtid_b32 v241 offset:26400\n\tds_write_addtid_b32 v242 offset:27456\n\tds_write_addtid_b32 v243 offset:28512\n\tv_pk_fma_f32 v[224:225], v[224:225], v[244:245], v[232:233] op_sel:[1,1,0] op_sel_hi:[1,0,1] neg_lo:[0,1,0]\n\tv_pk_fma_f32 v[226:227], v[226:227], v[244:245], v[234:235] op_sel:[1,1,0] op_sel_hi:[1,0,1] neg_lo:[0,1,0]\n\tv_pk_fma_f32 v[228:229], v[228:229], v[244:245], v[236:237] op_sel:[1,1,0] op_sel_hi:[1,0,1] neg_lo:[0,1,0]\n\tv_pk_fma_f32 v[230:231], v[230:231], v[244:245], v[238:239] op_sel:[1,1,0] op_sel_hi:[1,0,1] neg_lo:[0,1,0]\n\tv_cvt_pk_f16_f32 v240, v224, v225\n\tv_cvt_pk_f16_f32 v241, v226, v227\n\tv_cvt_pk_f16_f32 v242, v228, v229\n\tv_cvt_pk_f16_f32 v243, v230, v231\n\tds_write_addtid_b32 v240 offset:29568\n\tds_write_addtid_b32 v241 offset:30624\n\tds_write_addtid_b32 v242 offset:31680\n\tds_write_addtid_b32 v243 offset:32736"
               : "+{v[226:227]}"(E8) :: "v224", "v225", "v228", "v229", "v230", "v231", "v232", "v233", "v234", "v235", "v236", "v237", "v238", "v239", "v240", "v241", "v242", "v243", "v244", "v245", "memory");
}
__device__ __forceinline__ void table_p(f32x2 W, f32x2 E1, f32x2 E256) {
  asm volatile("v_pk_mul_f32 v[238:239], v[226:227], v[226:227] op_sel_hi:[0,1]\n\tv_pk_mul_f32 v[240:241], v[228:229], v[228:229] op_sel_hi:[0,1]\n\tv_pk_mul_f32 v[242:243], v[224:225], v[226:227] op_sel_hi:[0,1]\n\tv_pk_mul_f32 v[244:245], v[224:225], v[228:229] op_sel_hi:[0,1]\n\tv_cvt_pk_f16_f32 v200, v224, v225\n\tv_pk_fma_f32 v[208:209], v[226:227], v[226:227], v[238:239] op_sel:[1,1,0] op_sel_hi:[1,0,1] neg_lo:[0,1,0]\n\tv_pk_fma_f32 v[212:213], v[228:229], v[228:229], v[240:241] op_sel:[1,1,0] op_sel_hi:[1,0,1] neg_lo:[0,1,0]\n\tv_pk_fma_f32 v[216:217], v[224:225], v[226:227], v[242:243] op_sel:[1,1,0] op_sel_hi:[1,0,1] neg_lo:[0,1,0]\n\tv_pk_fma_f32 v[222:223], v[224:225], v[228:229], v[244:245] op_sel:[1,1,0] op_sel_hi:[1,0,1] neg_lo:[0,1,0]\n\tds_write_addtid_b32 v200 offset:33792\n\tv_pk_mul_f32 v[238:239], v[208:209], v[208:209] op_sel_hi:[0,1]\n\tv_pk_mul_f32 v[240:241], v[212:213], v[212:213] op_sel_hi:[0,1]\n\tv_pk_mul_f32 v[242:243], v[224:225], v[208:209] op_sel_hi:[0,1]\n\tv_pk_mul_f32 v[244:245], v[224:225], v[212:213] op_sel_hi:[0,1]\n\tv_cvt_pk_f16_f32 v201, v216, v217\n\tv_cvt_pk_f16_f32 v202, v222, v223\n\tv_pk_fma_f32 v[210:211], v[208:209], v[208:209], v[238:239] op_sel:[1,1,0] op_sel_hi:[1,0,1] neg_lo:[0,1,0]\n\tv_pk_fma_f32 v[214:215], v[212:213], v[212:213], v[240:241] op_sel:[1,1,0] op_sel_hi:[1,0,1] neg_lo:[0,1,0]\n\tv_pk_fma_f32 v[218:219], v[224:225], v[208:209], v[242:243] op_sel:[1,1,0] op_sel_hi:[1,0,1] neg_lo:[0,1,0]\n\tv_pk_fma_f32 v[232:233], v[224:225], v[212:213], v[244:245] op_sel:[1,1,0] op_sel_hi:[1,0,1] neg_lo:[0,1,0]\n\tds_write_addtid_b32 v201 offset:34848\n\tds_write_addtid_b32 v202 offset:42240\n\tv_pk_mul_f32 v[238:239], v[216:217], v[208:209] op_sel_hi:[0,1]\n\tv_pk_mul_f32 v[240:241], v[222:223], v[208:209] op_sel_hi:[0,1]\n\tv_pk_mul_f32 v[242:243], v[222:223], v[212:213] op_sel_hi:[0,1]\n\tv_pk_mul_f32 v[244:245], v[224:225], v[214:215] op_sel_hi:[0,1]\n\tv_cvt_pk_f16_f32 v203, v218, v219\n\tv_cvt_pk_f16_f32 v204, v232, v233\n\tv_pk_mul_f32 v[246:247], v[224:225], v[210:211] op_sel_hi:[0,1]\n\tv_pk_mul_f32 v[248:249], v[216:217], v[210:211] op_sel_hi:[0,1]\n\tv_pk_mul_f32 v[250:251], v[218:219], v[210:211] op_sel_hi:[0,1]\n\tv_pk_mul_f32 v[252:253], v[222:223], v[210:211] op_sel_hi:[0,1]\n\tv_pk_fma_f32 v[220:221], v[216:217], v[208:209], v[238:239] op_sel:[1,1,0] op_sel_hi:[1,0,1] neg_lo:[0,1,0]\n\tv_pk_fma_f32 v[230:231], v[222:223], v[208:209], v[240:241] op_sel:[1,1,0] op_sel_hi:[1,0,1] neg_lo:[0,1,0]\n\tv_pk_fma_f32 v[234:235], v[222:223], v[212:213], v[242:243] op_sel:[1,1,0] op_sel_hi:[1,0,1] neg_lo:[0,1,0]\n\tv_pk_fma_f32 v[236:237], v[224:225], v[214:215], v[244:245] op_sel:[1,1,0] op_sel_hi:[1,0,1] neg_lo:[0,1,0]\n\tds_write_addtid_b32 v203 offset:35904\n\tds_write_addtid_b32 v204 offset:46464\n\tv_pk_fma_f32 v[246:247], v[224:225], v[210:211], v[246:247] op_sel:[1,1,0] op_sel_hi:[1,0,1] neg_lo:[0,1,0]\n\tv_pk_fma_f32 v[248:249], v[216:217], v[210:211], v[248:249] op_sel:[1,1,0] op_sel_hi:[1,0,1] neg_lo:[0,1,0]\n\tv_pk_fma_f32 v[250:251], v[218:219], v[210:211], v[250:251] op_sel:[1,1,0] op_sel_hi:[1,0,1] neg_lo:[0,1,0]\n\tv_pk_fma_f32 v[252:253], v[222:223], v[210:211], v[252:253] op_sel:[1,1,0] op_sel_hi:[1,0,1] neg_lo:[0,1,0]\n\tv_pk_mul_f32 v[238:239], v[220:221], v[210:211] op_sel_hi:[0,1]\n\tv_pk_mul_f32 v[240:241], v[230:231], v[210:211] op_sel_hi:[0,1]\n\tv_pk_mul_f32 v[242:243], v[232:233], v[210:211] op_sel_hi:[0,1]\n\tv_pk_mul_f32 v[244:245], v[234:235], v[210:211] op_sel_hi:[0,1]\n\tv_cvt_pk_f16_f32 v200, v220, v221\n\tv_cvt_pk_f16_f32 v201, v230, v231\n\tv_cvt_pk_f16_f32 v202, v234, v235\n\tv_cvt_pk_f16_f32 v205, v236, v237\n\tv_cvt_pk_f16_f32 v206, v246, v247\n\tv_cvt_pk_f16_f32 v207, v248, v249\n\tv_cvt_pk_f16_f32 v203, v250, v251\n\tv_cvt_pk_f16_f32 v204, v252, v253\n\tv_pk_fma_f32 v[238:239], v[220:221], v[210:211], v[238:239] op_sel:[1,1,0] op_sel_hi:[1,0,1] neg_lo:[0,1,0]\n\tv_pk_fma_f32 v[240:241], v[230:231], v[210:211], v[240:241] op_sel:[1,1,0] op_sel_hi:[1,0,1] neg_lo:[0,1,0]\n\tv_pk_fma_f32 v[242:243], v[232:233], v[210:211], v[242:243] op_sel:[1,1,0] op_sel_hi:[1,0,1] neg_lo:[0,1,0]\n\tv_pk_fma_f32 v[244:245], v[234:235], v[210:211], v[244:245] op_sel:[1,1,0] op_sel_hi:[1,0,1] neg_lo:[0,1,0]\n\tds_write_addtid_b32 v200 offset:36960\n\tds_write_addtid_b32 v201 offset:43296\n\tds_write_addtid_b32 v202 offset:48576\n\tds_write_addtid_b32 v205 offset:50688\n\tv_pk_mul_f32 v[246:247], v[222:223], v[214:215] op_sel_hi:[0,1]\n\tv_pk_mul_f32 v[248:249], v[232:233], v[214:215] op_sel_hi:[0,1]\n\tv_pk_mul_f32 v[250:251], v[234:235], v[214:215] op_sel_hi:[0,1]\n\tv_pk_mul_f32 v[252:253], v[236:237], v[214:215] op_sel_hi:[0,1]\n\tds_write_addtid_b32 v206 offset:38016\n\tds_write_addtid_b32 v207 offset:39072\n\tds_write_addtid_b32 v203 offset:40128\n\tds_write_addtid_b32 v204 offset:44352\n\tv_cvt_pk_f16_f32 v200, v238, v239\n\tv_cvt_pk_f16_f32 v201, v240, v241\n\tv_cvt_pk_f16_f32 v202, v242, v243\n\tv_cvt_pk_f16_f32 v205, v244, v245\n\tv_pk_fma_f32 v[246:247], v[222:223], v[214:215], v[246:247] op_sel:[1,1,0] op_sel_hi:[1,0,1] neg_lo:[0,1,0]\n\tv_pk_fma_f32 v[248:249], v[232:233], v[214:215], v[248:249] op_sel:[1,1,0] op_sel_hi:[1,0,1] neg_lo:[0,1,0]\n\tv_pk_fma_f32 v[250:251], v[234:235], v[214:215], v[250:251] op_sel:[1,1,0] op_sel_hi:[1,0,1] neg_lo:[0,1,0]\n\tv_pk_fma_f32 v[252:253], v[236:237], v[214:215], v[252:253] op_sel:[1,1,0] op_sel_hi:[1,0,1] neg_lo:[0,1,0]\n\tds_write_addtid_b32 v200 offset:41184\n\tds_write_addtid_b32 v201 offset:45408\n\tds_write_addtid_b32 v202 offset:47520\n\tds_write_addtid_b32 v205 offset:49632\n\tv_cvt_pk_f16_f32 v206, v246, v247\n\tv_cvt_pk_f16_f32 v207, v248, v249\n\tv_cvt_pk_f16_f32 v203, v250, v251\n\tv_cvt_pk_f16_f32 v204, v252, v253\n\tds_write_addtid_b32 v206 offset:51744\n\tds_write_addtid_b32 v207 offset:52800\n\tds_write_addtid_b32 v203 offset:53856\n\tds_write_addtid_b32 v204 offset:54912"
               :: "{v[224:225]}"(W), "{v[226:227]}"(E1), "{v[228:229]}"(E256) : "v200", "v201", "v202", "v203", "v204", "v205", "v206", "v207", "v208", "v209", "v210", "v211", "v212", "v213", "v214", "v215", "v216", "v217", "v218", "v219", "v220", "v221", "v222", "v223", "v230", "v231", "v232", "v233", "v234", "v235", "v236", "v237", "v238", "v239", "v240", "v241", "v242", "v243", "v244", "v245", "v246", "v247", "v248", "v249", "v250", "v251", "v252", "v253", "memory");
}
__device__ __forceinline__ void lds_barrier() { asm volatile("s_waitcnt lgkmcnt(0)\n\ts_barrier" ::: "memory"); }
#define LDSP(T, off) ((__attribute__((address_space(3))) T*)((lds_ptr)smem + (off)))
#define MFMA16H(a, b, c) __builtin_amdgcn_mfma_f32_16x16x32_f16(__builtin_bit_cast(f16x8, a), __builtin_bit_cast(f16x8, b), c, 0, 0, 0)
__device__ __forceinline__ void glds16v(const void* sbase, u32 voff, unsigned lds_base) {
  asm volatile("s_mov_b32 m0, %2\n\ts_nop 0\n\tglobal_load_lds_dwordx4 %0, %1" :: "v"(voff), "s"(sbase), "s"(lds_base) : "memory");
}
__device__ __forceinline__ void reload16_sc1(u32x4& dst, const void* sbase, u32 voff, int imm) {
  asm volatile("global_load_dwordx4 %0, %1, %2 offset:%3 sc1" : "+v"(dst) : "v"(voff), "s"(sbase), "i"(imm) : "memory");
}
__device__ __forceinline__ void vm0() { asm volatile("s_waitcnt vmcnt(0)" ::: "memory"); }
template <int N> __device__ __forceinline__ void vmw() { asm volatile("s_waitcnt vmcnt(%0)" :: "i"(N) : "memory"); }
#define PIN(x) asm volatile("" : "+v"(x))
#define SB() __builtin_amdgcn_sched_barrier(0)
__device__ __forceinline__ u32x4 frag128(const char* smem, int base, int lbase, int phi, int ks, int t) {
  lds_cptr a = (lds_cptr)smem + base + lbase + 32 * (t ^ phi) + ks * 4096;
  const u32x2 t0 = tr_read(a), t1 = tr_read(a + 512);
  return u32x4{t0[0], t0[1], t1[0], t1[1]};
}
__device__ __forceinline__ void st16_wt(const __amdgpu_buffer_rsrc_t& r, int off, u32x4 v) { __builtin_amdgcn_raw_buffer_store_b128(v, r, off, 0, WT_AUX); }
__device__ __forceinline__ u32 cld32(const void* q) { return *(const __attribute__((address_space(4))) u32*)(uintptr_t)q; }
__device__ __forceinline__ float pow2_inv(float s) { return __uint_as_float(0x7F000000u - __float_as_uint(s)); }
__device__ __forceinline__ float rlane(u32 v, int l) { return __uint_as_float(__builtin_amdgcn_readlane(v, l)); }
#define LDS4(adr) (*(const __attribute__((address_space(3))) u32x4*)(adr))
#define LDS2(adr) (*(const __attribute__((address_space(3))) u32x2*)(adr))

__device__ __forceinline__ float sel4(const f32x4& a, int r) {
  const float lo = (r & 1) ? a[1] : a[0], hi = (r & 1) ? a[3] : a[2];
  return (r & 2) ? hi : lo;
}
__device__ __forceinline__ void ln_rows(const Params& p, u32x4 (&xr)[14], int row16, int lane, u32 tag) {
  const int col = lane & 15, g = lane >> 4;
  f32x4 ag = {0, 0, 0, 0}, as = {0, 0, 0, 0};
  const u32x4 ones = {0x3F803F80u, 0x3F803F80u, 0x3F803F80u, 0x3F803F80u};
#pragma unroll
  for (int s = 0; s < 14; ++s) { PIN4(xr[s]); ag = MFMA16(xr[s], xr[s], ag); as = MFMA16(xr[s], ones, as); }
  const float qq = sel4(ag, col), ss = sel4(as, col);
  const float mean = ss * (1.0f / D);
  const float var = fmaxf(qq * (1.0f / D) - mean * mean, 0.f);
  const float sd = __builtin_amdgcn_sqrtf(rnd(rnd(var) + 1e-5f));
  if (g == (col >> 2)) st_gran(p.ws + WS_SG, row16 + col, pkbf(mean, sd), tag);
}
template <int N> __device__ __forceinline__ void vmw_pin(u32x4& a) { asm volatile("s_waitcnt vmcnt(%1)" : "+v"(a) : "i"(N) : "memory"); }
template <int N> __device__ __forceinline__ void vmw_pin(u32x4& a, u32x4& b) { asm volatile("s_waitcnt vmcnt(%2)" : "+v"(a), "+v"(b) : "i"(N) : "memory"); }
#define LDS4U(adr) (*(const __attribute__((address_space(3))) u32x4 __attribute__((aligned(8)))*)(adr))
typedef u32x4 __attribute__((aligned(8))) u32x4_a8;

#define PINS(x) asm volatile("" : "+s"(x))
template <int OFF> __device__ __forceinline__ void lds_rd64(u32x2& dst, u32 adr) {
  asm volatile("ds_read_b64 %0, %1 offset:%2" : "=v"(dst) : "v"(adr), "i"(OFF) : "memory");
}
__device__ __forceinline__ void swap16(u32& a, u32& b) {
  const auto s = __builtin_amdgcn_permlane16_swap(a, b, false, false);
  a = s[0]; b = s[1];
}
#define X1_AUX 0

__device__ __forceinline__ void conv_main(const Params& p, int b, char* smem) {
  const int tid = threadIdx.x, lane = tid & 63;
  const int w = __builtin_amdgcn_readfirstlane(tid >> 6), ch = w >> 2, pq = w & 3;
  const int cp = (b & 7) * 28 + (b >> 3), d0 = 2 * cp, d = d0 + ch;
  const int col = lane & 15, g = lane >> 4;
  const unsigned lds0 = (unsigned)(uintptr_t)smem;
  SDECL(14); SST(0);
  const bool ln = w == 0 && b < 128;
  u32x4 xr[14];
  if (ln) {
    const char* xs = (const char*)(p.u + (size_t)(16 * b) * D);
    const u32 xo = (u32)(col * D + 8 * g) * 2;
#pragma unroll
    for (int s = 0; s < 14; ++s) gload16(xr[s], xs, xo, s * 64);
  }
  u32 pa[6];
  {
    const u32 po = (u32)(d * NS + pq * 64 + lane) * 2;
    const u16* const ps[6] = {p.A_re, p.A_im, p.B_re, p.B_im, p.C_re, p.C_im};
#pragma unroll
    for (int i = 0; i < 6; ++i) asm volatile("global_load_short_d16_hi %0, %1, %2" : "=v"(pa[i]) : "v"(po), "s"(ps[i]) : "memory");
  }
  const u32 ldt2 = cld32(p.log_dt + d0), dsk2 = cld32(p.Dskip + d0), tag = cld32(p.ws + WS_EP) + TAGC;
  asm volatile("s_barrier" ::: "memory");
  const int row0 = 256 * w + 16 * col + 4 * g;
  const u32 xo4 = (u32)row0 * (D * 2) + cp * 4;
  u32 xg[4];
  u32x4 gt[3];
#pragma unroll
  for (int i = 0; i < 3; ++i) gload16(gt[i], &gtab.v[ch][i][0][0], (u32)(tid & 255) * 16, 0);
  asm volatile("global_load_dword %0, %1, %2" : "=v"(xg[0]) : "v"(xo4), "s"(p.u) : "memory");
  asm volatile("global_load_dword %0, %1, %2 offset:896" : "=v"(xg[1]) : "v"(xo4), "s"(p.u) : "memory");
  const int wt = b < 196 ? b : b - 196, wta = wt / 7, wtb = wt - 7 * wta;
  const u16* wsrc0 = p.w_gate + (size_t)(64 * wtb + 8 * w) * F + 64 * wta;
  const u16* wsrc1 = p.w_up + (size_t)(64 * wtb + 8 * w) * F + 64 * wta;
  const u16* wsrc2 = p.w_down + (size_t)(64 * wta + 8 * w) * D + 64 * wtb;
  u32 vf, vd;
  {
    const int dchunk = (lane & 7) ^ ((((lane >> 4) & 1) | ((w & 1) << 1)) << 1);
    vf = (u32)((lane >> 3) * F + dchunk * 8) * 2; vd = (u32)((lane >> 3) * D + dchunk * 8) * 2;
  }
  PINS(wsrc0); PINS(wsrc1); PINS(wsrc2); PIN(vf); PIN(vd);
  if (tid < 60) *LDSP(u32x4, L_H + (tid >= 30 ? H_CH - 480 : 0) + tid * 16) = u32x4{0, 0, 0, 0};
  if (tid >= 128 && tid < 130) *LDSP(u16, L_KT + (tid & 1) * KT_CH + KT_Z) = 0;
  const int rt = pq & 1, ct = pq >> 1;
  u32 a_fq = lds0 + ch * T_CH + (16 * ct + col) * T_RS + 16 * g;
  u32 a_fp = lds0 + ch * T_CH + T_P + (16 * rt + col) * T_RS + 16 * g;
  u32 a_kt = lds0 + L_KT + ch * KT_CH + pq * 512 + lane * 8;
  u32 a_rw = lds0 + L_R + ch * R_CH + (tid & 255) * 16;
  u32 a_ar;
  {
    const int e0 = 2031 - col + 8 * g, c4 = e0 & 3;
    a_ar = lds0 + L_R + ch * R_CH + c4 * R_CP + 2 * (e0 - c4) - 64 * 63 - 64 * pq + 192;
  }
  u32 a_hw = lds0 + L_H + 480 + row0 * 2;
  u32 a_br = lds0 + L_H + ch * H_CH + 448 - 256 - 64 * pq + 32 * col + 16 * g;
  u32 a_pw = lds0 + w * 8192 + lane * 16, a_pr = lds0 + w * 1024 + lane * 16;
  u32 a_wf;
  {
    const int q4 = col >> 2, pp = lane & 3, phi = (q4 >> 1) | ((g & 1) << 1);
    a_wf = lds0 + L_W + (8 * g + q4) * 128 + 8 * pp + 32 * ((w & 3) ^ phi) + (w >> 2) * 4096;
  }
  u32 sgo = (u32)WS_SG + row0 * 8, mgo = (u32)WS_MG + (((lane & 3) == 3 ? 4 : (lane & 3)) * D + d0) * 8;
  u32 oo = (u32)(((2 * w + (col >> 3)) * D + d0 + (g & 1)) * 128 + 16 * (col & 7) + 8 * (g >> 1)) * 2 + (u32)WS_X1T;
  u32 wo0, wo1;
  {
    const int ks = 2 * wtb + (w >> 2), ft = 4 * wta + (w & 3), kk = 2 * wta + (w >> 2), w7 = kk / 7;
    wo0 = (u32)WS_WF + ((ft * 14 + ks) * 2) * 1024;
    wo1 = (u32)WS_WDF + (((wtb * 8 + w7) * 4 + (w & 3)) * 7 + (kk - 7 * w7)) * 1024;
  }
  PIN(a_fq); PIN(a_fp); PIN(a_kt); PIN(a_rw); PIN(a_ar); PIN(a_hw); PIN(a_br); PIN(a_pw); PIN(a_pr); PIN(a_wf); PIN(sgo); PIN(mgo); PIN(oo);
  PINS(wo0); PINS(wo1);
  float dt = rnd(expf(ch ? bfhi(ldt2) : bflo(ldt2)));
  PIN(dt);
  const u32 m0v = lds0 + ch * T_CH + pq * 256;
  SB();
  SST(1);
  if (ln) { vmw<11>(); ln_rows(p, xr, 16 * b, lane, tag); vmw<6>(); }
  else vmw<5>();
#pragma unroll
  for (int i = 0; i < 6; ++i) PIN(pa[i]);
  if (tid == 448) __hip_atomic_store((gu32*)(p.ws + WS_RD) + b, tag, __ATOMIC_RELAXED, __HIP_MEMORY_SCOPE_AGENT);
  SST(2);
  const float zr = dt * bfhi(pa[0]), zi = dt * bfhi(pa[1]);
  asm volatile("s_mov_b32 m0, %0" :: "s"(m0v) : "memory");
  {
    table_q(cexpm(zr, zi, 8.f));
  }
  asm volatile("global_load_dword %0, %1, %2 offset:1792" : "=v"(xg[2]) : "v"(xo4), "s"(p.u) : "memory");
  asm volatile("global_load_dword %0, %1, %2 offset:2688" : "=v"(xg[3]) : "v"(xo4), "s"(p.u) : "memory");
  SST(3);
  lds_barrier();
  u32x4 fq[16];
#pragma unroll
  for (int s = 0; s < 16; ++s) fq[s] = LDS4((lds_cptr)(uintptr_t)a_fq + 64 * s);
  SB();
  {
    const float br = dt * bfhi(pa[2]), bi = dt * bfhi(pa[3]), cr = bfhi(pa[4]), ci = bfhi(pa[5]);
    f32x2 W = {cr * br - ci * bi, -(cr * bi + ci * br)};
    const float S = wave_sum(fabsf(W.x) + fabsf(W.y));
    const f32x2 E1 = cexpm(zr, -zi, 1.f), E256 = cexpm(zr, -zi, 256.f);
    const float sw = pow2_scale(S, 14);
    W *= sw;
    if (lane == 0) *LDSP(f32x2, L_SC + w * 8) = f32x2{pow2_inv(sw), S};
    table_p(W, E1, E256);
  }
  SST(4);
  lds_barrier();
  float kt0, kt1;
  u32x4 mg, st0, st1;
  {
    u32x4 fp[16];
#pragma unroll
    for (int s = 0; s < 4; ++s)
#pragma unroll
      for (int c = 0; c < 4; ++c) fp[4 * c + s] = LDS4((lds_cptr)(uintptr_t)a_fp + 256 * c + 64 * s);
    const f32x4 s0 = *LDSP(f32x4, L_SC), s1 = *LDSP(f32x4, L_SC + 16), s2 = *LDSP(f32x4, L_SC + 32), s3 = *LDSP(f32x4, L_SC + 48);
    const f32x4 sa = *LDSP(f32x4, L_SC + ch * 32), sb = *LDSP(f32x4, L_SC + ch * 32 + 16);
    gload16_sc1(mg, p.ws, mgo, 0); gload16_sc1(st0, p.ws, sgo, 0); gload16_sc1(st1, p.ws, sgo, 16);
    glds16v(wsrc0, vf, lds0 + L_W + w * 1024);
    glds16v(wsrc1, vf, lds0 + L_W + 8192 + w * 1024);
    glds16v(wsrc2, vd, lds0 + L_W + 16384 + w * 1024);
    SB();
    f32x4 acc[4];
#pragma unroll
    for (int s = 0; s < 4; ++s)
#pragma unroll
      for (int c = 0; c < 4; ++c) acc[c] = MFMA16H(fp[4 * c + s], fq[4 * c + s], (s == 0 ? f32x4{0, 0, 0, 0} : acc[c]));
    kt0 = pow2_scale(s0[1] + s0[3] + s1[1] + s1[3], 14);
    kt1 = pow2_scale(s2[1] + s2[3] + s3[1] + s3[3], 14);
    const float kt = pow2_scale(sa[1] + sa[3] + sb[1] + sb[3], 14);
    const f32x4 kv = acc[0] * (sa[0] * kt) + acc[1] * (sa[2] * kt) + acc[2] * (sb[0] * kt) + acc[3] * (sb[2] * kt);
    *(__attribute__((address_space(3))) u32x2*)(uintptr_t)a_kt = u32x2{pkh(kv[0], kv[1]), pkh(kv[2], kv[3])};
  }
  asm volatile("s_waitcnt vmcnt(10)" : "+v"(gt[0]), "+v"(gt[1]), "+v"(gt[2]) :: "memory");
  SST(5);
  lds_barrier();
  {
    u32 v[11];
#pragma unroll
    for (int j = 0; j < 11; ++j) v[j] = *(const __attribute__((address_space(3))) u16*)(uintptr_t)(gt[j >> 2][j & 3] + lds0);
    u32 e[5], o[5];
#pragma unroll
    for (int j = 0; j < 5; ++j) { e[j] = v[2 * j] | (v[2 * j + 1] << 16); o[j] = v[2 * j + 1] | (v[2 * j + 2] << 16); }
    lds_ptr rb = (lds_ptr)(uintptr_t)a_rw;
    *(__attribute__((address_space(3))) u32x4*)(rb) = u32x4{e[0], e[1], e[2], e[3]};
    *(__attribute__((address_space(3))) u32x4*)(rb + R_CP) = u32x4{o[0], o[1], o[2], o[3]};
    *(__attribute__((address_space(3))) u32x4*)(rb + 2 * R_CP) = u32x4{e[1], e[2], e[3], e[4]};
    *(__attribute__((address_space(3))) u32x4*)(rb + 3 * R_CP) = u32x4{o[1], o[2], o[3], o[4]};
    if ((tid & 255) < 8) *LDSP(u32x4, L_R + ch * R_CH + ((tid & 7) >> 1) * R_CP + 4096 + (tid & 1) * 16) = u32x4{0, 0, 0, 0};
  }
  SST(6);
  lds_barrier();
  u32x2 Al[2][8], Ah[2][8];
  {
    u32 a2 = a_ar + 2048;
    PIN(a2);
#define RDA(u, k) do { if (k < 4) { lds_rd64<64 * (28 - 4 * u - 8 * k) + 0 * (k < 4)>(Al[u][k], a2); lds_rd64<64 * (28 - 4 * u - 8 * k) + 8>(Ah[u][k], a2); } } while (0)
#define RDB(u, k) do { lds_rd64<64 * (60 - 4 * u - 8 * k)>(Al[u][k], a_ar); lds_rd64<64 * (60 - 4 * u - 8 * k) + 8>(Ah[u][k], a_ar); } while (0)
    RDA(0, 0); RDA(0, 1); RDA(0, 2); RDA(0, 3); RDB(0, 4); RDB(0, 5); RDB(0, 6); RDB(0, 7);
    RDA(1, 0); RDA(1, 1); RDA(1, 2); RDA(1, 3); RDB(1, 4); RDB(1, 5); RDB(1, 6); RDB(1, 7);
#undef RDA
#undef RDB
  }
  asm volatile("s_waitcnt vmcnt(3)" : "+v"(mg), "+v"(st0), "+v"(st1) :: "memory");
#define NE(x) __builtin_amdgcn_ballot_w64((x) != tag)
  unsigned long long bm = NE(mg[1]) | NE(mg[3]);
  if ((NE(st0[1]) | NE(st0[3]) | NE(st1[1]) | NE(st1[3]) | (bm & 3)) != 0) {
#pragma unroll 1
    for (unsigned spins = 0; spins < (1u << 20); ++spins) {
      __builtin_amdgcn_s_sleep(1);
      asm volatile("global_load_dwordx4 %0, %3, %5 sc1\n\tglobal_load_dwordx4 %1, %3, %5 offset:16 sc1\n\tglobal_load_dwordx4 %2, %4, %5 sc1\n\ts_waitcnt vmcnt(0)"
                   : "+v"(st0), "+v"(st1), "+v"(mg) : "v"(sgo), "v"(mgo), "s"(p.ws) : "memory");
      bm = NE(mg[1]) | NE(mg[3]);
      if ((NE(st0[1]) | NE(st0[3]) | NE(st1[1]) | NE(st1[3]) | (bm & 3)) == 0) break;
    }
  }
#pragma unroll
  for (int r = 0; r < 4; ++r) PIN(xg[r]);
  SST(7);
  f32x2 xf[2][2], h[2][2];
  float ihs[2], dks[2];
  {
    const u32 sw_[4] = {st0[0], st0[2], st1[0], st1[2]};
    f32x2 m2[2], r2[2];
#pragma unroll
    for (int hh = 0; hh < 2; ++hh) {
      m2[hh] = f32x2{bflo(sw_[2 * hh]), bflo(sw_[2 * hh + 1])};
      r2[hh] = f32x2{__builtin_amdgcn_rcpf(bfhi(sw_[2 * hh])), __builtin_amdgcn_rcpf(bfhi(sw_[2 * hh + 1]))};
      xf[0][hh] = f32x2{bflo(xg[2 * hh]), bflo(xg[2 * hh + 1])};
      xf[1][hh] = f32x2{bfhi(xg[2 * hh]), bfhi(xg[2 * hh + 1])};
    }
#pragma unroll
    for (int c = 0; c < 2; ++c) {
      const float sa = rlane(mg[2 * c], 0), ca = rnd(rlane(mg[2 * c], 1) + 1.0f);
      const float hs = pow2_scale(22.0f * fabsf(ca) + fabsf(sa), 14);
      ihs[c] = pow2_inv(hs);
      dks[c] = (c ? bfhi(dsk2) : bflo(dsk2)) * ihs[c];
      const float cas = ca * hs, sas = sa * hs;
#pragma unroll
      for (int hh = 0; hh < 2; ++hh) {
        const f32x2 xn = rnd2(rnd2(xf[c][hh] - m2[hh]) * r2[hh]);
        h[c][hh] = rnd2(rnd2(xn * cas) + sas);
      }
      *(__attribute__((address_space(3))) u32x2*)(uintptr_t)(a_hw + c * H_CH) = u32x2{pkh2(h[c][0]), pkh2(h[c][1])};
    }
  }
  const bool again = (bm & 12) != 0;
  vm0();
  if (again) reload16_sc1(mg, p.ws, mgo, 0);
  SST(8);
  lds_barrier();
  f32x2 hd[2][2];
  {
    u32x4 B[2][8], A[2][8];
#pragma unroll
    for (int u = 0; u < 2; ++u)
#pragma unroll
      for (int k = 0; k < 8; ++k) B[u][k] = LDS4((lds_cptr)(uintptr_t)a_br + 512 * k + 256 * (1 - u));
    u32x4 wfr[3];
#pragma unroll
    for (int i = 0; i < 3; ++i) {
      lds_cptr a = (lds_cptr)(uintptr_t)a_wf + i * 8192;
      const u32x2 t0 = tr_read(a), t1 = tr_read(a + 512);
      wfr[i] = u32x4{t0[0], t0[1], t1[0], t1[1]};
    }
#pragma unroll
    for (int u = 0; u < 2; ++u)
#pragma unroll
      for (int k = 0; k < 8; ++k) { PIN(Al[u][k]); PIN(Ah[u][k]); A[u][k] = u32x4{Al[u][k][0], Al[u][k][1], Ah[u][k][0], Ah[u][k][1]}; }
    SB();
#pragma unroll
    for (int c = 0; c < 2; ++c)
#pragma unroll
      for (int hh = 0; hh < 2; ++hh) { hd[c][hh] = rnd2(h[c][hh] * dks[c]); PIN(hd[c][hh]); }
    f32x4 acc[8];
#pragma unroll
    for (int jj = 0; jj < 8; ++jj)
#pragma unroll
      for (int k = 0; k + jj < 8; ++k) acc[k + jj] = MFMA16H(A[0][k], B[0][jj], (jj == 0 ? f32x4{0, 0, 0, 0} : acc[k + jj]));
    SB();
    if (b < 196) {
      const __amdgpu_buffer_rsrc_t wr = __builtin_amdgcn_make_buffer_rsrc(p.ws, 0, (int)WS_X1T, 0x00020000);
      __builtin_amdgcn_raw_buffer_store_b128(wfr[0], wr, lane * 16, wo0, WT_AUX);
      __builtin_amdgcn_raw_buffer_store_b128(wfr[1], wr, lane * 16 + 1024, wo0, WT_AUX);
      __builtin_amdgcn_raw_buffer_store_b128(wfr[2], wr, lane * 16, wo1, WT_AUX);
    }
    SB();
#pragma unroll
    for (int jj = 0; jj < 8; ++jj) {
#pragma unroll
      for (int k = 0; k + jj < 8; ++k) acc[k + jj] = MFMA16H(A[1][k], B[1][jj], acc[k + jj]);
      if (jj > 0) *(__attribute__((address_space(3))) f32x4*)(uintptr_t)(a_pw + (jj - 1) * 1024) = acc[jj - 1];
      SB();
    }
    *(__attribute__((address_space(3))) f32x4*)(uintptr_t)(a_pw + 7 * 1024) = acc[7];
  }
  SST(9);
  lds_barrier();
  SST(10);
  {
    f32x4 y[2];
#pragma unroll
    for (int c = 0; c < 2; ++c) {
      lds_cptr rb = (lds_cptr)(uintptr_t)a_pr + c * 32768;
      typedef const __attribute__((address_space(3))) f32x4* lf4;
      y[c] = (*(lf4)rb + *(lf4)(rb + 8192)) + (*(lf4)(rb + 16384) + *(lf4)(rb + 24576));
    }
    if (again) {
      if (b < 196) asm volatile("s_waitcnt vmcnt(3)" : "+v"(mg) :: "memory"); else asm volatile("s_waitcnt vmcnt(0)" : "+v"(mg) :: "memory");
#pragma unroll 1
      for (unsigned spins = 0; spins < (1u << 20); ++spins) {
        if (((NE(mg[1]) | NE(mg[3])) & 12) == 0) break;
        __builtin_amdgcn_s_sleep(1);
        asm volatile("global_load_dwordx4 %0, %1, %2 sc1\n\ts_waitcnt vmcnt(0)" : "+v"(mg) : "v"(mgo), "s"(p.ws) : "memory");
      }
    }
#undef NE
    u32 o1[2][2], oc[2][2];
#pragma unroll
    for (int c = 0; c < 2; ++c) {
      const float ga = rlane(mg[2 * c], 2), c1 = rnd(rlane(mg[2 * c], 3) + 1.0f);
      const float ysc = pow2_inv(c ? kt1 : kt0) * ihs[c];
#pragma unroll
      for (int hh = 0; hh < 2; ++hh) {
        const f32x2 yy = rnd2(f32x2{y[c][2 * hh], y[c][2 * hh + 1]} * ysc);
        const f32x2 t = rnd2(yy + hd[c][hh]);
        const f32x2 xs = xf[c][hh] + rnd2(t * ga);
        o1[c][hh] = pkbf(xs.x, xs.y);
        const f32x2 xc = f32x2{bflo(o1[c][hh]), bfhi(o1[c][hh])} * c1;
        oc[c][hh] = pkbf(xc.x, xc.y);
      }
    }
    swap16(o1[0][0], o1[1][0]); swap16(o1[0][1], o1[1][1]); swap16(oc[0][0], oc[1][0]); swap16(oc[0][1], oc[1][1]);
    const __amdgpu_buffer_rsrc_t xr_ = __builtin_amdgcn_make_buffer_rsrc(p.ws, 0, (int)WS_HH, 0x00020000);
    __builtin_amdgcn_raw_buffer_store_b128(u32x4{o1[0][0], o1[0][1], o1[1][0], o1[1][1]}, xr_, oo, 0, X1_AUX);
    __builtin_amdgcn_raw_buffer_store_b128(u32x4{oc[0][0], oc[0][1], oc[1][0], oc[1][1]}, xr_, oo, (int)(WS_X1C - WS_X1T), X1_AUX);
  }
  SSTE(11); SEND(0, 14);
}

constexpr int H_S = 0, H_CS = 1024, H_CNT = 2944, H_PART = 3072, H_G2 = 4096, H_WG = H_G2 + 57344, H_G1 = H_WG + 57344, H_END = H_G1 + 28672;
constexpr int AB_LDS = AB_MAIN_LDS > H_END ? AB_MAIN_LDS : H_END;

__device__ __forceinline__ void helper(const Params& p, int j, char* smem) {
  const int tid = threadIdx.x, lane = tid & 63;
  const int w = __builtin_amdgcn_readfirstlane(tid >> 6);
  const int col = lane & 15, g = lane >> 4, q4 = col >> 2, pp = lane & 3;
  const unsigned lds0 = (unsigned)(uintptr_t)smem;
  const u32 tag = cld32(p.ws + WS_EP) + TAGC;
  if (tid == 448) __hip_atomic_store((gu32*)(p.ws + WS_RD) + AB_MAIN + j, tag, __ATOMIC_RELAXED, __HIP_MEMORY_SCOPE_AGENT);
  if (j >= 28) {
    if (j == 28) ((u32*)(p.ws + WS_FLG))[tid] = 0;
    if (j == 29) *(u32x4*)(p.ws + WS_STG + tid * 16) = u32x4{0, 0, 0, 0};
    if (j == 30) {
      if (tid < 256) {
        for (unsigned spins = 0; spins < (1u << 20); ++spins) {
          if (__hip_atomic_load((gu32*)(p.ws + WS_RD) + tid, __ATOMIC_RELAXED, __HIP_MEMORY_SCOPE_AGENT) == tag) break;
          __builtin_amdgcn_s_sleep(4);
        }
      }
      __syncthreads();
      if (tid == 0) *(u32*)(p.ws + WS_EP) = tag;
    }
    return;
  }
  SDECL(12); SST(0);
  const int lbase = (8 * g + q4) * 128 + 8 * pp, phi = (q4 >> 1) | ((g & 1) << 1);
  const int dchunk = (lane & 7) ^ ((((lane >> 4) & 1) | ((w & 1) << 1)) << 1);
  const int ks0 = w < 6 ? 2 * w : w + 6, nks = w < 6 ? 2 : 1;
  u32 cw;
  asm volatile("global_load_short_d16_hi %0, %1, %2" : "=v"(cw) : "v"((u32)min(32 * ks0 + lane, D - 1) * 2), "s"(p.c) : "memory");
  {
    const u32 go = (u32)((lane >> 2) * (6 * D) + ((lane & 3) ^ (((lane >> 5) & 1) << 1)) * 8) * 2;
    for (int i = 0; i < 2 * nks; ++i)
      glds16v(p.w_mod + (size_t)(32 * ks0 + 16 * i) * (6 * D) + 32 * j, go, lds0 + H_G1 + (32 * ks0 + 16 * i) * 64);
  }
  {
    const u32 go = (u32)((lane >> 3) * (6 * D) + dchunk * 8) * 2;
#pragma unroll
    for (int i = 0; i < 7; ++i) glds16v(p.w_mod + (size_t)(8 * (w + 8 * i)) * (6 * D) + 2 * D + 64 * j, go, lds0 + H_G2 + (w + 8 * i) * 1024);
  }
  if (tid == 0) *LDSP(u32, H_CNT) = 0;
  lds_barrier();
  {
    vmw<7>();
    PIN(cw);
    const float c0 = bfhi(cw);
    if (lane < 32 * nks) *LDSP(u16, H_S + (32 * ks0 + lane) * 2) = (u16)pkbf(c0 / (1.0f + expf(-c0)), 0.f);
    f32x4 a0 = {0, 0, 0, 0}, a1 = {0, 0, 0, 0};
    const int lb64 = (8 * g + q4) * 64 + 8 * pp;
    for (int s = 0; s < nks; ++s) {
      const int ks = ks0 + s;
      const u32x4 sf = *LDSP(u32x4, H_S + (32 * ks + 8 * g) * 2);
      lds_cptr a = (lds_cptr)smem + H_G1 + lb64 + ks * 2048;
      const u32x2 t0 = tr_read(a + 32 * (g & 1)), t1 = tr_read(a + 32 * (g & 1) + 256), t2 = tr_read(a + 32 * ((g & 1) ^ 1)), t3 = tr_read(a + 32 * ((g & 1) ^ 1) + 256);
      a0 = MFMA16(sf, (u32x4{t0[0], t0[1], t1[0], t1[1]}), a0);
      a1 = MFMA16(sf, (u32x4{t2[0], t2[1], t3[0], t3[1]}), a1);
    }
    if (lane < 16) { *LDSP(float, H_PART + (w * 32 + lane) * 4) = a0[0]; *LDSP(float, H_PART + (w * 32 + 16 + lane) * 4) = a1[0]; }
    asm volatile("s_waitcnt lgkmcnt(0)" ::: "memory");
    u32 old = 0;
    if (lane == 0) old = __hip_atomic_fetch_add(LDSP(u32, H_CNT), 1u, __ATOMIC_RELAXED, __HIP_MEMORY_SCOPE_WORKGROUP);
    old = __builtin_amdgcn_readfirstlane(old);
    if (old == 7 && lane < 32) {
      float t = 0.f;
#pragma unroll
      for (int u = 0; u < 8; ++u) t += *LDSP(float, H_PART + (u * 32 + lane) * 4);
      st_gran(p.ws + WS_MG, 32 * j + lane, __float_as_uint(rnd(t)), tag);
    }
  }
  SST(1);
  const u32 vofff = (u32)((lane >> 3) * F + dchunk * 8) * 2;
#pragma unroll
  for (int i = 0; i < 7; ++i) glds16v(p.w_gate + (size_t)(8 * (w + 8 * i)) * F + 64 * j, vofff, lds0 + H_WG + (w + 8 * i) * 1024);
  vmw<7>();
  lds_barrier();
  SST(2);
  if (w < 4) {
    f32x4 a = {0, 0, 0, 0};
#pragma unroll
    for (int ks = 0; ks < 14; ++ks) a = MFMA16(*LDSP(u32x4, H_S + (32 * ks + 8 * g) * 2), frag128(smem, H_G2, lbase, phi, ks, w), a);
    if (lane < 16) {
      const float t = rnd(a[0]);
      const int mi = 2 * D + 64 * j + 16 * w + lane;
      st_gran(p.ws + WS_MG, mi, __float_as_uint(t), tag);
      ((float*)(p.ws + WS_MOD))[mi] = t;
    }
  }
  lds_barrier();
  SST(3);
#pragma unroll
  for (int i = 0; i < 7; ++i) glds16v(p.w_up + (size_t)(8 * (w + 8 * i)) * F + 64 * j, vofff, lds0 + H_G2 + (w + 8 * i) * 1024);
  if (tid < D) {
    unsigned long long a, c;
    for (unsigned spins = 0; spins < (1u << 20); ++spins) {
      a = __hip_atomic_load((gu64*)(p.ws + WS_MG) + 3 * D + tid, __ATOMIC_RELAXED, __HIP_MEMORY_SCOPE_AGENT);
      c = __hip_atomic_load((gu64*)(p.ws + WS_MG) + 4 * D + tid, __ATOMIC_RELAXED, __HIP_MEMORY_SCOPE_AGENT);
      if ((u32)(a >> 32) == tag && (u32)(c >> 32) == tag) break;
      __builtin_amdgcn_s_sleep(1);
    }
    *LDSP(u16, H_CS + tid * 2) = (u16)(pkbf(1.0f + __uint_as_float((u32)c), 0.f));
    *LDSP(u16, H_CS + 896 + tid * 2) = (u16)((u32)a >> 16);
  }
  vm0();
  lds_barrier();
  SST(4);
  {
    f32x4 a = {0, 0, 0, 0};
#pragma unroll
    for (int ks = 0; ks < 14; ++ks)
      a = MFMA16(*LDSP(u32x4, H_CS + (col & 1) * 896 + (32 * ks + 8 * g) * 2), frag128(smem, w < 4 ? H_WG : H_G2, lbase, phi, ks, w & 3), a);
    if (lane < 16) {
      float* uv = (float*)(p.ws + WS_UV) + (size_t)(w >> 2) * 2 * F + 64 * j + 16 * (w & 3) + lane;
      uv[0] = a[0]; uv[F] = a[1];
    }
  }
  SSTE(5); SEND(1, 12);
}

__global__ __launch_bounds__(512) void kAB(Params p) {
  extern __shared__ __attribute__((aligned(16))) char smem[];
  asm volatile("" :: "s"(p.u), "s"(p.A_re), "s"(p.A_im), "s"(p.B_re), "s"(p.B_im), "s"(p.C_re), "s"(p.C_im), "s"(p.log_dt), "s"(p.Dskip),
               "s"(p.w_gate), "s"(p.w_up), "s"(p.w_down), "s"(p.c), "s"(p.w_mod), "s"(p.ws));
  if (__builtin_expect(blockIdx.x < AB_MAIN, 1)) conv_main(p, blockIdx.x, smem);
  else helper(p, blockIdx.x - AB_MAIN, smem);
}

constexpr int C_XT = 448 * 256, C_UV = C_XT, C_RES = C_UV + 2048, CD_LDS = 131072;

__device__ __forceinline__ void stats_role(const Params& p, int j, char* smem) {
  const int tid = threadIdx.x, lane = tid & 63, w = tid >> 6;
  const int lc = tid & 7, dr = tid >> 3;
  const char* src = p.ws + WS_X1T + (size_t)(j >> 1) * D * 256 + (j & 1) * 128 + lc * 16;
  u32x4 v[7];
#pragma unroll
  for (int i = 0; i < 7; ++i) v[i] = *(const u32x4*)(src + (dr + 64 * i) * 256);
  float s[8] = {}, q[8] = {};
#pragma unroll
  for (int i = 0; i < 7; ++i)
#pragma unroll
    for (int k = 0; k < 4; ++k) {
      const float a = bflo(v[i][k]), b = bfhi(v[i][k]);
      s[2 * k] += a; q[2 * k] = fmaf(a, a, q[2 * k]); s[2 * k + 1] += b; q[2 * k + 1] = fmaf(b, b, q[2 * k + 1]);
    }
#pragma unroll
  for (int k = 0; k < 8; ++k) {
    s[k] += dppf<0x128>(s[k]); q[k] += dppf<0x128>(q[k]);
    s[k] = xrow16_sum(s[k]); q[k] = xrow16_sum(q[k]);
  }
  float* red = (float*)smem;
  if (lane < 8) {
    *(f32x4*)(red + (w * 2 + 0) * 64 + lane * 8) = f32x4{s[0], s[1], s[2], s[3]};
    *(f32x4*)(red + (w * 2 + 0) * 64 + lane * 8 + 4) = f32x4{s[4], s[5], s[6], s[7]};
    *(f32x4*)(red + (w * 2 + 1) * 64 + lane * 8) = f32x4{q[0], q[1], q[2], q[3]};
    *(f32x4*)(red + (w * 2 + 1) * 64 + lane * 8 + 4) = f32x4{q[4], q[5], q[6], q[7]};
  }
  __syncthreads();
  if (tid < 64) {
    float ss = 0.f, qq = 0.f;
#pragma unroll
    for (int u = 0; u < 8; ++u) { ss += red[(u * 2) * 64 + tid]; qq += red[(u * 2 + 1) * 64 + tid]; }
    const float mean = ss * (1.0f / D);
    const float var = fmaxf(qq * (1.0f / D) - mean * mean, 0.f);
    const float sd = sqrtf(bfr(bfr(var) + 1e-5f));
    const u32 word = pkbf(mean, sd);
    __hip_atomic_store((gu32*)(p.ws + WS_STG) + 64 * j + tid, word, __ATOMIC_RELAXED, __HIP_MEMORY_SCOPE_AGENT);
  }
}

constexpr int C_LA = 3;
struct COp { int kind, arg; };
constexpr COp c_opA(int ks, int g) {
  if (g < 2 && ks + C_LA <= 13) return {1, 2 * (ks + C_LA) + g};
  if (ks == 0 && (g == 2 || g == 4 || g == 6)) return {2, 4 + (g - 2) / 2};
  if (ks == 1 && (g == 2 || g == 4)) return {2, 7 + (g - 2) / 2};
  if (ks == 1 && g == 6) return {5, 0};
  if (ks >= 2 && ks <= 6 && g == 2) return {2, ks + 7};
  if (ks >= 4 && ks <= 9 && (g == 4 || g == 6)) return {4, 2 * (ks - 4) + (g - 4) / 2};
  if (ks >= 10 && (g == 3 || g == 5 || g == 7)) return {4, 12 + 3 * (ks - 10) + (g - 3) / 2};
  return {0, 0};
}
constexpr COp c_opB(int ks, int g) { return ks == 7 ? COp{3, g} : COp{0, 0}; }
struct CSched { int wait_x0, wait_w[14], wait_x3, wait_x8, wait_st; };
constexpr CSched c_make_sched() {
  CSched r{};
  int seqW[28] = {}, seqX[14] = {}, seqS = 0, n = 0;
  for (int k = 0; k < 4; ++k) seqX[k] = n++;
  for (int k = 0; k < 2 * C_LA; ++k) seqW[k] = n++;
  ++n;
  r.wait_x0 = n - 1 - seqX[3];
  for (int ks = 0; ks < 14; ++ks) {
    r.wait_w[ks] = n - 1 - seqW[2 * ks + 1];
    for (int g = 0; g < 8; ++g) {
      if (g == 4 && ks == 3) r.wait_x3 = n - 1 - seqX[8];
      if (g == 4 && ks == 8) r.wait_x8 = n - 1 - seqX[13];
      for (int u = 0; u < 2; ++u) {
        const COp o = u ? c_opB(ks, g) : c_opA(ks, g);
        if (o.kind == 1) seqW[o.arg] = n;
        if (o.kind == 2) seqX[o.arg] = n;
        if (o.kind == 3) seqS = n;
        if (o.kind) ++n;
      }
    }
  }
  r.wait_st = n - 1 - seqS;
  return r;
}
constexpr CSched C_SCHED = c_make_sched();
constexpr int C_WDM = 24;
constexpr size_t WS_END = WS_HH + (size_t)L * F * 2;

typedef __amdgpu_buffer_rsrc_t rsrc_t;
__device__ __forceinline__ void bload16(u32x4& dst, rsrc_t r, u32 voff, u32 soff, int imm) {
  asm volatile("buffer_load_dwordx4 %0, %1, %2, %3 offen offset:%4" : "=v"(dst) : "v"(voff), "s"(r), "s"(soff), "i"(imm) : "memory");
}
__device__ __forceinline__ void bload16_sc1(u32x4& dst, rsrc_t r, u32 voff, u32 soff, int imm) {
  asm volatile("buffer_load_dwordx4 %0, %1, %2, %3 offen offset:%4 sc1" : "=v"(dst) : "v"(voff), "s"(r), "s"(soff), "i"(imm) : "memory");
}
__device__ __forceinline__ void bdma16(rsrc_t r, u32 voff, u32 sbase, u32 lbase, int add) {
  u32 t;
  asm volatile("s_add_i32 m0, %4, %5\n\ts_add_i32 %0, %3, %5\n\tbuffer_load_dwordx4 %1, %2, %0 offen lds"
               : "=&s"(t) : "v"(voff), "s"(r), "s"(sbase), "s"(lbase), "i"(add) : "memory", "scc");
}

__global__ __launch_bounds__(512) void kCD(Params p) {
  extern __shared__ __attribute__((aligned(16))) char smem[];
  if (blockIdx.x >= 224) { stats_role(p, blockIdx.x - 224, smem); return; }
  const int tid = threadIdx.x, lane = tid & 63;
  const int w = __builtin_amdgcn_readfirstlane(tid >> 6);
  const int xcd = blockIdx.x & 7, idx = blockIdx.x >> 3, i7 = (idx * 37) >> 8, Q = idx - 7 * i7;
  const int Lt = 4 * (xcd >> 1) + i7, Fi = 7 * (xcd & 1) + Q, P = 2 * Lt + (xcd & 1);
  const unsigned lds0 = (unsigned)(uintptr_t)smem;
  const int g = lane >> 4, q4 = (lane & 15) >> 2, pp = lane & 3;
  const u32 l16 = lane * 16;
  const rsrc_t ws = __builtin_amdgcn_make_buffer_rsrc(p.ws, 0, (int)WS_END, 0x00020000);

  SDECL(14); SST(0);
  u32x4 wf[28];
  const int xrow = 4 * w + g;
  const u32 xoff = (u32)(xrow * 128 + ((lane & 15) ^ (((xrow & 3) << 2) | (((xrow >> 3) & 1) << 1))) * 8) * 2;
  const u32 sx = (u32)WS_X1C + Lt * (D * 256), sl = lds0 + w * 1024, sw = (u32)WS_WF + (Fi * 8 + w) * (28 * 1024);
  auto ld_x = [&](int ks) { bdma16(ws, xoff, sx, sl, ks * 8192); };
  auto ld_w = [&](int n) { bload16(wf[n], ws, l16, sw + (n >> 2) * 4096, (n & 3) * 1024); };
#pragma unroll
  for (int ks = 0; ks < 4; ++ks) ld_x(ks);
#pragma unroll
  for (int n = 0; n < 2 * C_LA; ++n) ld_w(n);
  SB();
  if (lane < 16) glds16s(p.ws + WS_UV + ((size_t)(w >> 1) * F + 128 * Fi + 64 * (w & 1)) * 4, l16, lds0 + C_UV + w * 256);
  const u32 swd = (u32)WS_WDF + (Q * 8 + w) * (28 * 1024);
  const char* stsrc = p.ws + WS_STG + Lt * 512;
  u32 stw[8];
  auto ld_wd = [&](int n) { bload16(wf[n], ws, l16, swd + (n >> 2) * 4096, (n & 3) * 1024); };
  auto ld_st = [&](int i) {
    asm volatile("global_load_dword %0, %1, %2 sc1" : "=v"(stw[i]) : "v"((u32)(lane & 15) * 4), "s"(stsrc + ((i ^ w) << 6)) : "memory");
  };
  auto issue = [&](COp o) {
    if (o.kind == 1) ld_w(o.arg);
    if (o.kind == 2) ld_x(o.arg);
    if (o.kind == 3) ld_st(o.arg);
    if (o.kind == 4) ld_wd(o.arg);
    if (o.kind == 5)
      bdma16(ws, (u32)((lane >> 3) * 128 + ((lane & 7) ^ ((((lane >> 4) & 1) | ((w & 1) << 1)) << 1)) * 8) * 2,
             (u32)WS_X1T + ((Lt * D + 64 * Q + 8 * w) * 128 + 64 * (P & 1)) * 2 - C_RES, sl, C_RES);
  };
  SST(1);
  f32x4 ag[8], au[8];
  unsigned ta[8];
  {
    const unsigned base = (unsigned)(256 * (8 * g + q4) + 32 * ((q4 << 1) | (g & 1)) + 8 * pp);
#pragma unroll
    for (int i = 0; i < 8; ++i) ta[i] = base ^ (32u * (unsigned)(i ^ w));
  }
  u32x4 xb[2][4];
  auto rd_tile = [&](int ks, int i8, u32x4& dst) {
    lds_cptr a = (lds_cptr)smem + ta[i8] + ks * 8192;
    const u32x2 t0 = tr_read(a), t1 = tr_read(a + 1024);
    dst = u32x4{t0[0], t0[1], t1[0], t1[1]};
  };
  vmwait(C_SCHED.wait_x0);
  asm volatile("s_barrier" ::: "memory");
#pragma unroll
  for (int i = 0; i < 4; ++i) rd_tile(0, i, xb[0][i]);
  u32 hso[8];
#pragma unroll
  for (int ks = 0; ks < 14; ++ks) {
    vmwait(C_SCHED.wait_w[ks]);
    PIN4(wf[2 * ks]); PIN4(wf[2 * ks + 1]);
#pragma unroll
    for (int gi = 0; gi < 8; ++gi) {
      const int h = gi >> 2, i = gi & 3;
      if (gi == 4 && (ks == 3 || ks == 8)) {
        vmwait(ks == 3 ? C_SCHED.wait_x3 : C_SCHED.wait_x8);
        asm volatile("s_barrier" ::: "memory");
      }
      if (ks == 0) { mfma_first(ag[gi], wf[0], xb[h][i]); mfma_first(au[gi], wf[1], xb[h][i]); }
      else { mfma_acc(ag[gi], wf[2 * ks], xb[h][i]); mfma_acc(au[gi], wf[2 * ks + 1], xb[h][i]); }
      if (h == 0) rd_tile(ks, 4 + i, xb[1][i]);
      else if (ks < 13) rd_tile(ks + 1, i, xb[0][i]);
      issue(c_opA(ks, gi)); issue(c_opB(ks, gi));
      if (ks == 12) { hso[gi] = __builtin_amdgcn_readfirstlane((u32)WS_HH + ((Lt * 8 + (gi ^ w)) * 56 + 4 * Fi + (w >> 1)) * 1024); PINS(hso[gi]); }
      __builtin_amdgcn_sched_barrier(0);
    }
  }
  mfma_fence();
  SST(2);
  u32x4 xres;
  {
    xres = frag128(smem, C_RES, (8 * g + q4) * 128 + 8 * pp, (q4 >> 1) | ((g & 1) << 1), w >> 2, w & 3);
  }
  vmwait(C_SCHED.wait_st);
  for (unsigned spins = 0; spins < (1u << 16); ++spins) {
    u32 mn = 0xffffffffu;
#pragma unroll
    for (int i = 0; i < 8; ++i) { asm volatile("" : "+v"(stw[i])); mn = min(mn, stw[i]); }
    if (__all(mn >= 0x10000u)) break;
#pragma unroll
    for (int i = 0; i < 8; ++i) ld_st(i);
    vmwait(0);
  }
  SST(3);
  {
    const float* uvl = (const float*)(smem + C_UV) + 16 * w + 4 * g;
    const f32x4 ug = *(const f32x4*)(uvl), uu = *(const f32x4*)(uvl + 256), vu = *(const f32x4*)(uvl + 384);
    const f32x4 vg = *(const f32x4*)(uvl + 128) * -1.4426950408889634f;
    const u32 hoff = (u32)((16 * (2 * (w & 1) + (g >> 1)) + (lane & 15)) * 16 + 8 * (g & 1));
#pragma unroll
    for (int i = 0; i < 8; ++i) {
      const float m = bflo(stw[i]), rs = __builtin_amdgcn_rcpf(bfhi(stw[i]));
      const float rsg = rs * -1.4426950408889634f;
      f32x4 gv = (ag[i] - m * ug) * rsg + vg;
      f32x4 uv = (au[i] - m * uu) * rs + vu;
      f32x4 h;
#pragma unroll
      for (int r = 0; r < 4; ++r) h[r] = __builtin_amdgcn_rcpf(1.0f + __builtin_amdgcn_exp2f(gv[r])) * uv[r];
      const u32x2 o = {pkbf(h[0], h[1]), pkbf(h[2], h[3])};
      asm volatile("global_store_dwordx2 %0, %1, %2 sc1" :: "v"(hoff + hso[i]), "v"(o), "s"(p.ws) : "memory");
    }
  }
#pragma unroll
  for (int n = C_WDM; n < 28; ++n) ld_wd(n);
  const int dcol = 64 * Q + 32 * (w >> 2) + 8 * g;
  u32x4 gb0, gb1;
  bload16(gb0, ws, (u32)dcol * 4, (u32)WS_MOD + 5 * D * 4, 0);
  bload16(gb1, ws, (u32)dcol * 4, (u32)WS_MOD + 5 * D * 4, 16);
  SST(4);
  vmwait(28 - C_WDM + 2);
  asm volatile("s_barrier" ::: "memory");
  SST(5);
  gu32* cnt = (gu32*)(p.ws + WS_FLG) + Lt * 32;
  if (tid == 0) __hip_atomic_fetch_add(cnt, 1u, __ATOMIC_RELAXED, __HIP_MEMORY_SCOPE_AGENT);
  if (w == 0) {
    for (unsigned spins = 0; spins < (1u << 20); ++spins) {
      if (__hip_atomic_load(cnt, __ATOMIC_RELAXED, __HIP_MEMORY_SCOPE_AGENT) >= 14u) break;
      __builtin_amdgcn_s_sleep(1);
    }
  }
  asm volatile("s_barrier" ::: "memory");
  SST(6);
  u32x4 hb[3][7];
  const u32 sh = (u32)WS_HH + (4 * P * 56 + 7 * w) * 1024;
  auto ld_il = [&](int il, u32x4 (&dst)[7]) {
#pragma unroll
    for (int j = 0; j < 7; ++j) bload16_sc1(dst[j], ws, l16, sh + il * 56 * 1024 + (j >> 2) * 4096, (j & 3) * 1024);
  };
  ld_il(0, hb[0]);
  asm volatile("s_barrier" ::: "memory");
  ld_il(1, hb[1]);
  asm volatile("s_barrier" ::: "memory");
  ld_il(2, hb[2]);
#pragma unroll
  for (int n = 0; n < 28; ++n) PIN4(wf[n]);
#pragma unroll
  for (int il = 0; il < 4; ++il) {
    f32x4 acc[4];
#pragma unroll
    for (int j = 0; j < 7; ++j) {
      vmwait((il == 0 ? 14 : (il == 1 ? 14 : (il == 2 ? 7 : 0))) + 6 - j);
      PIN4(hb[il % 3][j]);
      if (il == 0 && j == 0) SST(13);
#pragma unroll
      for (int id = 0; id < 4; ++id) { if (j == 0) mfma_first(acc[id], wf[id * 7], hb[il % 3][0]); else mfma_acc(acc[id], wf[id * 7 + j], hb[il % 3][j]); }
      __builtin_amdgcn_sched_barrier(0);
    }
    if (il == 0) ld_il(3, hb[0]);
    if (il == 0) SST(9);
    if (il == 1) SST(10);
    if (il == 2) SST(11);
    if (il == 3) SST(12);
    mfma_fence();
#pragma unroll
    for (int id = 0; id < 4; ++id) *(f32x4*)(smem + ((w * 4 + il) * 4 + id) * 1024 + l16) = acc[id];
    __builtin_amdgcn_sched_barrier(0);
  }
  SST(7);
  __syncthreads();
  {
    f32x4 o0 = {0, 0, 0, 0}, o1 = {0, 0, 0, 0};
    const char* rb = smem + ((w & 3) * 4 + 2 * (w >> 2) + (g >> 1)) * 1024 + (32 * (g & 1) + (lane & 15)) * 16;
#pragma unroll
    for (int s = 0; s < 8; ++s) { o0 += *(const f32x4*)(rb + s * 16384); o1 += *(const f32x4*)(rb + s * 16384 + 256); }
    PIN4(gb0); PIN4(gb1);
    const f32x4 g0 = __builtin_bit_cast(f32x4, gb0), g1 = __builtin_bit_cast(f32x4, gb1);
    float r[8];
#pragma unroll
    for (int k = 0; k < 4; ++k) { r[k] = bfr(g0[k] * bfr(o0[k])); r[4 + k] = bfr(g1[k] * bfr(o1[k])); }
    u32x4 o;
#pragma unroll
    for (int k = 0; k < 4; ++k) o[k] = pkbf(bflo(xres[k]) + r[2 * k], bfhi(xres[k]) + r[2 * k + 1]);
    const int lrow = 64 * P + 16 * (w & 3) + (lane & 15);
    *(u32x4*)(p.out + (size_t)lrow * D + dcol) = o;
  }
  SSTE(8); SEND(2, 14);
}

extern "C" void kernel_launch(void* const* d_in, const int* in_sizes, int n_in, void* d_out, int out_size, void* d_ws,
                              size_t ws_size, hipStream_t stream) {
  Params p;
  p.u = (const u16*)d_in[0]; p.A_re = (const u16*)d_in[1]; p.A_im = (const u16*)d_in[2]; p.B_re = (const u16*)d_in[3];
  p.B_im = (const u16*)d_in[4]; p.C_re = (const u16*)d_in[5]; p.C_im = (const u16*)d_in[6]; p.log_dt = (const u16*)d_in[7];
  p.Dskip = (const u16*)d_in[8]; p.w_gate = (const u16*)d_in[10]; p.w_up = (const u16*)d_in[11]; p.w_down = (const u16*)d_in[12];
  p.c = (const u16*)d_in[13]; p.w_mod = (const u16*)d_in[14];
  p.ws = (char*)d_ws; p.out = (u16*)d_out;
  hipFuncSetAttribute((const void*)kAB, hipFuncAttributeMaxDynamicSharedMemorySize, AB_LDS);
  hipFuncSetAttribute((const void*)kCD, hipFuncAttributeMaxDynamicSharedMemorySize, CD_LDS);
  kAB<<<256, 512, AB_LDS, stream>>>(p);
  kCD<<<256, 512, CD_LDS, stream>>>(p);
}
```

```cpp
#define EXPORT 0
#include <hip/hip_runtime.h>
#include <hip/hip_bf16.h>
#include <stdint.h>

typedef unsigned short u16;
typedef unsigned int u32;
typedef __attribute__((ext_vector_type(8))) short s16x8;
typedef __attribute__((ext_vector_type(4))) short s16x4;
typedef __attribute__((ext_vector_type(8))) _Float16 f16x8;
typedef __attribute__((ext_vector_type(2))) _Float16 f16x2;
typedef __attribute__((ext_vector_type(2))) __bf16 bf16x2;
typedef __attribute__((ext_vector_type(8))) __bf16 bf16x8;
typedef __attribute__((ext_vector_type(2))) float f32x2;
typedef __attribute__((ext_vector_type(4))) float f32x4;
typedef __attribute__((ext_vector_type(16))) float f32x16;
typedef __attribute__((ext_vector_type(2))) unsigned u32x2;
typedef __attribute__((ext_vector_type(4))) unsigned u32x4;
typedef __attribute__((address_space(3))) char* lds_ptr;
typedef __attribute__((address_space(3))) const char* lds_cptr;

constexpr int L = 2048, D = 448, NS = 256, F = 1792;

constexpr size_t WS_EP = 0;
constexpr size_t WS_RD = 2048;
constexpr size_t WS_MG = 4096;
constexpr size_t WS_SG = 32768;
constexpr size_t WS_MOD = 49152;
constexpr size_t WS_FLG = 61440;
constexpr size_t WS_UV = 65536;
constexpr size_t WS_STG = 98304;
constexpr size_t WS_WDF = 131072;
constexpr size_t WS_WF = WS_WDF + (size_t)F * D * 2;
constexpr size_t WS_X1T = WS_WF + (size_t)2 * F * D * 2;
constexpr size_t WS_X1C = WS_X1T + (size_t)D * L * 2;
constexpr size_t WS_HH = WS_X1C + (size_t)D * L * 2;

struct Params {
  const u16* u; const u16* A_re; const u16* A_im; const u16* B_re; const u16* B_im; const u16* C_re; const u16* C_im;
  const u16* log_dt; const u16* Dskip; const u16* w_gate; const u16* w_up; const u16* w_down; const u16* c; const u16* w_mod;
  char* ws; u16* out;
};

#define SDECL(n) do {} while (0)
#define SST(i) do {} while (0)
#define SSTE(i) do {} while (0)
#define SEND(kid, n) do {} while (0)
__device__ __forceinline__ float bf2f(u16 v) { return __uint_as_float((u32)v << 16); }
__device__ __forceinline__ float bflo(u32 v) { return __uint_as_float(v << 16); }
__device__ __forceinline__ float bfhi(u32 v) { return __uint_as_float(v & 0xffff0000u); }
__device__ __forceinline__ u32 pkbf(float lo, float hi) {
  f32x2 v = {lo, hi}; bf16x2 b = __builtin_convertvector(v, bf16x2); return __builtin_bit_cast(u32, b);
}
__device__ __forceinline__ float bfr(float x) { return bflo(pkbf(x, x)); }
__device__ __forceinline__ u32 pkh(float lo, float hi) {
  f32x2 v = {lo, hi}; f16x2 b = __builtin_convertvector(v, f16x2); return __builtin_bit_cast(u32, b);
}
template <int CTRL> __device__ __forceinline__ float dppf(float x) {
  return __builtin_bit_cast(float, __builtin_amdgcn_mov_dpp(__builtin_bit_cast(int, x), CTRL, 0xf, 0xf, true));
}
__device__ __forceinline__ float xrow16_sum(float x) {
  auto s = __builtin_amdgcn_permlane16_swap(__float_as_uint(x), __float_as_uint(x), false, false);
  x = __uint_as_float(s[0]) + __uint_as_float(s[1]);
  auto t = __builtin_amdgcn_permlane32_swap(__float_as_uint(x), __float_as_uint(x), false, false);
  return __uint_as_float(t[0]) + __uint_as_float(t[1]);
}
__device__ __forceinline__ float xrow16_max(float x) {
  auto s = __builtin_amdgcn_permlane16_swap(__float_as_uint(x), __float_as_uint(x), false, false);
  x = fmaxf(__uint_as_float(s[0]), __uint_as_float(s[1]));
  auto t = __builtin_amdgcn_permlane32_swap(__float_as_uint(x), __float_as_uint(x), false, false);
  return fmaxf(__uint_as_float(t[0]), __uint_as_float(t[1]));
}
__device__ __forceinline__ float wave_sum(float x) {
  x += dppf<0xB1>(x); x += dppf<0x4E>(x); x += dppf<0x141>(x); x += dppf<0x128>(x);
  return xrow16_sum(x);
}
__device__ __forceinline__ float wave_max(float x) {
  x = fmaxf(x, dppf<0xB1>(x)); x = fmaxf(x, dppf<0x4E>(x)); x = fmaxf(x, dppf<0x141>(x)); x = fmaxf(x, dppf<0x128>(x));
  return xrow16_max(x);
}
__device__ __forceinline__ float dot2bf(u32 a, u32 b, float acc) {
  return __builtin_amdgcn_fdot2_f32_bf16(__builtin_bit_cast(bf16x2, a), __builtin_bit_cast(bf16x2, b), acc, false);
}
typedef short v4i16_t __attribute__((ext_vector_type(4)));
__device__ __forceinline__ u32x2 tr_read(lds_cptr p) {
  return __builtin_bit_cast(u32x2, __builtin_amdgcn_ds_read_tr16_b64_v4i16((__attribute__((address_space(3))) v4i16_t*)p));
}
__device__ __forceinline__ void glds16(const void* g, unsigned lds_base) {
  unsigned sv;
  asm volatile("s_mov_b32 %0, m0\n\ts_mov_b32 m0, %2\n\ts_nop 0\n\tglobal_load_lds_dwordx4 %1, off\n\ts_mov_b32 m0, %0"
               : "=&s"(sv) : "v"(g), "s"(lds_base) : "memory");
}
__device__ __forceinline__ float pow2_scale(float x, int target) {
  const int e = (int)(__float_as_uint(x) >> 23);
  return __uint_as_float((u32)(127 + min(max(target + 127 - e, -100), 100)) << 23);
}
#define WT_AUX 16
__device__ __forceinline__ void gload16(u32x4& dst, const void* sbase, u32 voff, int imm) {
  asm volatile("global_load_dwordx4 %0, %1, %2 offset:%3" : "=v"(dst) : "v"(voff), "s"(sbase), "i"(imm) : "memory");
}
__device__ __forceinline__ void gload16_sc1(u32x4& dst, const void* sbase, u32 voff, int imm) {
  asm volatile("global_load_dwordx4 %0, %1, %2 offset:%3 sc1" : "=v"(dst) : "v"(voff), "s"(sbase), "i"(imm) : "memory");
}
__device__ __forceinline__ void glds16s(const void* sbase, u32 voff, unsigned lds_base) {
  asm volatile("s_mov_b32 m0, %2\n\ts_nop 0\n\tglobal_load_lds_dwordx4 %0, %1" :: "v"(voff), "s"(sbase), "s"(lds_base) : "memory");
}
#define MFMA16(a, b, c) __builtin_amdgcn_mfma_f32_16x16x32_bf16(__builtin_bit_cast(bf16x8, a), __builtin_bit_cast(bf16x8, b), c, 0, 0, 0)
__device__ __forceinline__ void mfma_acc(f32x4& c, const u32x4& a, const u32x4& b) {
  asm volatile("v_mfma_f32_16x16x32_bf16 %0, %1, %2, %0" : "+v"(c) : "v"(a), "v"(b));
}
__device__ __forceinline__ void mfma_first(f32x4& c, const u32x4& a, const u32x4& b) {
  asm volatile("v_mfma_f32_16x16x32_bf16 %0, %1, %2, 0" : "=&v"(c) : "v"(a), "v"(b));
}
__device__ __forceinline__ void mfma_fence() { asm volatile("s_nop 7\n\ts_nop 7" ::: "memory"); }
#define VMWAIT(n) case n: asm volatile("s_waitcnt vmcnt(" #n ")" ::: "memory"); break
__device__ __forceinline__ void vmwait(int n) {
  switch (n) {
    VMWAIT(0); VMWAIT(1); VMWAIT(2); VMWAIT(3); VMWAIT(4); VMWAIT(5); VMWAIT(6); VMWAIT(7); VMWAIT(8); VMWAIT(9);
    VMWAIT(10); VMWAIT(11); VMWAIT(12); VMWAIT(13); VMWAIT(14); VMWAIT(15); VMWAIT(16); VMWAIT(17); VMWAIT(18); VMWAIT(19);
    VMWAIT(20); VMWAIT(21); VMWAIT(22); VMWAIT(23); VMWAIT(24); VMWAIT(25); VMWAIT(26); VMWAIT(27); VMWAIT(28); VMWAIT(29);
    VMWAIT(30); VMWAIT(31); VMWAIT(32); VMWAIT(33); VMWAIT(34); VMWAIT(35); VMWAIT(36); VMWAIT(37); VMWAIT(38); VMWAIT(39);
    default: asm volatile("s_waitcnt vmcnt(0)" ::: "memory"); break;
  }
}
#define PIN4(x) asm volatile("" : "+v"(x))

typedef __attribute__((address_space(1))) unsigned gu32;
typedef __attribute__((address_space(1))) unsigned long long gu64;
constexpr u32 TAGC = 0x9E3779B9u;
constexpr int AB_MAIN = 224;
constexpr int T_RS = 1056, T_P = 32 * T_RS, T_CH = 53 * T_RS;
constexpr int L_R = 65536, R_CP = 4160, R_CH = 4 * R_CP;
constexpr int L_W = 2 * T_CH;
constexpr int L_KT = L_W + 24576, KT_Z = 2048, KT_CH = 2064;
constexpr int L_H = L_KT + 2 * KT_CH, H_CH = 4608;
constexpr int L_SC = L_H + 2 * H_CH, AB_MAIN_LDS = L_SC + 64;

constexpr u32 kt_pos(int l) {
  if (l < 0) return KT_Z;
  int v = l;
  if (l >= 256) {
    const int s = l >= 1024 ? 3 : (l >= 512 ? 2 : 1), rem = l & ((1 << s) - 1), half = 1 << (s - 1);
    int q = l >> s;
    if (rem > half || (rem == half && (q & 1))) ++q;
    v = q << s;
  }
  int rr = 20, q = 0;
  if (v < 256) { rr = v & 7; q = v >> 3; }
  else if (v < 512) { rr = 8 + ((v >> 1) & 3); q = (v - 256) >> 3; }
  else if (v < 1024) { rr = 12 + 2 * ((v - 512) >> 8) + ((v >> 2) & 1); q = (v & 255) >> 3; }
  else if (v < 2048) { rr = 16 + ((v - 1024) >> 8); q = (v & 255) >> 3; }
  return (u32)(((rr >> 4) + 2 * (q >> 4)) * 512 + ((q & 15) + 16 * ((rr & 15) >> 2)) * 8 + 2 * (rr & 3));
}
struct GTab { u32 v[2][3][256][4]; };
constexpr GTab make_gtab() {
  GTab t{};
  for (int c = 0; c < 2; ++c)
    for (int i = 0; i < 256; ++i)
      for (int j = 0; j < 12; ++j) t.v[c][j >> 2][i][j & 3] = (u32)(L_KT + c * KT_CH) + kt_pos(2047 - 8 * i - j);
  return t;
}
static __device__ const GTab gtab = make_gtab();

__device__ __forceinline__ float rnd(float x) { return __uint_as_float(pkbf(0.f, x)); }
__device__ __forceinline__ f32x2 rnd2(f32x2 v) { return f32x2{rnd(v.x), rnd(v.y)}; }
__device__ __forceinline__ u32 pkh2(f32x2 v) { return __builtin_bit_cast(u32, __builtin_convertvector(v, f16x2)); }
struct cm { f32x2 v, n; };
__device__ __forceinline__ cm mkcm(f32x2 b) { return {b, f32x2{-b.y, b.x}}; }
__device__ __forceinline__ f32x2 cmul(f32x2 a, const cm& b) { return __builtin_elementwise_fma(a.yy, b.n, a.xx * b.v); }
__device__ __forceinline__ void cmul4(f32x2& o0, f32x2& o1, f32x2& o2, f32x2& o3, f32x2 a0, const cm& m0, f32x2 a1, const cm& m1, f32x2 a2, const cm& m2,
                                      f32x2 a3, const cm& m3) {
  f32x2 t0 = a0.xx * m0.v, t1 = a1.xx * m1.v, t2 = a2.xx * m2.v, t3 = a3.xx * m3.v;
  asm volatile("" : "+v"(t0), "+v"(t1), "+v"(t2), "+v"(t3));
  t0 = __builtin_elementwise_fma(a0.yy, m0.n, t0); t1 = __builtin_elementwise_fma(a1.yy, m1.n, t1);
  t2 = __builtin_elementwise_fma(a2.yy, m2.n, t2); t3 = __builtin_elementwise_fma(a3.yy, m3.n, t3);
  asm volatile("" : "+v"(t0), "+v"(t1), "+v"(t2), "+v"(t3));
  o0 = t0; o1 = t1; o2 = t2; o3 = t3;
}
__device__ __forceinline__ f32x2 cexpm(float zr, float zi, float m) {
  const float mag = __builtin_amdgcn_exp2f(zr * (m * 1.4426950408889634f));
  const float rev = __builtin_amdgcn_fractf(zi * (m * 0.15915494309189535f));
  return f32x2{mag * __builtin_amdgcn_cosf(rev), mag * __builtin_amdgcn_sinf(rev)};
}
__device__ __forceinline__ void st_gran(char* base, int idx, u32 val, u32 tag) {
  __hip_atomic_store((gu64*)base + idx, ((unsigned long long)tag << 32) | val, __ATOMIC_RELAXED, __HIP_MEMORY_SCOPE_AGENT);
}
template <int O0, int O1, int O2, int O3>
__device__ __forceinline__ void wr_rows4(u32 a, u32 b, u32 c, u32 d) {
  asm volatile("ds_write_addtid_b32 %0 offset:%4\n\tds_write_addtid_b32 %1 offset:%5\n\t"
               "ds_write_addtid_b32 %2 offset:%6\n\tds_write_addtid_b32 %3 offset:%7"
               :: "v"(a), "v"(b), "v"(c), "v"(d), "i"(O0), "i"(O1), "i"(O2), "i"(O3) : "memory");
}
template <int O0>
__device__ __forceinline__ void wr_rows1(u32 a) {
  asm volatile("ds_write_addtid_b32 %0 offset:%1" :: "v"(a), "i"(O0) : "memory");
}
__device__ __forceinline__ void table_q(f32x2 E8) {
  asm volatile("v_pk_mul_f32 v[232:233], v[226:227], v[226:227] op_sel_hi:[0,1]\n\tv_mov_b32 v240, 0x3c00\n\tv_cvt_pk_f16_f32 v241, v226, v227\n\tv_pk_fma_f32 v[228:229], v[226:227], v[226:227], v[232:233] op_sel:[1,1,0] op_sel_hi:[1,0,1] neg_lo:[0,1,0]\n\tds_write_addtid_b32 v240 offset:0\n\tds_write_addtid_b32 v241 offset:1056\n\tv_pk_mul_f32 v[232:233], v[228:229], v[226:227] op_sel_hi:[0,1]\n\tv_pk_mul_f32 v[234:235], v[228:229], v[228:229] op_sel_hi:[0,1]\n\tv_cvt_pk_f16_f32 v242, v228, v229\n\tv_pk_fma_f32 v[230:231], v[228:229], v[226:227], v[232:233] op_sel:[1,1,0] op_sel_hi:[1,0,1] neg_lo:[0,1,0]\n\tv_pk_fma_f32 v[244:245], v[228:229], v[228:229], v[234:235] op_sel:[1,1,0] op_sel_hi:[1,0,1] neg_lo:[0,1,0]\n\tds_write_addtid_b32 v242 offset:2112\n\tv_cvt_pk_f16_f32 v243, v230, v231\n\tv_pk_mul_f32 v[234:235], v[226:227], v[244:245] op_sel_hi:[0,1]\n\tv_pk_mul_f32 v[236:237], v[228:229], v[244:245] op_sel_hi:[0,1]\n\tv_pk_mul_f32 v[238:239], v[230:231], v[244:245] op_sel_hi:[0,1]\n\tds_write_addtid_b32 v243 offset:3168\n\tv_mov_b64 v[224:225], v[244:245]\n\tv_pk_fma_f32 v[226:227], v[226:227], v[244:245], v[234:235] op_sel:[1,1,0] op_sel_hi:[1,0,1] neg_lo:[0,1,0]\n\tv_pk_fma_f32 v[228:229], v[228:229], v[244:245], v[236:237] op_sel:[1,1,0] op_sel_hi:[1,0,1] neg_lo:[0,1,0]\n\tv_pk_fma_f32 v[230:231], v[230:231], v[244:245], v[238:239] op_sel:[1,1,0] op_sel_hi:[1,0,1] neg_lo:[0,1,0]\n\tv_cvt_pk_f16_f32 v240, v224, v225\n\tv_cvt_pk_f16_f32 v241, v226, v227\n\tv_cvt_pk_f16_f32 v242, v228, v229\n\tv_cvt_pk_f16_f32 v243, v230, v231\n\tv_pk_mul_f32 v[232:233], v[224:225], v[244:245] op_sel_hi:[0,1]\n\tv_pk_mul_f32 v[234:235], v[226:227], v[244:245] op_sel_hi:[0,1]\n\tv_pk_mul_f32 v[236:237], v[228:229], v[244:245] op_sel_hi:[0,1]\n\tv_pk_mul_f32 v[238:239], v[230:231], v[244:245] op_sel_hi:[0,1]\n\tds_write_addtid_b32 v240 offset:4224\n\tds_write_addtid_b32 v241 offset:5280\n\tds_write_addtid_b32 v242 offset:6336\n\tds_write_addtid_b32 v243 offset:7392\n\tv_pk_fma_f32 v[224:225], v[224:225], v[244:245], v[232:233] op_sel:[1,1,0] op_sel_hi:[1,0,1] neg_lo:[0,1,0]\n\tv_pk_fma_f32 v[226:227], v[226:227], v[244:245], v[234:235] op_sel:[1,1,0] op_sel_hi:[1,0,1] neg_lo:[0,1,0]\n\tv_pk_fma_f32 v[228:229], v[228:229], v[244:245], v[236:237] op_sel:[1,1,0] op_sel_hi:[1,0,1] neg_lo:[0,1,0]\n\tv_pk_fma_f32 v[230:231], v[230:231], v[244:245], v[238:239] op_sel:[1,1,0] op_sel_hi:[1,0,1] neg_lo:[0,1,0]\n\tv_cvt_pk_f16_f32 v240, v224, v225\n\tv_cvt_pk_f16_f32 v241, v226, v227\n\tv_cvt_pk_f16_f32 v242, v228, v229\n\tv_cvt_pk_f16_f32 v243, v230, v231\n\tv_pk_mul_f32 v[232:233], v[224:225], v[244:245] op_sel_hi:[0,1]\n\tv_pk_mul_f32 v[234:235], v[226:227], v[244:245] op_sel_hi:[0,1]\n\tv_pk_mul_f32 v[236:237], v[228:229], v[244:245] op_sel_hi:[0,1]\n\tv_pk_mul_f32 v[238:239], v[230:231], v[244:245] op_sel_hi:[0,1]\n\tds_write_addtid_b32 v240 offset:8448\n\tds_write_addtid_b32 v241 offset:9504\n\tds_write_addtid_b32 v242 offset:10560\n\tds_write_addtid_b32 v243 offset:11616\n\tv_pk_fma_f32 v[224:225], v[224:225], v[244:245], v[232:233] op_sel:[1,1,0] op_sel_hi:[1,0,1] neg_lo:[0,1,0]\n\tv_pk_fma_f32 v[226:227], v[226:227], v[244:245], v[234:235] op_sel:[1,1,0] op_sel_hi:[1,0,1] neg_lo:[0,1,0]\n\tv_pk_fma_f32 v[228:229], v[228:229], v[244:245], v[236:237] op_sel:[1,1,0] op_sel_hi:[1,0,1] neg_lo:[0,1,0]\n\tv_pk_fma_f32 v[230:231], v[230:231], v[244:245], v[238:239] op_sel:[1,1,0] op_sel_hi:[1,0,1] neg_lo:[0,1,0]\n\tv_cvt_pk_f16_f32 v240, v224, v225\n\tv_cvt_pk_f16_f32 v241, v226, v227\n\tv_cvt_pk_f16_f32 v242, v228, v229\n\tv_cvt_pk_f16_f32 v243, v230, v231\n\tv_pk_mul_f32 v[232:233], v[224:225], v[244:245] op_sel_hi:[0,1]\n\tv_pk_mul_f32 v[234:235], v[226:227], v[244:245] op_sel_hi:[0,1]\n\tv_pk_mul_f32 v[236:237], v[228:229], v[244:245] op_sel_hi:[0,1]\n\tv_pk_mul_f32 v[238:239], v[230:231], v[244:245] op_sel_hi:[0,1]\n\tds_write_addtid_b32 v240 offset:12672\n\tds_write_addtid_b32 v241 offset:13728\n\tds_write_addtid_b32 v242 offset:14784\n\tds_write_addtid_b32 v243 offset:15840\n\tv_pk_fma_f32 v[224:225], v[224:225], v[244:245], v[232:233] op_sel:[1,1,0] op_sel_hi:[1,0,1] neg_lo:[0,1,0]\n\tv_pk_fma_f32 v[226:227], v[226:227], v[244:245], v[234:235] op_sel:[1,1,0] op_sel_hi:[1,0,1] neg_lo:[0,1,0]\n\tv_pk_fma_f32 v[228:229], v[228:229], v[244:245], v[236:237] op_sel:[1,1,0] op_sel_hi:[1,0,1] neg_lo:[0,1,0]\n\tv_pk_fma_f32 v[230:231], v[230:231], v[244:245], v[238:239] op_sel:[1,1,0] op_sel_hi:[1,0,1] neg_lo:[0,1,0]\n\tv_cvt_pk_f16_f32 v240, v224, v225\n\tv_cvt_pk_f16_f32 v241, v226, v227\n\tv_cvt_pk_f16_f32 v242, v228, v229\n\tv_cvt_pk_f16_f32 v243, v230, v231\n\tv_pk_mul_f32 v[232:233], v[224:225], v[244:245] op_sel_hi:[0,1]\n\tv_pk_mul_f32 v[234:235], v[226:227], v[244:245] op_sel_hi:[0,1]\n\tv_pk_mul_f32 v[236:237], v[228:229], v[244:245] op_sel_hi:[0,1]\n\tv_pk_mul_f32 v[238:239], v[230:231], v[244:245] op_sel_hi:[0,1]\n\tds_write_addtid_b32 v240 offset:16896\n\tds_write_addtid_b32 v241 offset:17952\n\tds_write_addtid_b32 v242 offset:19008\n\tds_write_addtid_b32 v243 offset:20064\n\tv_pk_fma_f32 v[224:225], v[224:225], v[244:245], v[232:233] op_sel:[1,1,0] op_sel_hi:[1,0,1] neg_lo:[0,1,0]\n\tv_pk_fma_f32 v[226:227], v[226:227], v[244:245], v[234:235] op_sel:[1,1,0] op_sel_hi:[1,0,1] neg_lo:[0,1,0]\n\tv_pk_fma_f32 v[228:229], v[228:229], v[244:245], v[236:237] op_sel:[1,1,0] op_sel_hi:[1,0,1] neg_lo:[0,1,0]\n\tv_pk_fma_f32 v[230:231], v[230:231], v[244:245], v[238:239] op_sel:[1,1,0] op_sel_hi:[1,0,1] neg_lo:[0,1,0]\n\tv_cvt_pk_f16_f32 v240, v224, v225\n\tv_cvt_pk_f16_f32 v241, v226, v227\n\tv_cvt_pk_f16_f32 v242, v228, v229\n\tv_cvt_pk_f16_f32 v243, v230, v231\n\tv_pk_mul_f32 v[232:233], v[224:225], v[244:245] op_sel_hi:[0,1]\n\tv_pk_mul_f32 v[234:235], v[226:227], v[244:245] op_sel_hi:[0,1]\n\tv_pk_mul_f32 v[236:237], v[228:229], v[244:245] op_sel_hi:[0,1]\n\tv_pk_mul_f32 v[238:239], v[230:231], v[244:245] op_sel_hi:[0,1]\n\tds_write_addtid_b32 v240 offset:21120\n\tds_write_addtid_b32 v241 offset:22176\n\tds_write_addtid_b32 v242 offset:23232\n\tds_write_addtid_b32 v243 offset:24288\n\tv_pk_fma_f32 v[224:225], v[224:225], v[244:245], v[232:233] op_sel:[1,1,0] op_sel_hi:[1,0,1] neg_lo:[0,1,0]\n\tv_pk_fma_f32 v[226:227], v[226:227], v[244:245], v[234:235] op_sel:[1,1,0] op_sel_hi:[1,0,1] neg_lo:[0,1,0]\n\tv_pk_fma_f32 v[228:229], v[228:229], v[244:245], v[236:237] op_sel:[1,1,0] op_sel_hi:[1,0,1] neg_lo:[0,1,0]\n\tv_pk_fma_f32 v[230:231], v[230:231], v[244:245], v[238:239] op_sel:[1,1,0] op_sel_hi:[1,0,1] neg_lo:[0,1,0]\n\tv_cvt_pk_f16_f32 v240, v224, v225\n\tv_cvt_pk_f16_f32 v241, v226, v227\n\tv_cvt_pk_f16_f32 v242, v228, v229\n\tv_cvt_pk_f16_f32 v243, v230, v231\n\tv_pk_mul_f32 v[232:233], v[224:225], v[244:245] op_sel_hi:[0,1]\n\tv_pk_mul_f32 v[234:235], v[226:227], v[244:245] op_sel_hi:[0,1]\n\tv_pk_mul_f32 v[236:237], v[228:229], v[244:245] op_sel_hi:[0,1]\n\tv_pk_mul_f32 v[238:239], v[230:231], v[244:245] op_sel_hi:[0,1]\n\tds_write_addtid_b32 v240 offset:25344\n\tds_write_addtid_b32 v241 offset:26400\n\tds_write_addtid_b32 v242 offset:27456\n\tds_write_addtid_b32 v243 offset:28512\n\tv_pk_fma_f32 v[224:225], v[224:225], v[244:245], v[232:233] op_sel:[1,1,0] op_sel_hi:[1,0,1] neg_lo:[0,1,0]\n\tv_pk_fma_f32 v[226:227], v[226:227], v[244:245], v[234:235] op_sel:[1,1,0] op_sel_hi:[1,0,1] neg_lo:[0,1,0]\n\tv_pk_fma_f32 v[228:229], v[228:229], v[244:245], v[236:237] op_sel:[1,1,0] op_sel_hi:[1,0,1] neg_lo:[0,1,0]\n\tv_pk_fma_f32 v[230:231], v[230:231], v[244:245], v[238:239] op_sel:[1,1,0] op_sel_hi:[1,0,1] neg_lo:[0,1,0]\n\tv_cvt_pk_f16_f32 v240, v224, v225\n\tv_cvt_pk_f16_f32 v241, v226, v227\n\tv_cvt_pk_f16_f32 v242, v228, v229\n\tv_cvt_pk_f16_f32 v243, v230, v231\n\tds_write_addtid_b32 v240 offset:29568\n\tds_write_addtid_b32 v241 offset:30624\n\tds_write_addtid_b32 v242 offset:31680\n\tds_write_addtid_b32 v243 offset:32736"
               : "+{v[226:227]}"(E8) :: "v224", "v225", "v228", "v229", "v230", "v231", "v232", "v233", "v234", "v235", "v236", "v237", "v238", "v239", "v240", "v241", "v242", "v243", "v244", "v245", "memory");
}
__device__ __forceinline__ void table_p(f32x2 W, f32x2 E1, f32x2 E256) {
  asm volatile("v_pk_mul_f32 v[238:239], v[226:227], v[226:227] op_sel_hi:[0,1]\n\tv_pk_mul_f32 v[240:241], v[228:229], v[228:229] op_sel_hi:[0,1]\n\tv_pk_mul_f32 v[242:243], v[224:225], v[226:227] op_sel_hi:[0,1]\n\tv_pk_mul_f32 v[244:245], v[224:225], v[228:229] op_sel_hi:[0,1]\n\tv_cvt_pk_f16_f32 v200, v224, v225\n\tv_pk_fma_f32 v[208:209], v[226:227], v[226:227], v[238:239] op_sel:[1,1,0] op_sel_hi:[1,0,1] neg_lo:[0,1,0]\n\tv_pk_fma_f32 v[212:213], v[228:229], v[228:229], v[240:241] op_sel:[1,1,0] op_sel_hi:[1,0,1] neg_lo:[0,1,0]\n\tv_pk_fma_f32 v[216:217], v[224:225], v[226:227], v[242:243] op_sel:[1,1,0] op_sel_hi:[1,0,1] neg_lo:[0,1,0]\n\tv_pk_fma_f32 v[222:223], v[224:225], v[228:229], v[244:245] op_sel:[1,1,0] op_sel_hi:[1,0,1] neg_lo:[0,1,0]\n\tds_write_addtid_b32 v200 offset:33792\n\tv_pk_mul_f32 v[238:239], v[208:209], v[208:209] op_sel_hi:[0,1]\n\tv_pk_mul_f32 v[240:241], v[212:213], v[212:213] op_sel_hi:[0,1]\n\tv_pk_mul_f32 v[242:243], v[224:225], v[208:209] op_sel_hi:[0,1]\n\tv_pk_mul_f32 v[244:245], v[224:225], v[212:213] op_sel_hi:[0,1]\n\tv_cvt_pk_f16_f32 v201, v216, v217\n\tv_cvt_pk_f16_f32 v202, v222, v223\n\tv_pk_fma_f32 v[210:211], v[208:209], v[208:209], v[238:239] op_sel:[1,1,0] op_sel_hi:[1,0,1] neg_lo:[0,1,0]\n\tv_pk_fma_f32 v[214:215], v[212:213], v[212:213], v[240:241] op_sel:[1,1,0] op_sel_hi:[1,0,1] neg_lo:[0,1,0]\n\tv_pk_fma_f32 v[218:219], v[224:225], v[208:209], v[242:243] op_sel:[1,1,0] op_sel_hi:[1,0,1] neg_lo:[0,1,0]\n\tv_pk_fma_f32 v[232:233], v[224:225], v[212:213], v[244:245] op_sel:[1,1,0] op_sel_hi:[1,0,1] neg_lo:[0,1,0]\n\tds_write_addtid_b32 v201 offset:34848\n\tds_write_addtid_b32 v202 offset:42240\n\tv_pk_mul_f32 v[238:239], v[216:217], v[208:209] op_sel_hi:[0,1]\n\tv_pk_mul_f32 v[240:241], v[222:223], v[208:209] op_sel_hi:[0,1]\n\tv_pk_mul_f32 v[242:243], v[222:223], v[212:213] op_sel_hi:[0,1]\n\tv_pk_mul_f32 v[244:245], v[224:225], v[214:215] op_sel_hi:[0,1]\n\tv_cvt_pk_f16_f32 v203, v218, v219\n\tv_cvt_pk_f16_f32 v204, v232, v233\n\tv_pk_mul_f32 v[246:247], v[224:225], v[210:211] op_sel_hi:[0,1]\n\tv_pk_mul_f32 v[248:249], v[216:217], v[210:211] op_sel_hi:[0,1]\n\tv_pk_mul_f32 v[250:251], v[218:219], v[210:211] op_sel_hi:[0,1]\n\tv_pk_mul_f32 v[252:253], v[222:223], v[210:211] op_sel_hi:[0,1]\n\tv_pk_fma_f32 v[220:221], v[216:217], v[208:209], v[238:239] op_sel:[1,1,0] op_sel_hi:[1,0,1] neg_lo:[0,1,0]\n\tv_pk_fma_f32 v[230:231], v[222:223], v[208:209], v[240:241] op_sel:[1,1,0] op_sel_hi:[1,0,1] neg_lo:[0,1,0]\n\tv_pk_fma_f32 v[234:235], v[222:223], v[212:213], v[242:243] op_sel:[1,1,0] op_sel_hi:[1,0,1] neg_lo:[0,1,0]\n\tv_pk_fma_f32 v[236:237], v[224:225], v[214:215], v[244:245] op_sel:[1,1,0] op_sel_hi:[1,0,1] neg_lo:[0,1,0]\n\tds_write_addtid_b32 v203 offset:35904\n\tds_write_addtid_b32 v204 offset:46464\n\tv_pk_fma_f32 v[246:247], v[224:225], v[210:211], v[246:247] op_sel:[1,1,0] op_sel_hi:[1,0,1] neg_lo:[0,1,0]\n\tv_pk_fma_f32 v[248:249], v[216:217], v[210:211], v[248:249] op_sel:[1,1,0] op_sel_hi:[1,0,1] neg_lo:[0,1,0]\n\tv_pk_fma_f32 v[250:251], v[218:219], v[210:211], v[250:251] op_sel:[1,1,0] op_sel_hi:[1,0,1] neg_lo:[0,1,0]\n\tv_pk_fma_f32 v[252:253], v[222:223], v[210:211], v[252:253] op_sel:[1,1,0] op_sel_hi:[1,0,1] neg_lo:[0,1,0]\n\tv_pk_mul_f32 v[238:239], v[220:221], v[210:211] op_sel_hi:[0,1]\n\tv_pk_mul_f32 v[240:241], v[230:231], v[210:211] op_sel_hi:[0,1]\n\tv_pk_mul_f32 v[242:243], v[232:233], v[210:211] op_sel_hi:[0,1]\n\tv_pk_mul_f32 v[244:245], v[234:235], v[210:211] op_sel_hi:[0,1]\n\tv_cvt_pk_f16_f32 v200, v220, v221\n\tv_cvt_pk_f16_f32 v201, v230, v231\n\tv_cvt_pk_f16_f32 v202, v234, v235\n\tv_cvt_pk_f16_f32 v205, v236, v237\n\tv_cvt_pk_f16_f32 v206, v246, v247\n\tv_cvt_pk_f16_f32 v207, v248, v249\n\tv_cvt_pk_f16_f32 v203, v250, v251\n\tv_cvt_pk_f16_f32 v204, v252, v253\n\tv_pk_fma_f32 v[238:239], v[220:221], v[210:211], v[238:239] op_sel:[1,1,0] op_sel_hi:[1,0,1] neg_lo:[0,1,0]\n\tv_pk_fma_f32 v[240:241], v[230:231], v[210:211], v[240:241] op_sel:[1,1,0] op_sel_hi:[1,0,1] neg_lo:[0,1,0]\n\tv_pk_fma_f32 v[242:243], v[232:233], v[210:211], v[242:243] op_sel:[1,1,0] op_sel_hi:[1,0,1] neg_lo:[0,1,0]\n\tv_pk_fma_f32 v[244:245], v[234:235], v[210:211], v[244:245] op_sel:[1,1,0] op_sel_hi:[1,0,1] neg_lo:[0,1,0]\n\tds_write_addtid_b32 v200 offset:36960\n\tds_write_addtid_b32 v201 offset:43296\n\tds_write_addtid_b32 v202 offset:48576\n\tds_write_addtid_b32 v205 offset:50688\n\tv_pk_mul_f32 v[246:247], v[222:223], v[214:215] op_sel_hi:[0,1]\n\tv_pk_mul_f32 v[248:249], v[232:233], v[214:215] op_sel_hi:[0,1]\n\tv_pk_mul_f32 v[250:251], v[234:235], v[214:215] op_sel_hi:[0,1]\n\tv_pk_mul_f32 v[252:253], v[236:237], v[214:215] op_sel_hi:[0,1]\n\tds_write_addtid_b32 v206 offset:38016\n\tds_write_addtid_b32 v207 offset:39072\n\tds_write_addtid_b32 v203 offset:40128\n\tds_write_addtid_b32 v204 offset:44352\n\tv_cvt_pk_f16_f32 v200, v238, v239\n\tv_cvt_pk_f16_f32 v201, v240, v241\n\tv_cvt_pk_f16_f32 v202, v242, v243\n\tv_cvt_pk_f16_f32 v205, v244, v245\n\tv_pk_fma_f32 v[246:247], v[222:223], v[214:215], v[246:247] op_sel:[1,1,0] op_sel_hi:[1,0,1] neg_lo:[0,1,0]\n\tv_pk_fma_f32 v[248:249], v[232:233], v[214:215], v[248:249] op_sel:[1,1,0] op_sel_hi:[1,0,1] neg_lo:[0,1,0]\n\tv_pk_fma_f32 v[250:251], v[234:235], v[214:215], v[250:251] op_sel:[1,1,0] op_sel_hi:[1,0,1] neg_lo:[0,1,0]\n\tv_pk_fma_f32 v[252:253], v[236:237], v[214:215], v[252:253] op_sel:[1,1,0] op_sel_hi:[1,0,1] neg_lo:[0,1,0]\n\tds_write_addtid_b32 v200 offset:41184\n\tds_write_addtid_b32 v201 offset:45408\n\tds_write_addtid_b32 v202 offset:47520\n\tds_write_addtid_b32 v205 offset:49632\n\tv_cvt_pk_f16_f32 v206, v246, v247\n\tv_cvt_pk_f16_f32 v207, v248, v249\n\tv_cvt_pk_f16_f32 v203, v250, v251\n\tv_cvt_pk_f16_f32 v204, v252, v253\n\tds_write_addtid_b32 v206 offset:51744\n\tds_write_addtid_b32 v207 offset:52800\n\tds_write_addtid_b32 v203 offset:53856\n\tds_write_addtid_b32 v204 offset:54912"
               :: "{v[224:225]}"(W), "{v[226:227]}"(E1), "{v[228:229]}"(E256) : "v200", "v201", "v202", "v203", "v204", "v205", "v206", "v207", "v208", "v209", "v210", "v211", "v212", "v213", "v214", "v215", "v216", "v217", "v218", "v219", "v220", "v221", "v222", "v223", "v230", "v231", "v232", "v233", "v234", "v235", "v236", "v237", "v238", "v239", "v240", "v241", "v242", "v243", "v244", "v245", "v246", "v247", "v248", "v249", "v250", "v251", "v252", "v253", "memory");
}
__device__ __forceinline__ void lds_barrier() { asm volatile("s_waitcnt lgkmcnt(0)\n\ts_barrier" ::: "memory"); }
#define LDSP(T, off) ((__attribute__((address_space(3))) T*)((lds_ptr)smem + (off)))
#define MFMA16H(a, b, c) __builtin_amdgcn_mfma_f32_16x16x32_f16(__builtin_bit_cast(f16x8, a), __builtin_bit_cast(f16x8, b), c, 0, 0, 0)
__device__ __forceinline__ void glds16v(const void* sbase, u32 voff, unsigned lds_base) {
  asm volatile("s_mov_b32 m0, %2\n\ts_nop 0\n\tglobal_load_lds_dwordx4 %0, %1" :: "v"(voff), "s"(sbase), "s"(lds_base) : "memory");
}
__device__ __forceinline__ void reload16_sc1(u32x4& dst, const void* sbase, u32 voff, int imm) {
  asm volatile("global_load_dwordx4 %0, %1, %2 offset:%3 sc1" : "+v"(dst) : "v"(voff), "s"(sbase), "i"(imm) : "memory");
}
__device__ __forceinline__ void vm0() { asm volatile("s_waitcnt vmcnt(0)" ::: "memory"); }
template <int N> __device__ __forceinline__ void vmw() { asm volatile("s_waitcnt vmcnt(%0)" :: "i"(N) : "memory"); }
#define PIN(x) asm volatile("" : "+v"(x))
#define SB() __builtin_amdgcn_sched_barrier(0)
__device__ __forceinline__ u32x4 frag128(const char* smem, int base, int lbase, int phi, int ks, int t) {
  lds_cptr a = (lds_cptr)smem + base + lbase + 32 * (t ^ phi) + ks * 4096;
  const u32x2 t0 = tr_read(a), t1 = tr_read(a + 512);
  return u32x4{t0[0], t0[1], t1[0], t1[1]};
}
__device__ __forceinline__ void st16_wt(const __amdgpu_buffer_rsrc_t& r, int off, u32x4 v) { __builtin_amdgcn_raw_buffer_store_b128(v, r, off, 0, WT_AUX); }
__device__ __forceinline__ u32 cld32(const void* q) { return *(const __attribute__((address_space(4))) u32*)(uintptr_t)q; }
__device__ __forceinline__ float pow2_inv(float s) { return __uint_as_float(0x7F000000u - __float_as_uint(s)); }
__device__ __forceinline__ float rlane(u32 v, int l) { return __uint_as_float(__builtin_amdgcn_readlane(v, l)); }
#define LDS4(adr) (*(const __attribute__((address_space(3))) u32x4*)(adr))
#define LDS2(adr) (*(const __attribute__((address_space(3))) u32x2*)(adr))

__device__ __forceinline__ float sel4(const f32x4& a, int r) {
  const float lo = (r & 1) ? a[1] : a[0], hi = (r & 1) ? a[3] : a[2];
  return (r & 2) ? hi : lo;
}
__device__ __forceinline__ void ln_rows(const Params& p, u32x4 (&xr)[14], int row16, int lane, u32 tag) {
  const int col = lane & 15, g = lane >> 4;
  f32x4 ag = {0, 0, 0, 0}, as = {0, 0, 0, 0};
  const u32x4 ones = {0x3F803F80u, 0x3F803F80u, 0x3F803F80u, 0x3F803F80u};
#pragma unroll
  for (int s = 0; s < 14; ++s) { PIN4(xr[s]); ag = MFMA16(xr[s], xr[s], ag); as = MFMA16(xr[s], ones, as); }
  const float qq = sel4(ag, col), ss = sel4(as, col);
  const float mean = ss * (1.0f / D);
  const float var = fmaxf(qq * (1.0f / D) - mean * mean, 0.f);
  const float sd = __builtin_amdgcn_sqrtf(rnd(rnd(var) + 1e-5f));
  if (g == (col >> 2)) st_gran(p.ws + WS_SG, row16 + col, pkbf(mean, sd), tag);
}
template <int N> __device__ __forceinline__ void vmw_pin(u32x4& a) { asm volatile("s_waitcnt vmcnt(%1)" : "+v"(a) : "i"(N) : "memory"); }
template <int N> __device__ __forceinline__ void vmw_pin(u32x4& a, u32x4& b) { asm volatile("s_waitcnt vmcnt(%2)" : "+v"(a), "+v"(b) : "i"(N) : "memory"); }
#define LDS4U(adr) (*(const __attribute__((address_space(3))) u32x4 __attribute__((aligned(8)))*)(adr))
typedef u32x4 __attribute__((aligned(8))) u32x4_a8;

#define PINS(x) asm volatile("" : "+s"(x))
template <int OFF> __device__ __forceinline__ void lds_rd64(u32x2& dst, u32 adr) {
  asm volatile("ds_read_b64 %0, %1 offset:%2" : "=v"(dst) : "v"(adr), "i"(OFF) : "memory");
}
__device__ __forceinline__ void swap16(u32& a, u32& b) {
  const auto s = __builtin_amdgcn_permlane16_swap(a, b, false, false);
  a = s[0]; b = s[1];
}
#define X1_AUX 0

__device__ __forceinline__ void conv_main(const Params& p, int b, char* smem) {
  const int tid = threadIdx.x, lane = tid & 63;
  const int w = __builtin_amdgcn_readfirstlane(tid >> 6), ch = w >> 2, pq = w & 3;
  const int cp = (b & 7) * 28 + (b >> 3), d0 = 2 * cp, d = d0 + ch;
  const int col = lane & 15, g = lane >> 4;
  const unsigned lds0 = (unsigned)(uintptr_t)smem;
  SDECL(14); SST(0);
  const unsigned long long ex0 = __builtin_amdgcn_s_memrealtime();
  unsigned long long ex7 = 0, ex4 = 0;
  const bool ln = w == 0 && b < 128;
  u32x4 xr[14];
  if (ln) {
    const char* xs = (const char*)(p.u + (size_t)(16 * b) * D);
    const u32 xo = (u32)(col * D + 8 * g) * 2;
#pragma unroll
    for (int s = 0; s < 14; ++s) gload16(xr[s], xs, xo, s * 64);
  }
  u32 pa[6];
  {
    const u32 po = (u32)(d * NS + pq * 64 + lane) * 2;
    const u16* const ps[6] = {p.A_re, p.A_im, p.B_re, p.B_im, p.C_re, p.C_im};
#pragma unroll
    for (int i = 0; i < 6; ++i) asm volatile("global_load_short_d16_hi %0, %1, %2" : "=v"(pa[i]) : "v"(po), "s"(ps[i]) : "memory");
  }
  const u32 ldt2 = cld32(p.log_dt + d0), dsk2 = cld32(p.Dskip + d0), tag = cld32(p.ws + WS_EP) + TAGC;
  asm volatile("s_barrier" ::: "memory");
  const int row0 = 256 * w + 16 * col + 4 * g;
  const u32 xo4 = (u32)row0 * (D * 2) + cp * 4;
  u32 xg[4];
  u32x4 gt[3];
#pragma unroll
  for (int i = 0; i < 3; ++i) gload16(gt[i], &gtab.v[ch][i][0][0], (u32)(tid & 255) * 16, 0);
  asm volatile("global_load_dword %0, %1, %2" : "=v"(xg[0]) : "v"(xo4), "s"(p.u) : "memory");
  asm volatile("global_load_dword %0, %1, %2 offset:896" : "=v"(xg[1]) : "v"(xo4), "s"(p.u) : "memory");
  const int wt = b < 196 ? b : b - 196, wta = wt / 7, wtb = wt - 7 * wta;
  const u16* wsrc0 = p.w_gate + (size_t)(64 * wtb + 8 * w) * F + 64 * wta;
  const u16* wsrc1 = p.w_up + (size_t)(64 * wtb + 8 * w) * F + 64 * wta;
  const u16* wsrc2 = p.w_down + (size_t)(64 * wta + 8 * w) * D + 64 * wtb;
  u32 vf, vd;
  {
    const int dchunk = (lane & 7) ^ ((((lane >> 4) & 1) | ((w & 1) << 1)) << 1);
    vf = (u32)((lane >> 3) * F + dchunk * 8) * 2; vd = (u32)((lane >> 3) * D + dchunk * 8) * 2;
  }
  PINS(wsrc0); PINS(wsrc1); PINS(wsrc2); PIN(vf); PIN(vd);
  if (tid < 60) *LDSP(u32x4, L_H + (tid >= 30 ? H_CH - 480 : 0) + tid * 16) = u32x4{0, 0, 0, 0};
  if (tid >= 128 && tid < 130) *LDSP(u16, L_KT + (tid & 1) * KT_CH + KT_Z) = 0;
  const int rt = pq & 1, ct = pq >> 1;
  u32 a_fq = lds0 + ch * T_CH + (16 * ct + col) * T_RS + 16 * g;
  u32 a_fp = lds0 + ch * T_CH + T_P + (16 * rt + col) * T_RS + 16 * g;
  u32 a_kt = lds0 + L_KT + ch * KT_CH + pq * 512 + lane * 8;
  u32 a_rw = lds0 + L_R + ch * R_CH + (tid & 255) * 16;
  u32 a_ar;
  {
    const int e0 = 2031 - col + 8 * g, c4 = e0 & 3;
    a_ar = lds0 + L_R + ch * R_CH + c4 * R_CP + 2 * (e0 - c4) - 64 * 63 - 64 * pq + 192;
  }
  u32 a_hw = lds0 + L_H + 480 + row0 * 2;
  u32 a_br = lds0 + L_H + ch * H_CH + 448 - 256 - 64 * pq + 32 * col + 16 * g;
  u32 a_pw = lds0 + w * 8192 + lane * 16, a_pr = lds0 + w * 1024 + lane * 16;
  u32 a_wf;
  {
    const int q4 = col >> 2, pp = lane & 3, phi = (q4 >> 1) | ((g & 1) << 1);
    a_wf = lds0 + L_W + (8 * g + q4) * 128 + 8 * pp + 32 * ((w & 3) ^ phi) + (w >> 2) * 4096;
  }
  u32 sgo = (u32)WS_SG + row0 * 8, mgo = (u32)WS_MG + (((lane & 3) == 3 ? 4 : (lane & 3)) * D + d0) * 8;
  u32 oo = (u32)(((2 * w + (col >> 3)) * D + d0 + (g & 1)) * 128 + 16 * (col & 7) + 8 * (g >> 1)) * 2 + (u32)WS_X1T;
  u32 wo0, wo1;
  {
    const int ks = 2 * wtb + (w >> 2), ft = 4 * wta + (w & 3), kk = 2 * wta + (w >> 2), w7 = kk / 7;
    wo0 = (u32)WS_WF + ((ft * 14 + ks) * 2) * 1024;
    wo1 = (u32)WS_WDF + (((wtb * 8 + w7) * 4 + (w & 3)) * 7 + (kk - 7 * w7)) * 1024;
  }
  PIN(a_fq); PIN(a_fp); PIN(a_kt); PIN(a_rw); PIN(a_ar); PIN(a_hw); PIN(a_br); PIN(a_pw); PIN(a_pr); PIN(a_wf); PIN(sgo); PIN(mgo); PIN(oo);
  PINS(wo0); PINS(wo1);
  float dt = rnd(expf(ch ? bfhi(ldt2) : bflo(ldt2)));
  PIN(dt);
  const u32 m0v = lds0 + ch * T_CH + pq * 256;
  SB();
  SST(1);
  if (ln) { vmw<11>(); ln_rows(p, xr, 16 * b, lane, tag); vmw<6>(); }
  else vmw<5>();
#pragma unroll
  for (int i = 0; i < 6; ++i) PIN(pa[i]);
  if (tid == 448) __hip_atomic_store((gu32*)(p.ws + WS_RD) + b, tag, __ATOMIC_RELAXED, __HIP_MEMORY_SCOPE_AGENT);
  SST(2);
  const float zr = dt * bfhi(pa[0]), zi = dt * bfhi(pa[1]);
  asm volatile("s_mov_b32 m0, %0" :: "s"(m0v) : "memory");
  {
    table_q(cexpm(zr, zi, 8.f));
  }
  asm volatile("global_load_dword %0, %1, %2 offset:1792" : "=v"(xg[2]) : "v"(xo4), "s"(p.u) : "memory");
  asm volatile("global_load_dword %0, %1, %2 offset:2688" : "=v"(xg[3]) : "v"(xo4), "s"(p.u) : "memory");
  SST(3);
  lds_barrier();
  u32x4 fq[16];
#pragma unroll
  for (int s = 0; s < 16; ++s) fq[s] = LDS4((lds_cptr)(uintptr_t)a_fq + 64 * s);
  SB();
  {
    const float br = dt * bfhi(pa[2]), bi = dt * bfhi(pa[3]), cr = bfhi(pa[4]), ci = bfhi(pa[5]);
    f32x2 W = {cr * br - ci * bi, -(cr * bi + ci * br)};
    const float S = wave_sum(fabsf(W.x) + fabsf(W.y));
    const f32x2 E1 = cexpm(zr, -zi, 1.f), E256 = cexpm(zr, -zi, 256.f);
    const float sw = pow2_scale(S, 14);
    W *= sw;
    if (lane == 0) *LDSP(f32x2, L_SC + w * 8) = f32x2{pow2_inv(sw), S};
    table_p(W, E1, E256);
  }
  SST(4);
  ex4 = __builtin_amdgcn_s_memrealtime();
  lds_barrier();
  float kt0, kt1;
  u32x4 mg, st0, st1;
  {
    u32x4 fp[16];
#pragma unroll
    for (int s = 0; s < 4; ++s)
#pragma unroll
      for (int c = 0; c < 4; ++c) fp[4 * c + s] = LDS4((lds_cptr)(uintptr_t)a_fp + 256 * c + 64 * s);
    const f32x4 s0 = *LDSP(f32x4, L_SC), s1 = *LDSP(f32x4, L_SC + 16), s2 = *LDSP(f32x4, L_SC + 32), s3 = *LDSP(f32x4, L_SC + 48);
    const f32x4 sa = *LDSP(f32x4, L_SC + ch * 32), sb = *LDSP(f32x4, L_SC + ch * 32 + 16);
    gload16_sc1(mg, p.ws, mgo, 0); gload16_sc1(st0, p.ws, sgo, 0); gload16_sc1(st1, p.ws, sgo, 16);
    glds16v(wsrc0, vf, lds0 + L_W + w * 1024);
    glds16v(wsrc1, vf, lds0 + L_W + 8192 + w * 1024);
    glds16v(wsrc2, vd, lds0 + L_W + 16384 + w * 1024);
    SB();
    f32x4 acc[4];
#pragma unroll
    for (int s = 0; s < 4; ++s)
#pragma unroll
      for (int c = 0; c < 4; ++c) acc[c] = MFMA16H(fp[4 * c + s], fq[4 * c + s], (s == 0 ? f32x4{0, 0, 0, 0} : acc[c]));
    kt0 = pow2_scale(s0[1] + s0[3] + s1[1] + s1[3], 14);
    kt1 = pow2_scale(s2[1] + s2[3] + s3[1] + s3[3], 14);
    const float kt = pow2_scale(sa[1] + sa[3] + sb[1] + sb[3], 14);
    const f32x4 kv = acc[0] * (sa[0] * kt) + acc[1] * (sa[2] * kt) + acc[2] * (sb[0] * kt) + acc[3] * (sb[2] * kt);
    *(__attribute__((address_space(3))) u32x2*)(uintptr_t)a_kt = u32x2{pkh(kv[0], kv[1]), pkh(kv[2], kv[3])};
  }
  asm volatile("s_waitcnt vmcnt(10)" : "+v"(gt[0]), "+v"(gt[1]), "+v"(gt[2]) :: "memory");
  SST(5);
  lds_barrier();
  {
    u32 v[11];
#pragma unroll
    for (int j = 0; j < 11; ++j) v[j] = *(const __attribute__((address_space(3))) u16*)(uintptr_t)(gt[j >> 2][j & 3] + lds0);
    u32 e[5], o[5];
#pragma unroll
    for (int j = 0; j < 5; ++j) { e[j] = v[2 * j] | (v[2 * j + 1] << 16); o[j] = v[2 * j + 1] | (v[2 * j + 2] << 16); }
    lds_ptr rb = (lds_ptr)(uintptr_t)a_rw;
    *(__attribute__((address_space(3))) u32x4*)(rb) = u32x4{e[0], e[1], e[2], e[3]};
    *(__attribute__((address_space(3))) u32x4*)(rb + R_CP) = u32x4{o[0], o[1], o[2], o[3]};
    *(__attribute__((address_space(3))) u32x4*)(rb + 2 * R_CP) = u32x4{e[1], e[2], e[3], e[4]};
    *(__attribute__((address_space(3))) u32x4*)(rb + 3 * R_CP) = u32x4{o[1], o[2], o[3], o[4]};
    if ((tid & 255) < 8) *LDSP(u32x4, L_R + ch * R_CH + ((tid & 7) >> 1) * R_CP + 4096 + (tid & 1) * 16) = u32x4{0, 0, 0, 0};
  }
  SST(6);
  lds_barrier();
  u32x2 Al[2][8], Ah[2][8];
  {
    u32 a2 = a_ar + 2048;
    PIN(a2);
#define RDA(u, k) do { if (k < 4) { lds_rd64<64 * (28 - 4 * u - 8 * k) + 0 * (k < 4)>(Al[u][k], a2); lds_rd64<64 * (28 - 4 * u - 8 * k) + 8>(Ah[u][k], a2); } } while (0)
#define RDB(u, k) do { lds_rd64<64 * (60 - 4 * u - 8 * k)>(Al[u][k], a_ar); lds_rd64<64 * (60 - 4 * u - 8 * k) + 8>(Ah[u][k], a_ar); } while (0)
    RDA(0, 0); RDA(0, 1); RDA(0, 2); RDA(0, 3); RDB(0, 4); RDB(0, 5); RDB(0, 6); RDB(0, 7);
    RDA(1, 0); RDA(1, 1); RDA(1, 2); RDA(1, 3); RDB(1, 4); RDB(1, 5); RDB(1, 6); RDB(1, 7);
#undef RDA
#undef RDB
  }
  asm volatile("s_waitcnt vmcnt(3)" : "+v"(mg), "+v"(st0), "+v"(st1) :: "memory");
#define NE(x) __builtin_amdgcn_ballot_w64((x) != tag)
  unsigned long long bm = NE(mg[1]) | NE(mg[3]);
  if ((NE(st0[1]) | NE(st0[3]) | NE(st1[1]) | NE(st1[3]) | (bm & 3)) != 0) {
#pragma unroll 1
    for (unsigned spins = 0; spins < (1u << 20); ++spins) {
      __builtin_amdgcn_s_sleep(1);
      asm volatile("global_load_dwordx4 %0, %3, %5 sc1\n\tglobal_load_dwordx4 %1, %3, %5 offset:16 sc1\n\tglobal_load_dwordx4 %2, %4, %5 sc1\n\ts_waitcnt vmcnt(0)"
                   : "+v"(st0), "+v"(st1), "+v"(mg) : "v"(sgo), "v"(mgo), "s"(p.ws) : "memory");
      bm = NE(mg[1]) | NE(mg[3]);
      if ((NE(st0[1]) | NE(st0[3]) | NE(st1[1]) | NE(st1[3]) | (bm & 3)) == 0) break;
    }
  }
#pragma unroll
  for (int r = 0; r < 4; ++r) PIN(xg[r]);
  SST(7);
  ex7 = __builtin_amdgcn_s_memrealtime();
  f32x2 xf[2][2], h[2][2];
  float ihs[2], dks[2];
  {
    const u32 sw_[4] = {st0[0], st0[2], st1[0], st1[2]};
    f32x2 m2[2], r2[2];
#pragma unroll
    for (int hh = 0; hh < 2; ++hh) {
      m2[hh] = f32x2{bflo(sw_[2 * hh]), bflo(sw_[2 * hh + 1])};
      r2[hh] = f32x2{__builtin_amdgcn_rcpf(bfhi(sw_[2 * hh])), __builtin_amdgcn_rcpf(bfhi(sw_[2 * hh + 1]))};
      xf[0][hh] = f32x2{bflo(xg[2 * hh]), bflo(xg[2 * hh + 1])};
      xf[1][hh] = f32x2{bfhi(xg[2 * hh]), bfhi(xg[2 * hh + 1])};
    }
#pragma unroll
    for (int c = 0; c < 2; ++c) {
      const float sa = rlane(mg[2 * c], 0), ca = rnd(rlane(mg[2 * c], 1) + 1.0f);
      const float hs = pow2_scale(22.0f * fabsf(ca) + fabsf(sa), 14);
      ihs[c] = pow2_inv(hs);
      dks[c] = (c ? bfhi(dsk2) : bflo(dsk2)) * ihs[c];
      const float cas = ca * hs, sas = sa * hs;
#pragma unroll
      for (int hh = 0; hh < 2; ++hh) {
        const f32x2 xn = rnd2(rnd2(xf[c][hh] - m2[hh]) * r2[hh]);
        h[c][hh] = rnd2(rnd2(xn * cas) + sas);
      }
      *(__attribute__((address_space(3))) u32x2*)(uintptr_t)(a_hw + c * H_CH) = u32x2{pkh2(h[c][0]), pkh2(h[c][1])};
    }
  }
  const bool again = (bm & 12) != 0;
  vm0();
  if (again) reload16_sc1(mg, p.ws, mgo, 0);
  SST(8);
  lds_barrier();
  f32x2 hd[2][2];
  {
    u32x4 B[2][8], A[2][8];
#pragma unroll
    for (int u = 0; u < 2; ++u)
#pragma unroll
      for (int k = 0; k < 8; ++k) B[u][k] = LDS4((lds_cptr)(uintptr_t)a_br + 512 * k + 256 * (1 - u));
    u32x4 wfr[3];
#pragma unroll
    for (int i = 0; i < 3; ++i) {
      lds_cptr a = (lds_cptr)(uintptr_t)a_wf + i * 8192;
      const u32x2 t0 = tr_read(a), t1 = tr_read(a + 512);
      wfr[i] = u32x4{t0[0], t0[1], t1[0], t1[1]};
    }
#pragma unroll
    for (int u = 0; u < 2; ++u)
#pragma unroll
      for (int k = 0; k < 8; ++k) { PIN(Al[u][k]); PIN(Ah[u][k]); A[u][k] = u32x4{Al[u][k][0], Al[u][k][1], Ah[u][k][0], Ah[u][k][1]}; }
    SB();
#pragma unroll
    for (int c = 0; c < 2; ++c)
#pragma unroll
      for (int hh = 0; hh < 2; ++hh) { hd[c][hh] = rnd2(h[c][hh] * dks[c]); PIN(hd[c][hh]); }
    f32x4 acc[8];
#pragma unroll
    for (int jj = 0; jj < 8; ++jj)
#pragma unroll
      for (int k = 0; k + jj < 8; ++k) acc[k + jj] = MFMA16H(A[0][k], B[0][jj], (jj == 0 ? f32x4{0, 0, 0, 0} : acc[k + jj]));
    SB();
    if (b < 196) {
      const __amdgpu_buffer_rsrc_t wr = __builtin_amdgcn_make_buffer_rsrc(p.ws, 0, (int)WS_X1T, 0x00020000);
      __builtin_amdgcn_raw_buffer_store_b128(wfr[0], wr, lane * 16, wo0, WT_AUX);
      __builtin_amdgcn_raw_buffer_store_b128(wfr[1], wr, lane * 16 + 1024, wo0, WT_AUX);
      __builtin_amdgcn_raw_buffer_store_b128(wfr[2], wr, lane * 16, wo1, WT_AUX);
    }
    SB();
#pragma unroll
    for (int jj = 0; jj < 8; ++jj) {
#pragma unroll
      for (int k = 0; k + jj < 8; ++k) acc[k + jj] = MFMA16H(A[1][k], B[1][jj], acc[k + jj]);
      if (jj > 0) *(__attribute__((address_space(3))) f32x4*)(uintptr_t)(a_pw + (jj - 1) * 1024) = acc[jj - 1];
      SB();
    }
    *(__attribute__((address_space(3))) f32x4*)(uintptr_t)(a_pw + 7 * 1024) = acc[7];
  }
  SST(9);
  lds_barrier();
  SST(10);
  {
    f32x4 y[2];
#pragma unroll
    for (int c = 0; c < 2; ++c) {
      lds_cptr rb = (lds_cptr)(uintptr_t)a_pr + c * 32768;
      typedef const __attribute__((address_space(3))) f32x4* lf4;
      y[c] = (*(lf4)rb + *(lf4)(rb + 8192)) + (*(lf4)(rb + 16384) + *(lf4)(rb + 24576));
    }
    if (again) {
      if (b < 196) asm volatile("s_waitcnt vmcnt(3)" : "+v"(mg) :: "memory"); else asm volatile("s_waitcnt vmcnt(0)" : "+v"(mg) :: "memory");
#pragma unroll 1
      for (unsigned spins = 0; spins < (1u << 20); ++spins) {
        if (((NE(mg[1]) | NE(mg[3])) & 12) == 0) break;
        __builtin_amdgcn_s_sleep(1);
        asm volatile("global_load_dwordx4 %0, %1, %2 sc1\n\ts_waitcnt vmcnt(0)" : "+v"(mg) : "v"(mgo), "s"(p.ws) : "memory");
      }
    }
#undef NE
    u32 o1[2][2], oc[2][2];
#pragma unroll
    for (int c = 0; c < 2; ++c) {
      const float ga = rlane(mg[2 * c], 2), c1 = rnd(rlane(mg[2 * c], 3) + 1.0f);
      const float ysc = pow2_inv(c ? kt1 : kt0) * ihs[c];
#pragma unroll
      for (int hh = 0; hh < 2; ++hh) {
        const f32x2 yy = rnd2(f32x2{y[c][2 * hh], y[c][2 * hh + 1]} * ysc);
        const f32x2 t = rnd2(yy + hd[c][hh]);
        const f32x2 xs = xf[c][hh] + rnd2(t * ga);
        o1[c][hh] = pkbf(xs.x, xs.y);
        const f32x2 xc = f32x2{bflo(o1[c][hh]), bfhi(o1[c][hh])} * c1;
        oc[c][hh] = pkbf(xc.x, xc.y);
      }
    }
    swap16(o1[0][0], o1[1][0]); swap16(o1[0][1], o1[1][1]); swap16(oc[0][0], oc[1][0]); swap16(oc[0][1], oc[1][1]);
    const __amdgpu_buffer_rsrc_t xr_ = __builtin_amdgcn_make_buffer_rsrc(p.ws, 0, (int)WS_HH, 0x00020000);
    __builtin_amdgcn_raw_buffer_store_b128(u32x4{o1[0][0], o1[0][1], o1[1][0], o1[1][1]}, xr_, oo, 0, X1_AUX);
    __builtin_amdgcn_raw_buffer_store_b128(u32x4{oc[0][0], oc[0][1], oc[1][0], oc[1][1]}, xr_, oo, (int)(WS_X1C - WS_X1T), X1_AUX);
  }
  SSTE(11); SEND(0, 14);
  if (tid == 448) { unsigned long long* e = (unsigned long long*)(p.ws + (80u << 20)) + b * 4; e[0] = ex0; e[1] = __builtin_amdgcn_s_memrealtime(); e[2] = ex7; e[3] = ex4; }
}

constexpr int H_S = 0, H_CS = 1024, H_CNT = 2944, H_PART = 3072, H_G2 = 4096, H_WG = H_G2 + 57344, H_G1 = H_WG + 57344, H_END = H_G1 + 28672;
constexpr int AB_LDS = AB_MAIN_LDS > H_END ? AB_MAIN_LDS : H_END;

__device__ __forceinline__ void helper(const Params& p, int j, char* smem) {
  const int tid = threadIdx.x, lane = tid & 63;
  const int w = __builtin_amdgcn_readfirstlane(tid >> 6);
  const int col = lane & 15, g = lane >> 4, q4 = col >> 2, pp = lane & 3;
  const unsigned lds0 = (unsigned)(uintptr_t)smem;
  const u32 tag = cld32(p.ws + WS_EP) + TAGC;
  if (tid == 448) __hip_atomic_store((gu32*)(p.ws + WS_RD) + AB_MAIN + j, tag, __ATOMIC_RELAXED, __HIP_MEMORY_SCOPE_AGENT);
  if (j >= 28) {
    if (j == 28) ((u32*)(p.ws + WS_FLG))[tid] = 0;
    if (j == 29) *(u32x4*)(p.ws + WS_STG + tid * 16) = u32x4{0, 0, 0, 0};
    if (j == 30) {
      if (tid < 256) {
        for (unsigned spins = 0; spins < (1u << 20); ++spins) {
          if (__hip_atomic_load((gu32*)(p.ws + WS_RD) + tid, __ATOMIC_RELAXED, __HIP_MEMORY_SCOPE_AGENT) == tag) break;
          __builtin_amdgcn_s_sleep(4);
        }
      }
      __syncthreads();
      if (tid == 0) *(u32*)(p.ws + WS_EP) = tag;
    }
    return;
  }
  SDECL(12); SST(0);
  const int lbase = (8 * g + q4) * 128 + 8 * pp, phi = (q4 >> 1) | ((g & 1) << 1);
  const int dchunk = (lane & 7) ^ ((((lane >> 4) & 1) | ((w & 1) << 1)) << 1);
  const int ks0 = w < 6 ? 2 * w : w + 6, nks = w < 6 ? 2 : 1;
  u32 cw;
  asm volatile("global_load_short_d16_hi %0, %1, %2" : "=v"(cw) : "v"((u32)min(32 * ks0 + lane, D - 1) * 2), "s"(p.c) : "memory");
  {
    const u32 go = (u32)((lane >> 2) * (6 * D) + ((lane & 3) ^ (((lane >> 5) & 1) << 1)) * 8) * 2;
    for (int i = 0; i < 2 * nks; ++i)
      glds16v(p.w_mod + (size_t)(32 * ks0 + 16 * i) * (6 * D) + 32 * j, go, lds0 + H_G1 + (32 * ks0 + 16 * i) * 64);
  }
  {
    const u32 go = (u32)((lane >> 3) * (6 * D) + dchunk * 8) * 2;
#pragma unroll
    for (int i = 0; i < 7; ++i) glds16v(p.w_mod + (size_t)(8 * (w + 8 * i)) * (6 * D) + 2 * D + 64 * j, go, lds0 + H_G2 + (w + 8 * i) * 1024);
  }
  if (tid == 0) *LDSP(u32, H_CNT) = 0;
  lds_barrier();
  {
    vmw<7>();
    PIN(cw);
    const float c0 = bfhi(cw);
    if (lane < 32 * nks) *LDSP(u16, H_S + (32 * ks0 + lane) * 2) = (u16)pkbf(c0 / (1.0f + expf(-c0)), 0.f);
    f32x4 a0 = {0, 0, 0, 0}, a1 = {0, 0, 0, 0};
    const int lb64 = (8 * g + q4) * 64 + 8 * pp;
    for (int s = 0; s < nks; ++s) {
      const int ks = ks0 + s;
      const u32x4 sf = *LDSP(u32x4, H_S + (32 * ks + 8 * g) * 2);
      lds_cptr a = (lds_cptr)smem + H_G1 + lb64 + ks * 2048;
      const u32x2 t0 = tr_read(a + 32 * (g & 1)), t1 = tr_read(a + 32 * (g & 1) + 256), t2 = tr_read(a + 32 * ((g & 1) ^ 1)), t3 = tr_read(a + 32 * ((g & 1) ^ 1) + 256);
      a0 = MFMA16(sf, (u32x4{t0[0], t0[1], t1[0], t1[1]}), a0);
      a1 = MFMA16(sf, (u32x4{t2[0], t2[1], t3[0], t3[1]}), a1);
    }
    if (lane < 16) { *LDSP(float, H_PART + (w * 32 + lane) * 4) = a0[0]; *LDSP(float, H_PART + (w * 32 + 16 + lane) * 4) = a1[0]; }
    asm volatile("s_waitcnt lgkmcnt(0)" ::: "memory");
    u32 old = 0;
    if (lane == 0) old = __hip_atomic_fetch_add(LDSP(u32, H_CNT), 1u, __ATOMIC_RELAXED, __HIP_MEMORY_SCOPE_WORKGROUP);
    old = __builtin_amdgcn_readfirstlane(old);
    if (old == 7 && lane < 32) {
      float t = 0.f;
#pragma unroll
      for (int u = 0; u < 8; ++u) t += *LDSP(float, H_PART + (u * 32 + lane) * 4);
      st_gran(p.ws + WS_MG, 32 * j + lane, __float_as_uint(rnd(t)), tag);
    }
  }
  SST(1);
  const u32 vofff = (u32)((lane >> 3) * F + dchunk * 8) * 2;
#pragma unroll
  for (int i = 0; i < 7; ++i) glds16v(p.w_gate + (size_t)(8 * (w + 8 * i)) * F + 64 * j, vofff, lds0 + H_WG + (w + 8 * i) * 1024);
  vmw<7>();
  lds_barrier();
  SST(2);
  if (w < 4) {
    f32x4 a = {0, 0, 0, 0};
#pragma unroll
    for (int ks = 0; ks < 14; ++ks) a = MFMA16(*LDSP(u32x4, H_S + (32 * ks + 8 * g) * 2), frag128(smem, H_G2, lbase, phi, ks, w), a);
    if (lane < 16) {
      const float t = rnd(a[0]);
      const int mi = 2 * D + 64 * j + 16 * w + lane;
      st_gran(p.ws + WS_MG, mi, __float_as_uint(t), tag);
      ((float*)(p.ws + WS_MOD))[mi] = t;
    }
  }
  lds_barrier();
  SST(3);
#pragma unroll
  for (int i = 0; i < 7; ++i) glds16v(p.w_up + (size_t)(8 * (w + 8 * i)) * F + 64 * j, vofff, lds0 + H_G2 + (w + 8 * i) * 1024);
  if (tid < D) {
    unsigned long long a, c;
    for (unsigned spins = 0; spins < (1u << 20); ++spins) {
      a = __hip_atomic_load((gu64*)(p.ws + WS_MG) + 3 * D + tid, __ATOMIC_RELAXED, __HIP_MEMORY_SCOPE_AGENT);
      c = __hip_atomic_load((gu64*)(p.ws + WS_MG) + 4 * D + tid, __ATOMIC_RELAXED, __HIP_MEMORY_SCOPE_AGENT);
      if ((u32)(a >> 32) == tag && (u32)(c >> 32) == tag) break;
      __builtin_amdgcn_s_sleep(1);
    }
    *LDSP(u16, H_CS + tid * 2) = (u16)(pkbf(1.0f + __uint_as_float((u32)c), 0.f));
    *LDSP(u16, H_CS + 896 + tid * 2) = (u16)((u32)a >> 16);
  }
  vm0();
  lds_barrier();
  SST(4);
  {
    f32x4 a = {0, 0, 0, 0};
#pragma unroll
    for (int ks = 0; ks < 14; ++ks)
      a = MFMA16(*LDSP(u32x4, H_CS + (col & 1) * 896 + (32 * ks + 8 * g) * 2), frag128(smem, w < 4 ? H_WG : H_G2, lbase, phi, ks, w & 3), a);
    if (lane < 16) {
      float* uv = (float*)(p.ws + WS_UV) + (size_t)(w >> 2) * 2 * F + 64 * j + 16 * (w & 3) + lane;
      uv[0] = a[0]; uv[F] = a[1];
    }
  }
  SSTE(5); SEND(1, 12);
}

__global__ __launch_bounds__(512) void kAB(Params p) {
  extern __shared__ __attribute__((aligned(16))) char smem[];
  asm volatile("" :: "s"(p.u), "s"(p.A_re), "s"(p.A_im), "s"(p.B_re), "s"(p.B_im), "s"(p.C_re), "s"(p.C_im), "s"(p.log_dt), "s"(p.Dskip),
               "s"(p.w_gate), "s"(p.w_up), "s"(p.w_down), "s"(p.c), "s"(p.w_mod), "s"(p.ws));
  if (__builtin_expect(blockIdx.x < AB_MAIN, 1)) conv_main(p, blockIdx.x, smem);
  else helper(p, blockIdx.x - AB_MAIN, smem);
}

constexpr int C_XT = 448 * 256, C_UV = C_XT, C_RES = 131072, CD_LDS = C_RES + 8192;

__device__ __forceinline__ void stats_role(const Params& p, int j, char* smem) {
  const int tid = threadIdx.x, lane = tid & 63, w = tid >> 6;
  const int lc = tid & 7, dr = tid >> 3;
  const char* src = p.ws + WS_X1T + (size_t)(j >> 1) * D * 256 + (j & 1) * 128 + lc * 16;
  u32x4 v[7];
#pragma unroll
  for (int i = 0; i < 7; ++i) v[i] = *(const u32x4*)(src + (dr + 64 * i) * 256);
  float s[8] = {}, q[8] = {};
#pragma unroll
  for (int i = 0; i < 7; ++i)
#pragma unroll
    for (int k = 0; k < 4; ++k) {
      const float a = bflo(v[i][k]), b = bfhi(v[i][k]);
      s[2 * k] += a; q[2 * k] = fmaf(a, a, q[2 * k]); s[2 * k + 1] += b; q[2 * k + 1] = fmaf(b, b, q[2 * k + 1]);
    }
#pragma unroll
  for (int k = 0; k < 8; ++k) {
    s[k] += dppf<0x128>(s[k]); q[k] += dppf<0x128>(q[k]);
    s[k] = xrow16_sum(s[k]); q[k] = xrow16_sum(q[k]);
  }
  float* red = (float*)smem;
  if (lane < 8) {
    *(f32x4*)(red + (w * 2 + 0) * 64 + lane * 8) = f32x4{s[0], s[1], s[2], s[3]};
    *(f32x4*)(red + (w * 2 + 0) * 64 + lane * 8 + 4) = f32x4{s[4], s[5], s[6], s[7]};
    *(f32x4*)(red + (w * 2 + 1) * 64 + lane * 8) = f32x4{q[0], q[1], q[2], q[3]};
    *(f32x4*)(red + (w * 2 + 1) * 64 + lane * 8 + 4) = f32x4{q[4], q[5], q[6], q[7]};
  }
  __syncthreads();
  if (tid < 64) {
    float ss = 0.f, qq = 0.f;
#pragma unroll
    for (int u = 0; u < 8; ++u) { ss += red[(u * 2) * 64 + tid]; qq += red[(u * 2 + 1) * 64 + tid]; }
    const float mean = ss * (1.0f / D);
    const float var = fmaxf(qq * (1.0f / D) - mean * mean, 0.f);
    const float sd = sqrtf(bfr(bfr(var) + 1e-5f));
    const u32 word = pkbf(mean, sd);
    __hip_atomic_store((gu32*)(p.ws + WS_STG) + 64 * j + tid, word, __ATOMIC_RELAXED, __HIP_MEMORY_SCOPE_AGENT);
  }
}

constexpr int C_LA = 3;
struct COp { int kind, arg; };
constexpr COp c_opA(int ks, int g) {
  if (g < 2 && ks + C_LA <= 13) return {1, 2 * (ks + C_LA) + g};
  if (ks == 0 && (g == 2 || g == 4 || g == 6)) return {2, 4 + (g - 2) / 2};
  if (ks == 1 && (g == 2 || g == 4)) return {2, 7 + (g - 2) / 2};
  if (ks == 1 && g == 6) return {5, 0};
  if (ks >= 2 && ks <= 6 && g == 2) return {2, ks + 7};
  if (ks >= 2 && ks <= 6 && g == 5) return {4, ks - 2};
  if (ks >= 7 && ks <= 10 && (g == 3 || g == 6)) return {4, 5 + 2 * (ks - 7) + (g - 3) / 3};
  if (ks >= 11 && (g & 1)) return {4, 13 + 4 * (ks - 11) + (g >> 1)};
  return {0, 0};
}
constexpr COp c_opB(int ks, int g) { return ks == 7 ? COp{3, g} : COp{0, 0}; }
struct CSched { int wait_x0, wait_w[14], wait_x3, wait_x8, wait_st; };
constexpr CSched c_make_sched() {
  CSched r{};
  int seqW[28] = {}, seqX[14] = {}, seqS = 0, n = 0;
  for (int k = 0; k < 4; ++k) seqX[k] = n++;
  for (int k = 0; k < 2 * C_LA; ++k) seqW[k] = n++;
  ++n;
  r.wait_x0 = n - 1 - seqX[3];
  for (int ks = 0; ks < 14; ++ks) {
    r.wait_w[ks] = n - 1 - seqW[2 * ks + 1];
    for (int g = 0; g < 8; ++g) {
      if (g == 4 && ks == 3) r.wait_x3 = n - 1 - seqX[8];
      if (g == 4 && ks == 8) r.wait_x8 = n - 1 - seqX[13];
      for (int u = 0; u < 2; ++u) {
        const COp o = u ? c_opB(ks, g) : c_opA(ks, g);
        if (o.kind == 1) seqW[o.arg] = n;
        if (o.kind == 2) seqX[o.arg] = n;
        if (o.kind == 3) seqS = n;
        if (o.kind) ++n;
      }
    }
  }
  r.wait_st = n - 1 - seqS;
  return r;
}
constexpr CSched C_SCHED = c_make_sched();
constexpr int C_WDM = 25;
constexpr size_t WS_END = WS_HH + (size_t)L * F * 2;

typedef __amdgpu_buffer_rsrc_t rsrc_t;
__device__ __forceinline__ void bload16(u32x4& dst, rsrc_t r, u32 voff, u32 soff, int imm) {
  asm volatile("buffer_load_dwordx4 %0, %1, %2, %3 offen offset:%4" : "=v"(dst) : "v"(voff), "s"(r), "s"(soff), "i"(imm) : "memory");
}
__device__ __forceinline__ void bload16_sc1(u32x4& dst, rsrc_t r, u32 voff, u32 soff, int imm) {
  asm volatile("buffer_load_dwordx4 %0, %1, %2, %3 offen offset:%4 sc1" : "=v"(dst) : "v"(voff), "s"(r), "s"(soff), "i"(imm) : "memory");
}
__device__ __forceinline__ void bdma16(rsrc_t r, u32 voff, u32 sbase, u32 lbase, int add) {
  u32 t;
  asm volatile("s_add_i32 m0, %4, %5\n\ts_add_i32 %0, %3, %5\n\tbuffer_load_dwordx4 %1, %2, %0 offen lds"
               : "=&s"(t) : "v"(voff), "s"(r), "s"(sbase), "s"(lbase), "i"(add) : "memory", "scc");
}

__global__ __launch_bounds__(512) void kCD(Params p) {
  extern __shared__ __attribute__((aligned(16))) char smem[];
  if (blockIdx.x >= 224) { stats_role(p, blockIdx.x - 224, smem); return; }
  const int tid = threadIdx.x, lane = tid & 63;
  const int w = __builtin_amdgcn_readfirstlane(tid >> 6);
  const int xcd = blockIdx.x & 7, idx = blockIdx.x >> 3, i7 = (idx * 37) >> 8, Q = idx - 7 * i7;
  const int Lt = 4 * (xcd >> 1) + i7, Fi = 7 * (xcd & 1) + Q, P = 2 * Lt + (xcd & 1);
  const unsigned lds0 = (unsigned)(uintptr_t)smem;
  const int g = lane >> 4, q4 = (lane & 15) >> 2, pp = lane & 3;
  const u32 l16 = lane * 16;
  const rsrc_t ws = __builtin_amdgcn_make_buffer_rsrc(p.ws, 0, (int)WS_END, 0x00020000);

  SDECL(14); SST(0);
  const unsigned long long ex0 = __builtin_amdgcn_s_memrealtime();
  unsigned long long ex2 = 0, ex6 = 0;
  u32x4 wf[28], wd[28];
  const int xrow = 4 * w + g;
  const u32 xoff = (u32)(xrow * 128 + ((lane & 15) ^ (((xrow & 3) << 2) | (((xrow >> 3) & 1) << 1))) * 8) * 2;
  const u32 sx = (u32)WS_X1C + Lt * (D * 256), sl = lds0 + w * 1024, sw = (u32)WS_WF + (Fi * 8 + w) * (28 * 1024);
  auto ld_x = [&](int ks) { bdma16(ws, xoff, sx, sl, ks * 8192); };
  auto ld_w = [&](int n) { bload16(wf[n], ws, l16, sw + (n >> 2) * 4096, (n & 3) * 1024); };
  if (w < 4) {
#pragma unroll
    for (int ks = 0; ks < 8; ++ks) bdma16(ws, xoff, sx, sl, ks * 4096);
  }
#pragma unroll
  for (int n = 0; n < 2 * C_LA; ++n) ld_w(n);
  SB();
  if (lane < 16) glds16s(p.ws + WS_UV + ((size_t)(w >> 1) * F + 128 * Fi + 64 * (w & 1)) * 4, l16, lds0 + C_UV + w * 256);
  const u32 swd = (u32)WS_WDF + (Q * 8 + w) * (28 * 1024);
  const char* stsrc = p.ws + WS_STG + Lt * 512;
  u32 stw[8];
  auto ld_wd = [&](int n) { bload16(wd[n], ws, l16, swd + (n >> 2) * 4096, (n & 3) * 1024); };
  auto ld_st = [&](int i) {
    asm volatile("global_load_dword %0, %1, %2 sc1" : "=v"(stw[i]) : "v"((u32)(lane & 15) * 4), "s"(stsrc + ((i ^ w) << 6)) : "memory");
  };
  auto issue = [&](COp o) {
    if (o.kind == 1) ld_w(o.arg);
    if (o.kind == 2) ld_x(o.arg);
    if (o.kind == 3) ld_st(o.arg);
    if (o.kind == 4) ld_wd(o.arg);
    if (o.kind == 5)
      bdma16(ws, (u32)((lane >> 3) * 128 + ((lane & 7) ^ ((((lane >> 4) & 1) | ((w & 1) << 1)) << 1)) * 8) * 2,
             (u32)WS_X1T + ((Lt * D + 64 * Q + 8 * w) * 128 + 64 * (P & 1)) * 2 - C_RES, sl, C_RES);
  };
  SST(1);
  f32x4 ag[8], au[8];
  unsigned ta[8];
  {
    const unsigned base = (unsigned)(256 * (8 * g + q4) + 32 * ((q4 << 1) | (g & 1)) + 8 * pp);
#pragma unroll
    for (int i = 0; i < 8; ++i) ta[i] = base ^ (32u * (unsigned)(i ^ w));
  }
  u32x4 xb[2][4];
  auto rd_tile = [&](int ks, int i8, u32x4& dst) {
    lds_cptr a = (lds_cptr)smem + ta[i8] + ks * 8192;
    const u32x2 t0 = tr_read(a), t1 = tr_read(a + 1024);
    dst = u32x4{t0[0], t0[1], t1[0], t1[1]};
  };
  vmwait(C_SCHED.wait_x0);
  asm volatile("s_barrier" ::: "memory");
#pragma unroll
  for (int i = 0; i < 4; ++i) rd_tile(0, i, xb[0][i]);
  u32 hso[8];
#pragma unroll
  for (int ks = 0; ks < 14; ++ks) {
    vmwait(C_SCHED.wait_w[ks]);
    PIN4(wf[2 * ks]); PIN4(wf[2 * ks + 1]);
#pragma unroll
    for (int gi = 0; gi < 8; ++gi) {
      const int h = gi >> 2, i = gi & 3;
      if (gi == 4 && (ks == 3 || ks == 8)) {
        vmwait(ks == 3 ? C_SCHED.wait_x3 : C_SCHED.wait_x8);
        asm volatile("s_barrier" ::: "memory");
      }
      if (ks == 0) { mfma_first(ag[gi], wf[0], xb[h][i]); mfma_first(au[gi], wf[1], xb[h][i]); }
      else { mfma_acc(ag[gi], wf[2 * ks], xb[h][i]); mfma_acc(au[gi], wf[2 * ks + 1], xb[h][i]); }
      if (h == 0) rd_tile(ks, 4 + i, xb[1][i]);
      else if (ks < 13) rd_tile(ks + 1, i, xb[0][i]);
      issue(c_opA(ks, gi)); issue(c_opB(ks, gi));
      if (ks == 12) { hso[gi] = __builtin_amdgcn_readfirstlane((u32)WS_HH + ((Lt * 8 + (gi ^ w)) * 56 + 4 * Fi + (w >> 1)) * 1024); PINS(hso[gi]); }
      __builtin_amdgcn_sched_barrier(0);
    }
  }
  mfma_fence();
  SST(2);
  ex2 = __builtin_amdgcn_s_memrealtime();
  vmwait(C_SCHED.wait_st);
  for (unsigned spins = 0; spins < (1u << 16); ++spins) {
    u32 mn = 0xffffffffu;
#pragma unroll
    for (int i = 0; i < 8; ++i) { asm volatile("" : "+v"(stw[i])); mn = min(mn, stw[i]); }
    if (__all(mn >= 0x10000u)) break;
#pragma unroll
    for (int i = 0; i < 8; ++i) ld_st(i);
    vmwait(0);
  }
  SST(3);
  {
    const float* uvl = (const float*)(smem + C_UV) + 16 * w + 4 * g;
    const f32x4 ug = *(const f32x4*)(uvl), uu = *(const f32x4*)(uvl + 256), vu = *(const f32x4*)(uvl + 384);
    const f32x4 vg = *(const f32x4*)(uvl + 128) * -1.4426950408889634f;
    const u32 hoff = (u32)((16 * (2 * (w & 1) + (g >> 1)) + (lane & 15)) * 16 + 8 * (g & 1));
#pragma unroll
    for (int i = 0; i < 8; ++i) {
      const float m = bflo(stw[i]), rs = __builtin_amdgcn_rcpf(bfhi(stw[i]));
      const float rsg = rs * -1.4426950408889634f;
      f32x4 gv = (ag[i] - m * ug) * rsg + vg;
      f32x4 uv = (au[i] - m * uu) * rs + vu;
      f32x4 h;
#pragma unroll
      for (int r = 0; r < 4; ++r) h[r] = __builtin_amdgcn_rcpf(1.0f + __builtin_amdgcn_exp2f(gv[r])) * uv[r];
      const u32x2 o = {pkbf(h[0], h[1]), pkbf(h[2], h[3])};
      asm volatile("global_store_dwordx2 %0, %1, %2 sc1" :: "v"(hoff + hso[i]), "v"(o), "s"(p.ws) : "memory");
    }
  }
#pragma unroll
  for (int n = C_WDM; n < 28; ++n) ld_wd(n);
  SST(4);
  vmwait(28 - C_WDM);
  asm volatile("s_barrier" ::: "memory");
  SST(5);
  gu32* cnt = (gu32*)(p.ws + WS_FLG) + Lt * 32;
  if (tid == 0) __hip_atomic_fetch_add(cnt, 1u, __ATOMIC_RELAXED, __HIP_MEMORY_SCOPE_AGENT);
  if (w == 0) {
    for (unsigned spins = 0; spins < (1u << 20); ++spins) {
      if (__hip_atomic_load(cnt, __ATOMIC_RELAXED, __HIP_MEMORY_SCOPE_AGENT) >= 14u) break;
      __builtin_amdgcn_s_sleep(1);
    }
  }
  asm volatile("s_barrier" ::: "memory");
  SST(6);
  ex6 = __builtin_amdgcn_s_memrealtime();
  u32x4 hb[4][7];
  const u32 sh = (u32)WS_HH + (4 * P * 56 + 7 * w) * 1024;
  auto ld_il = [&](int il, u32x4 (&dst)[7]) {
#pragma unroll
    for (int j = 0; j < 7; ++j) bload16_sc1(dst[j], ws, l16, sh + il * 56 * 1024 + (j >> 2) * 4096, (j & 3) * 1024);
  };
  ld_il(0, hb[0]);
  asm volatile("s_barrier" ::: "memory");
  ld_il(1, hb[1]);
  asm volatile("s_barrier" ::: "memory");
  ld_il(2, hb[2]);
  asm volatile("s_barrier" ::: "memory");
  ld_il(3, hb[3]);
#pragma unroll
  for (int n = 0; n < 28; ++n) PIN4(wd[n]);
  const int dcol = 64 * Q + 32 * (w >> 2) + 8 * g;
  u32x4 gb0, gb1;
#pragma unroll
  for (int il = 0; il < 4; ++il) {
    f32x4 acc[4];
#pragma unroll
    for (int j = 0; j < 7; ++j) {
      vmwait(7 * (3 - il) + 6 - j + (il >= 2 ? 2 : 0));
      PIN4(hb[il][j]);
      if (il == 0 && j == 0) SST(13);
#pragma unroll
      for (int id = 0; id < 4; ++id) { if (j == 0) mfma_first(acc[id], wd[id * 7], hb[il][0]); else mfma_acc(acc[id], wd[id * 7 + j], hb[il][j]); }
      __builtin_amdgcn_sched_barrier(0);
    }
    if (il == 1) {
      bload16(gb0, ws, (u32)dcol * 4, (u32)WS_MOD + 5 * D * 4, 0);
      bload16(gb1, ws, (u32)dcol * 4, (u32)WS_MOD + 5 * D * 4, 16);
    }
    if (il == 0) SST(9);
    if (il == 1) SST(10);
    if (il == 2) SST(11);
    if (il == 3) SST(12);
    mfma_fence();
#pragma unroll
    for (int id = 0; id < 4; ++id) *(f32x4*)(smem + ((w * 4 + il) * 4 + id) * 1024 + l16) = acc[id];
    __builtin_amdgcn_sched_barrier(0);
  }
  vmwait(0);
  SST(7);
  __syncthreads();
  {
    f32x4 o0 = {0, 0, 0, 0}, o1 = {0, 0, 0, 0};
    const char* rb = smem + ((w & 3) * 4 + 2 * (w >> 2) + (g >> 1)) * 1024 + (32 * (g & 1) + (lane & 15)) * 16;
#pragma unroll
    for (int s = 0; s < 8; ++s) { o0 += *(const f32x4*)(rb + s * 16384); o1 += *(const f32x4*)(rb + s * 16384 + 256); }
    const u32x4 xres = frag128(smem, C_RES, (8 * g + q4) * 128 + 8 * pp, (q4 >> 1) | ((g & 1) << 1), w >> 2, w & 3);
    PIN4(gb0); PIN4(gb1);
    const f32x4 g0 = __builtin_bit_cast(f32x4, gb0), g1 = __builtin_bit_cast(f32x4, gb1);
    float r[8];
#pragma unroll
    for (int k = 0; k < 4; ++k) { r[k] = bfr(g0[k] * bfr(o0[k])); r[4 + k] = bfr(g1[k] * bfr(o1[k])); }
    u32x4 o;
#pragma unroll
    for (int k = 0; k < 4; ++k) o[k] = pkbf(bflo(xres[k]) + r[2 * k], bfhi(xres[k]) + r[2 * k + 1]);
    const int lrow = 64 * P + 16 * (w & 3) + (lane & 15);
    *(u32x4*)(p.out + (size_t)lrow * D + dcol) = o;
  }
  SSTE(8); SEND(2, 14);
  if (blockIdx.x == 0 && w == 0) {
    const unsigned long long t1 = __builtin_amdgcn_s_memrealtime();
    const unsigned long long* e = (const unsigned long long*)(p.ws + (80u << 20));
    unsigned a0 = 0, a1 = 0xffffffffu, a7 = 0xffffffffu, a4 = 0xffffffffu;
    for (int i = lane; i < 224; i += 64) { a0 = max(a0, (unsigned)(t1 - e[4 * i])); a1 = min(a1, (unsigned)(t1 - e[4 * i + 1]));
      a7 = min(a7, (unsigned)(t1 - e[4 * i + 2])); a4 = min(a4, (unsigned)(t1 - e[4 * i + 3])); }
    for (int o = 32; o; o >>= 1) { a0 = max(a0, (unsigned)__shfl_xor((int)a0, o)); a1 = min(a1, (unsigned)__shfl_xor((int)a1, o));
      a7 = min(a7, (unsigned)__shfl_xor((int)a7, o)); a4 = min(a4, (unsigned)__shfl_xor((int)a4, o)); }
    const unsigned cdur = (unsigned)(t1 - ex0);
    unsigned q = EXPORT == 1 ? a0 : EXPORT == 2 ? a0 - a1 : EXPORT == 3 ? cdur : EXPORT == 4 ? a1 - cdur : EXPORT == 5 ? a0 - a7 : EXPORT == 6 ? a0 - a4
               : EXPORT == 7 ? (unsigned)(ex2 - ex0) : EXPORT == 8 ? (unsigned)(ex6 - ex0) : 0;
    q = min(q, 10000u);
    q = __builtin_amdgcn_readfirstlane(q);
    while (__builtin_amdgcn_s_memrealtime() - t1 < 2ull * q) __builtin_amdgcn_s_sleep(1);
  }
}

extern "C" void kernel_launch(void* const* d_in, const int* in_sizes, int n_in, void* d_out, int out_size, void* d_ws,
                              size_t ws_size, hipStream_t stream) {
  Params p;
  p.u = (const u16*)d_in[0]; p.A_re = (const u16*)d_in[1]; p.A_im = (const u16*)d_in[2]; p.B_re = (const u16*)d_in[3];
  p.B_im = (const u16*)d_in[4]; p.C_re = (const u16*)d_in[5]; p.C_im = (const u16*)d_in[6]; p.log_dt = (const u16*)d_in[7];
  p.Dskip = (const u16*)d_in[8]; p.w_gate = (const u16*)d_in[10]; p.w_up = (const u16*)d_in[11]; p.w_down = (const u16*)d_in[12];
  p.c = (const u16*)d_in[13]; p.w_mod = (const u16*)d_in[14];
  p.ws = (char*)d_ws; p.out = (u16*)d_out;
  hipFuncSetAttribute((const void*)kAB, hipFuncAttributeMaxDynamicSharedMemorySize, AB_LDS);
  hipFuncSetAttribute((const void*)kCD, hipFuncAttributeMaxDynamicSharedMemorySize, CD_LDS);
  kAB<<<256, 512, AB_LDS, stream>>>(p);
  kCD<<<256, 512, CD_LDS, stream>>>(p);
}
```

```cpp
#include <hip/hip_runtime.h>
#include <hip/hip_bf16.h>
#include <stdint.h>

typedef unsigned short u16;
typedef unsigned int u32;
typedef __attribute__((ext_vector_type(8))) short s16x8;
typedef __attribute__((ext_vector_type(4))) short s16x4;
typedef __attribute__((ext_vector_type(8))) _Float16 f16x8;
typedef __attribute__((ext_vector_type(2))) _Float16 f16x2;
typedef __attribute__((ext_vector_type(2))) __bf16 bf16x2;
typedef __attribute__((ext_vector_type(8))) __bf16 bf16x8;
typedef __attribute__((ext_vector_type(2))) float f32x2;
typedef __attribute__((ext_vector_type(4))) float f32x4;
typedef __attribute__((ext_vector_type(16))) float f32x16;
typedef __attribute__((ext_vector_type(2))) unsigned u32x2;
typedef __attribute__((ext_vector_type(4))) unsigned u32x4;
typedef __attribute__((address_space(3))) char* lds_ptr;
typedef __attribute__((address_space(3))) const char* lds_cptr;

constexpr int L = 2048, D = 448, NS = 256, F = 1792;

constexpr size_t WS_EP = 0;
constexpr size_t WS_RD = 2048;
constexpr size_t WS_MG = 4096;
constexpr size_t WS_SG = 32768;
constexpr size_t WS_MOD = 49152;
constexpr size_t WS_FLG = 61440;
constexpr size_t WS_UV = 65536;
constexpr size_t WS_STG = 98304;
constexpr size_t WS_WDF = 131072;
constexpr size_t WS_WF = WS_WDF + (size_t)F * D * 2;
constexpr size_t WS_X1T = WS_WF + (size_t)2 * F * D * 2;
constexpr size_t WS_X1C = WS_X1T + (size_t)D * L * 2;
constexpr size_t WS_HH = WS_X1C + (size_t)D * L * 2;

struct Params {
  const u16* u; const u16* A_re; const u16* A_im; const u16* B_re; const u16* B_im; const u16* C_re; const u16* C_im;
  const u16* log_dt; const u16* Dskip; const u16* w_gate; const u16* w_up; const u16* w_down; const u16* c; const u16* w_mod;
  char* ws; u16* out;
};

#define SDECL(n) do {} while (0)
#define SST(i) do {} while (0)
#define SSTE(i) do {} while (0)
#define SEND(kid, n, bid) do {} while (0)
__device__ __forceinline__ float bf2f(u16 v) { return __uint_as_float((u32)v << 16); }
__device__ __forceinline__ float bflo(u32 v) { return __uint_as_float(v << 16); }
__device__ __forceinline__ float bfhi(u32 v) { return __uint_as_float(v & 0xffff0000u); }
__device__ __forceinline__ u32 pkbf(float lo, float hi) {
  f32x2 v = {lo, hi}; bf16x2 b = __builtin_convertvector(v, bf16x2); return __builtin_bit_cast(u32, b);
}
__device__ __forceinline__ float bfr(float x) { return bflo(pkbf(x, x)); }
__device__ __forceinline__ u32 pkh(float lo, float hi) {
  f32x2 v = {lo, hi}; f16x2 b = __builtin_convertvector(v, f16x2); return __builtin_bit_cast(u32, b);
}
template <int CTRL> __device__ __forceinline__ float dppf(float x) {
  return __builtin_bit_cast(float, __builtin_amdgcn_mov_dpp(__builtin_bit_cast(int, x), CTRL, 0xf, 0xf, true));
}
__device__ __forceinline__ float xrow16_sum(float x) {
  auto s = __builtin_amdgcn_permlane16_swap(__float_as_uint(x), __float_as_uint(x), false, false);
  x = __uint_as_float(s[0]) + __uint_as_float(s[1]);
  auto t = __builtin_amdgcn_permlane32_swap(__float_as_uint(x), __float_as_uint(x), false, false);
  return __uint_as_float(t[0]) + __uint_as_float(t[1]);
}
__device__ __forceinline__ float xrow16_max(float x) {
  auto s = __builtin_amdgcn_permlane16_swap(__float_as_uint(x), __float_as_uint(x), false, false);
  x = fmaxf(__uint_as_float(s[0]), __uint_as_float(s[1]));
  auto t = __builtin_amdgcn_permlane32_swap(__float_as_uint(x), __float_as_uint(x), false, false);
  return fmaxf(__uint_as_float(t[0]), __uint_as_float(t[1]));
}
__device__ __forceinline__ float wave_sum(float x) {
  x += dppf<0xB1>(x); x += dppf<0x4E>(x); x += dppf<0x141>(x); x += dppf<0x128>(x);
  return xrow16_sum(x);
}
__device__ __forceinline__ float wave_max(float x) {
  x = fmaxf(x, dppf<0xB1>(x)); x = fmaxf(x, dppf<0x4E>(x)); x = fmaxf(x, dppf<0x141>(x)); x = fmaxf(x, dppf<0x128>(x));
  return xrow16_max(x);
}
__device__ __forceinline__ float dot2bf(u32 a, u32 b, float acc) {
  return __builtin_amdgcn_fdot2_f32_bf16(__builtin_bit_cast(bf16x2, a), __builtin_bit_cast(bf16x2, b), acc, false);
}
typedef short v4i16_t __attribute__((ext_vector_type(4)));
__device__ __forceinline__ u32x2 tr_read(lds_cptr p) {
  return __builtin_bit_cast(u32x2, __builtin_amdgcn_ds_read_tr16_b64_v4i16((__attribute__((address_space(3))) v4i16_t*)p));
}
__device__ __forceinline__ void glds16(const void* g, unsigned lds_base) {
  unsigned sv;
  asm volatile("s_mov_b32 %0, m0\n\ts_mov_b32 m0, %2\n\ts_nop 0\n\tglobal_load_lds_dwordx4 %1, off\n\ts_mov_b32 m0, %0"
               : "=&s"(sv) : "v"(g), "s"(lds_base) : "memory");
}
__device__ __forceinline__ float pow2_scale(float x, int target) {
  const int e = (int)(__float_as_uint(x) >> 23);
  return __uint_as_float((u32)(127 + min(max(target + 127 - e, -100), 100)) << 23);
}
#define WT_AUX 16
__device__ __forceinline__ void gload16(u32x4& dst, const void* sbase, u32 voff, int imm) {
  asm volatile("global_load_dwordx4 %0, %1, %2 offset:%3" : "=v"(dst) : "v"(voff), "s"(sbase), "i"(imm) : "memory");
}
__device__ __forceinline__ void gload16_sc1(u32x4& dst, const void* sbase, u32 voff, int imm) {
  asm volatile("global_load_dwordx4 %0, %1, %2 offset:%3 sc1" : "=v"(dst) : "v"(voff), "s"(sbase), "i"(imm) : "memory");
}
__device__ __forceinline__ void glds16s(const void* sbase, u32 voff, unsigned lds_base) {
  asm volatile("s_mov_b32 m0, %2\n\ts_nop 0\n\tglobal_load_lds_dwordx4 %0, %1" :: "v"(voff), "s"(sbase), "s"(lds_base) : "memory");
}
#define MFMA16(a, b, c) __builtin_amdgcn_mfma_f32_16x16x32_bf16(__builtin_bit_cast(bf16x8, a), __builtin_bit_cast(bf16x8, b), c, 0, 0, 0)
__device__ __forceinline__ void mfma_acc(f32x4& c, const u32x4& a, const u32x4& b) {
  asm volatile("v_mfma_f32_16x16x32_bf16 %0, %1, %2, %0" : "+v"(c) : "v"(a), "v"(b));
}
__device__ __forceinline__ void mfma_first(f32x4& c, const u32x4& a, const u32x4& b) {
  asm volatile("v_mfma_f32_16x16x32_bf16 %0, %1, %2, 0" : "=&v"(c) : "v"(a), "v"(b));
}
__device__ __forceinline__ void mfma_fence() { asm volatile("s_nop 7\n\ts_nop 7" ::: "memory"); }
#define VMWAIT(n) case n: asm volatile("s_waitcnt vmcnt(" #n ")" ::: "memory"); break
__device__ __forceinline__ void vmwait(int n) {
  switch (n) {
    VMWAIT(0); VMWAIT(1); VMWAIT(2); VMWAIT(3); VMWAIT(4); VMWAIT(5); VMWAIT(6); VMWAIT(7); VMWAIT(8); VMWAIT(9);
    VMWAIT(10); VMWAIT(11); VMWAIT(12); VMWAIT(13); VMWAIT(14); VMWAIT(15); VMWAIT(16); VMWAIT(17); VMWAIT(18); VMWAIT(19);
    VMWAIT(20); VMWAIT(21); VMWAIT(22); VMWAIT(23); VMWAIT(24); VMWAIT(25); VMWAIT(26); VMWAIT(27); VMWAIT(28); VMWAIT(29);
    VMWAIT(30); VMWAIT(31); VMWAIT(32); VMWAIT(33); VMWAIT(34); VMWAIT(35); VMWAIT(36); VMWAIT(37); VMWAIT(38); VMWAIT(39);
    default: asm volatile("s_waitcnt vmcnt(0)" ::: "memory"); break;
  }
}
#define PIN4(x) asm volatile("" : "+v"(x))

typedef __attribute__((address_space(1))) unsigned gu32;
typedef __attribute__((address_space(1))) unsigned long long gu64;
constexpr u32 TAGC = 0x9E3779B9u;
constexpr int AB_MAIN = 224;
constexpr int T_RS = 1056, T_P = 32 * T_RS, T_CH = 53 * T_RS;
constexpr int L_R = 65536, R_CP = 4160, R_CH = 4 * R_CP;
constexpr int L_W = 2 * T_CH;
constexpr int L_KT = L_W + 24576, KT_Z = 2048, KT_CH = 2064;
constexpr int L_H = L_KT + 2 * KT_CH, H_CH = 4608;
constexpr int L_SC = L_H + 2 * H_CH, AB_MAIN_LDS = L_SC + 64;

constexpr u32 kt_pos(int l) {
  if (l < 0) return KT_Z;
  int v = l;
  if (l >= 256) {
    const int s = l >= 1024 ? 3 : (l >= 512 ? 2 : 1), rem = l & ((1 << s) - 1), half = 1 << (s - 1);
    int q = l >> s;
    if (rem > half || (rem == half && (q & 1))) ++q;
    v = q << s;
  }
  int rr = 20, q = 0;
  if (v < 256) { rr = v & 7; q = v >> 3; }
  else if (v < 512) { rr = 8 + ((v >> 1) & 3); q = (v - 256) >> 3; }
  else if (v < 1024) { rr = 12 + 2 * ((v - 512) >> 8) + ((v >> 2) & 1); q = (v & 255) >> 3; }
  else if (v < 2048) { rr = 16 + ((v - 1024) >> 8); q = (v & 255) >> 3; }
  return (u32)(((rr >> 4) + 2 * (q >> 4)) * 512 + ((q & 15) + 16 * ((rr & 15) >> 2)) * 8 + 2 * (rr & 3));
}
struct GTab { u32 v[2][3][256][4]; };
constexpr GTab make_gtab() {
  GTab t{};
  for (int c = 0; c < 2; ++c)
    for (int i = 0; i < 256; ++i)
      for (int j = 0; j < 12; ++j) t.v[c][j >> 2][i][j & 3] = (u32)(L_KT + c * KT_CH) + kt_pos(2047 - 8 * i - j);
  return t;
}
static __device__ const GTab gtab = make_gtab();

__device__ __forceinline__ float rnd(float x) { return __uint_as_float(pkbf(0.f, x)); }
__device__ __forceinline__ f32x2 rnd2(f32x2 v) { return f32x2{rnd(v.x), rnd(v.y)}; }
__device__ __forceinline__ u32 pkh2(f32x2 v) { return __builtin_bit_cast(u32, __builtin_convertvector(v, f16x2)); }
struct cm { f32x2 v, n; };
__device__ __forceinline__ cm mkcm(f32x2 b) { return {b, f32x2{-b.y, b.x}}; }
__device__ __forceinline__ f32x2 cmul(f32x2 a, const cm& b) { return __builtin_elementwise_fma(a.yy, b.n, a.xx * b.v); }
__device__ __forceinline__ void cmul4(f32x2& o0, f32x2& o1, f32x2& o2, f32x2& o3, f32x2 a0, const cm& m0, f32x2 a1, const cm& m1, f32x2 a2, const cm& m2,
                                      f32x2 a3, const cm& m3) {
  f32x2 t0 = a0.xx * m0.v, t1 = a1.xx * m1.v, t2 = a2.xx * m2.v, t3 = a3.xx * m3.v;
  asm volatile("" : "+v"(t0), "+v"(t1), "+v"(t2), "+v"(t3));
  t0 = __builtin_elementwise_fma(a0.yy, m0.n, t0); t1 = __builtin_elementwise_fma(a1.yy, m1.n, t1);
  t2 = __builtin_elementwise_fma(a2.yy, m2.n, t2); t3 = __builtin_elementwise_fma(a3.yy, m3.n, t3);
  asm volatile("" : "+v"(t0), "+v"(t1), "+v"(t2), "+v"(t3));
  o0 = t0; o1 = t1; o2 = t2; o3 = t3;
}
__device__ __forceinline__ f32x2 cexpm(float zr, float zi, float m) {
  const float mag = __builtin_amdgcn_exp2f(zr * (m * 1.4426950408889634f));
  const float rev = __builtin_amdgcn_fractf(zi * (m * 0.15915494309189535f));
  return f32x2{mag * __builtin_amdgcn_cosf(rev), mag * __builtin_amdgcn_sinf(rev)};
}
__device__ __forceinline__ void st_gran(char* base, int idx, u32 val, u32 tag) {
  __hip_atomic_store((gu64*)base + idx, ((unsigned long long)tag << 32) | val, __ATOMIC_RELAXED, __HIP_MEMORY_SCOPE_AGENT);
}
template <int O0, int O1, int O2, int O3>
__device__ __forceinline__ void wr_rows4(u32 a, u32 b, u32 c, u32 d) {
  asm volatile("ds_write_addtid_b32 %0 offset:%4\n\tds_write_addtid_b32 %1 offset:%5\n\t"
               "ds_write_addtid_b32 %2 offset:%6\n\tds_write_addtid_b32 %3 offset:%7"
               :: "v"(a), "v"(b), "v"(c), "v"(d), "i"(O0), "i"(O1), "i"(O2), "i"(O3) : "memory");
}
template <int O0>
__device__ __forceinline__ void wr_rows1(u32 a) {
  asm volatile("ds_write_addtid_b32 %0 offset:%1" :: "v"(a), "i"(O0) : "memory");
}
__device__ __forceinline__ void table_q(f32x2 E8) {
  asm volatile("v_pk_mul_f32 v[232:233], v[226:227], v[226:227] op_sel_hi:[0,1]\n\tv_mov_b32 v240, 0x3c00\n\tv_cvt_pk_f16_f32 v241, v226, v227\n\tv_pk_fma_f32 v[228:229], v[226:227], v[226:227], v[232:233] op_sel:[1,1,0] op_sel_hi:[1,0,1] neg_lo:[0,1,0]\n\tds_write_addtid_b32 v240 offset:0\n\tds_write_addtid_b32 v241 offset:1056\n\tv_pk_mul_f32 v[232:233], v[228:229], v[226:227] op_sel_hi:[0,1]\n\tv_pk_mul_f32 v[234:235], v[228:229], v[228:229] op_sel_hi:[0,1]\n\tv_cvt_pk_f16_f32 v242, v228, v229\n\tv_pk_fma_f32 v[230:231], v[228:229], v[226:227], v[232:233] op_sel:[1,1,0] op_sel_hi:[1,0,1] neg_lo:[0,1,0]\n\tv_pk_fma_f32 v[244:245], v[228:229], v[228:229], v[234:235] op_sel:[1,1,0] op_sel_hi:[1,0,1] neg_lo:[0,1,0]\n\tds_write_addtid_b32 v242 offset:2112\n\tv_cvt_pk_f16_f32 v243, v230, v231\n\tv_pk_mul_f32 v[234:235], v[226:227], v[244:245] op_sel_hi:[0,1]\n\tv_pk_mul_f32 v[236:237], v[228:229], v[244:245] op_sel_hi:[0,1]\n\tv_pk_mul_f32 v[238:239], v[230:231], v[244:245] op_sel_hi:[0,1]\n\tds_write_addtid_b32 v243 offset:3168\n\tv_mov_b64 v[224:225], v[244:245]\n\tv_pk_fma_f32 v[226:227], v[226:227], v[244:245], v[234:235] op_sel:[1,1,0] op_sel_hi:[1,0,1] neg_lo:[0,1,0]\n\tv_pk_fma_f32 v[228:229], v[228:229], v[244:245], v[236:237] op_sel:[1,1,0] op_sel_hi:[1,0,1] neg_lo:[0,1,0]\n\tv_pk_fma_f32 v[230:231], v[230:231], v[244:245], v[238:239] op_sel:[1,1,0] op_sel_hi:[1,0,1] neg_lo:[0,1,0]\n\tv_cvt_pk_f16_f32 v240, v224, v225\n\tv_cvt_pk_f16_f32 v241, v226, v227\n\tv_cvt_pk_f16_f32 v242, v228, v229\n\tv_cvt_pk_f16_f32 v243, v230, v231\n\tv_pk_mul_f32 v[232:233], v[224:225], v[244:245] op_sel_hi:[0,1]\n\tv_pk_mul_f32 v[234:235], v[226:227], v[244:245] op_sel_hi:[0,1]\n\tv_pk_mul_f32 v[236:237], v[228:229], v[244:245] op_sel_hi:[0,1]\n\tv_pk_mul_f32 v[238:239], v[230:231], v[244:245] op_sel_hi:[0,1]\n\tds_write_addtid_b32 v240 offset:4224\n\tds_write_addtid_b32 v241 offset:5280\n\tds_write_addtid_b32 v242 offset:6336\n\tds_write_addtid_b32 v243 offset:7392\n\tv_pk_fma_f32 v[224:225], v[224:225], v[244:245], v[232:233] op_sel:[1,1,0] op_sel_hi:[1,0,1] neg_lo:[0,1,0]\n\tv_pk_fma_f32 v[226:227], v[226:227], v[244:245], v[234:235] op_sel:[1,1,0] op_sel_hi:[1,0,1] neg_lo:[0,1,0]\n\tv_pk_fma_f32 v[228:229], v[228:229], v[244:245], v[236:237] op_sel:[1,1,0] op_sel_hi:[1,0,1] neg_lo:[0,1,0]\n\tv_pk_fma_f32 v[230:231], v[230:231], v[244:245], v[238:239] op_sel:[1,1,0] op_sel_hi:[1,0,1] neg_lo:[0,1,0]\n\tv_cvt_pk_f16_f32 v240, v224, v225\n\tv_cvt_pk_f16_f32 v241, v226, v227\n\tv_cvt_pk_f16_f32 v242, v228, v229\n\tv_cvt_pk_f16_f32 v243, v230, v231\n\tv_pk_mul_f32 v[232:233], v[224:225], v[244:245] op_sel_hi:[0,1]\n\tv_pk_mul_f32 v[234:235], v[226:227], v[244:245] op_sel_hi:[0,1]\n\tv_pk_mul_f32 v[236:237], v[228:229], v[244:245] op_sel_hi:[0,1]\n\tv_pk_mul_f32 v[238:239], v[230:231], v[244:245] op_sel_hi:[0,1]\n\tds_write_addtid_b32 v240 offset:8448\n\tds_write_addtid_b32 v241 offset:9504\n\tds_write_addtid_b32 v242 offset:10560\n\tds_write_addtid_b32 v243 offset:11616\n\tv_pk_fma_f32 v[224:225], v[224:225], v[244:245], v[232:233] op_sel:[1,1,0] op_sel_hi:[1,0,1] neg_lo:[0,1,0]\n\tv_pk_fma_f32 v[226:227], v[226:227], v[244:245], v[234:235] op_sel:[1,1,0] op_sel_hi:[1,0,1] neg_lo:[0,1,0]\n\tv_pk_fma_f32 v[228:229], v[228:229], v[244:245], v[236:237] op_sel:[1,1,0] op_sel_hi:[1,0,1] neg_lo:[0,1,0]\n\tv_pk_fma_f32 v[230:231], v[230:231], v[244:245], v[238:239] op_sel:[1,1,0] op_sel_hi:[1,0,1] neg_lo:[0,1,0]\n\tv_cvt_pk_f16_f32 v240, v224, v225\n\tv_cvt_pk_f16_f32 v241, v226, v227\n\tv_cvt_pk_f16_f32 v242, v228, v229\n\tv_cvt_pk_f16_f32 v243, v230, v231\n\tv_pk_mul_f32 v[232:233], v[224:225], v[244:245] op_sel_hi:[0,1]\n\tv_pk_mul_f32 v[234:235], v[226:227], v[244:245] op_sel_hi:[0,1]\n\tv_pk_mul_f32 v[236:237], v[228:229], v[244:245] op_sel_hi:[0,1]\n\tv_pk_mul_f32 v[238:239], v[230:231], v[244:245] op_sel_hi:[0,1]\n\tds_write_addtid_b32 v240 offset:12672\n\tds_write_addtid_b32 v241 offset:13728\n\tds_write_addtid_b32 v242 offset:14784\n\tds_write_addtid_b32 v243 offset:15840\n\tv_pk_fma_f32 v[224:225], v[224:225], v[244:245], v[232:233] op_sel:[1,1,0] op_sel_hi:[1,0,1] neg_lo:[0,1,0]\n\tv_pk_fma_f32 v[226:227], v[226:227], v[244:245], v[234:235] op_sel:[1,1,0] op_sel_hi:[1,0,1] neg_lo:[0,1,0]\n\tv_pk_fma_f32 v[228:229], v[228:229], v[244:245], v[236:237] op_sel:[1,1,0] op_sel_hi:[1,0,1] neg_lo:[0,1,0]\n\tv_pk_fma_f32 v[230:231], v[230:231], v[244:245], v[238:239] op_sel:[1,1,0] op_sel_hi:[1,0,1] neg_lo:[0,1,0]\n\tv_cvt_pk_f16_f32 v240, v224, v225\n\tv_cvt_pk_f16_f32 v241, v226, v227\n\tv_cvt_pk_f16_f32 v242, v228, v229\n\tv_cvt_pk_f16_f32 v243, v230, v231\n\tv_pk_mul_f32 v[232:233], v[224:225], v[244:245] op_sel_hi:[0,1]\n\tv_pk_mul_f32 v[234:235], v[226:227], v[244:245] op_sel_hi:[0,1]\n\tv_pk_mul_f32 v[236:237], v[228:229], v[244:245] op_sel_hi:[0,1]\n\tv_pk_mul_f32 v[238:239], v[230:231], v[244:245] op_sel_hi:[0,1]\n\tds_write_addtid_b32 v240 offset:16896\n\tds_write_addtid_b32 v241 offset:17952\n\tds_write_addtid_b32 v242 offset:19008\n\tds_write_addtid_b32 v243 offset:20064\n\tv_pk_fma_f32 v[224:225], v[224:225], v[244:245], v[232:233] op_sel:[1,1,0] op_sel_hi:[1,0,1] neg_lo:[0,1,0]\n\tv_pk_fma_f32 v[226:227], v[226:227], v[244:245], v[234:235] op_sel:[1,1,0] op_sel_hi:[1,0,1] neg_lo:[0,1,0]\n\tv_pk_fma_f32 v[228:229], v[228:229], v[244:245], v[236:237] op_sel:[1,1,0] op_sel_hi:[1,0,1] neg_lo:[0,1,0]\n\tv_pk_fma_f32 v[230:231], v[230:231], v[244:245], v[238:239] op_sel:[1,1,0] op_sel_hi:[1,0,1] neg_lo:[0,1,0]\n\tv_cvt_pk_f16_f32 v240, v224, v225\n\tv_cvt_pk_f16_f32 v241, v226, v227\n\tv_cvt_pk_f16_f32 v242, v228, v229\n\tv_cvt_pk_f16_f32 v243, v230, v231\n\tv_pk_mul_f32 v[232:233], v[224:225], v[244:245] op_sel_hi:[0,1]\n\tv_pk_mul_f32 v[234:235], v[226:227], v[244:245] op_sel_hi:[0,1]\n\tv_pk_mul_f32 v[236:237], v[228:229], v[244:245] op_sel_hi:[0,1]\n\tv_pk_mul_f32 v[238:239], v[230:231], v[244:245] op_sel_hi:[0,1]\n\tds_write_addtid_b32 v240 offset:21120\n\tds_write_addtid_b32 v241 offset:22176\n\tds_write_addtid_b32 v242 offset:23232\n\tds_write_addtid_b32 v243 offset:24288\n\tv_pk_fma_f32 v[224:225], v[224:225], v[244:245], v[232:233] op_sel:[1,1,0] op_sel_hi:[1,0,1] neg_lo:[0,1,0]\n\tv_pk_fma_f32 v[226:227], v[226:227], v[244:245], v[234:235] op_sel:[1,1,0] op_sel_hi:[1,0,1] neg_lo:[0,1,0]\n\tv_pk_fma_f32 v[228:229], v[228:229], v[244:245], v[236:237] op_sel:[1,1,0] op_sel_hi:[1,0,1] neg_lo:[0,1,0]\n\tv_pk_fma_f32 v[230:231], v[230:231], v[244:245], v[238:239] op_sel:[1,1,0] op_sel_hi:[1,0,1] neg_lo:[0,1,0]\n\tv_cvt_pk_f16_f32 v240, v224, v225\n\tv_cvt_pk_f16_f32 v241, v226, v227\n\tv_cvt_pk_f16_f32 v242, v228, v229\n\tv_cvt_pk_f16_f32 v243, v230, v231\n\tv_pk_mul_f32 v[232:233], v[224:225], v[244:245] op_sel_hi:[0,1]\n\tv_pk_mul_f32 v[234:235], v[226:227], v[244:245] op_sel_hi:[0,1]\n\tv_pk_mul_f32 v[236:237], v[228:229], v[244:245] op_sel_hi:[0,1]\n\tv_pk_mul_f32 v[238:239], v[230:231], v[244:245] op_sel_hi:[0,1]\n\tds_write_addtid_b32 v240 offset:25344\n\tds_write_addtid_b32 v241 offset:26400\n\tds_write_addtid_b32 v242 offset:27456\n\tds_write_addtid_b32 v243 offset:28512\n\tv_pk_fma_f32 v[224:225], v[224:225], v[244:245], v[232:233] op_sel:[1,1,0] op_sel_hi:[1,0,1] neg_lo:[0,1,0]\n\tv_pk_fma_f32 v[226:227], v[226:227], v[244:245], v[234:235] op_sel:[1,1,0] op_sel_hi:[1,0,1] neg_lo:[0,1,0]\n\tv_pk_fma_f32 v[228:229], v[228:229], v[244:245], v[236:237] op_sel:[1,1,0] op_sel_hi:[1,0,1] neg_lo:[0,1,0]\n\tv_pk_fma_f32 v[230:231], v[230:231], v[244:245], v[238:239] op_sel:[1,1,0] op_sel_hi:[1,0,1] neg_lo:[0,1,0]\n\tv_cvt_pk_f16_f32 v240, v224, v225\n\tv_cvt_pk_f16_f32 v241, v226, v227\n\tv_cvt_pk_f16_f32 v242, v228, v229\n\tv_cvt_pk_f16_f32 v243, v230, v231\n\tds_write_addtid_b32 v240 offset:29568\n\tds_write_addtid_b32 v241 offset:30624\n\tds_write_addtid_b32 v242 offset:31680\n\tds_write_addtid_b32 v243 offset:32736"
               : "+{v[226:227]}"(E8) :: "v224", "v225", "v228", "v229", "v230", "v231", "v232", "v233", "v234", "v235", "v236", "v237", "v238", "v239", "v240", "v241", "v242", "v243", "v244", "v245", "memory");
}
__device__ __forceinline__ void table_p(f32x2 W, f32x2 E1, f32x2 E256) {
  asm volatile("v_pk_mul_f32 v[238:239], v[226:227], v[226:227] op_sel_hi:[0,1]\n\tv_pk_mul_f32 v[240:241], v[228:229], v[228:229] op_sel_hi:[0,1]\n\tv_pk_mul_f32 v[242:243], v[224:225], v[226:227] op_sel_hi:[0,1]\n\tv_pk_mul_f32 v[244:245], v[224:225], v[228:229] op_sel_hi:[0,1]\n\tv_cvt_pk_f16_f32 v200, v224, v225\n\tv_pk_fma_f32 v[208:209], v[226:227], v[226:227], v[238:239] op_sel:[1,1,0] op_sel_hi:[1,0,1] neg_lo:[0,1,0]\n\tv_pk_fma_f32 v[212:213], v[228:229], v[228:229], v[240:241] op_sel:[1,1,0] op_sel_hi:[1,0,1] neg_lo:[0,1,0]\n\tv_pk_fma_f32 v[216:217], v[224:225], v[226:227], v[242:243] op_sel:[1,1,0] op_sel_hi:[1,0,1] neg_lo:[0,1,0]\n\tv_pk_fma_f32 v[222:223], v[224:225], v[228:229], v[244:245] op_sel:[1,1,0] op_sel_hi:[1,0,1] neg_lo:[0,1,0]\n\tds_write_addtid_b32 v200 offset:33792\n\tv_pk_mul_f32 v[238:239], v[208:209], v[208:209] op_sel_hi:[0,1]\n\tv_pk_mul_f32 v[240:241], v[212:213], v[212:213] op_sel_hi:[0,1]\n\tv_pk_mul_f32 v[242:243], v[224:225], v[208:209] op_sel_hi:[0,1]\n\tv_pk_mul_f32 v[244:245], v[224:225], v[212:213] op_sel_hi:[0,1]\n\tv_cvt_pk_f16_f32 v201, v216, v217\n\tv_cvt_pk_f16_f32 v202, v222, v223\n\tv_pk_fma_f32 v[210:211], v[208:209], v[208:209], v[238:239] op_sel:[1,1,0] op_sel_hi:[1,0,1] neg_lo:[0,1,0]\n\tv_pk_fma_f32 v[214:215], v[212:213], v[212:213], v[240:241] op_sel:[1,1,0] op_sel_hi:[1,0,1] neg_lo:[0,1,0]\n\tv_pk_fma_f32 v[218:219], v[224:225], v[208:209], v[242:243] op_sel:[1,1,0] op_sel_hi:[1,0,1] neg_lo:[0,1,0]\n\tv_pk_fma_f32 v[232:233], v[224:225], v[212:213], v[244:245] op_sel:[1,1,0] op_sel_hi:[1,0,1] neg_lo:[0,1,0]\n\tds_write_addtid_b32 v201 offset:34848\n\tds_write_addtid_b32 v202 offset:42240\n\tv_pk_mul_f32 v[238:239], v[216:217], v[208:209] op_sel_hi:[0,1]\n\tv_pk_mul_f32 v[240:241], v[222:223], v[208:209] op_sel_hi:[0,1]\n\tv_pk_mul_f32 v[242:243], v[222:223], v[212:213] op_sel_hi:[0,1]\n\tv_pk_mul_f32 v[244:245], v[224:225], v[214:215] op_sel_hi:[0,1]\n\tv_cvt_pk_f16_f32 v203, v218, v219\n\tv_cvt_pk_f16_f32 v204, v232, v233\n\tv_pk_mul_f32 v[246:247], v[224:225], v[210:211] op_sel_hi:[0,1]\n\tv_pk_mul_f32 v[248:249], v[216:217], v[210:211] op_sel_hi:[0,1]\n\tv_pk_mul_f32 v[250:251], v[218:219], v[210:211] op_sel_hi:[0,1]\n\tv_pk_mul_f32 v[252:253], v[222:223], v[210:211] op_sel_hi:[0,1]\n\tv_pk_fma_f32 v[220:221], v[216:217], v[208:209], v[238:239] op_sel:[1,1,0] op_sel_hi:[1,0,1] neg_lo:[0,1,0]\n\tv_pk_fma_f32 v[230:231], v[222:223], v[208:209], v[240:241] op_sel:[1,1,0] op_sel_hi:[1,0,1] neg_lo:[0,1,0]\n\tv_pk_fma_f32 v[234:235], v[222:223], v[212:213], v[242:243] op_sel:[1,1,0] op_sel_hi:[1,0,1] neg_lo:[0,1,0]\n\tv_pk_fma_f32 v[236:237], v[224:225], v[214:215], v[244:245] op_sel:[1,1,0] op_sel_hi:[1,0,1] neg_lo:[0,1,0]\n\tds_write_addtid_b32 v203 offset:35904\n\tds_write_addtid_b32 v204 offset:46464\n\tv_pk_fma_f32 v[246:247], v[224:225], v[210:211], v[246:247] op_sel:[1,1,0] op_sel_hi:[1,0,1] neg_lo:[0,1,0]\n\tv_pk_fma_f32 v[248:249], v[216:217], v[210:211], v[248:249] op_sel:[1,1,0] op_sel_hi:[1,0,1] neg_lo:[0,1,0]\n\tv_pk_fma_f32 v[250:251], v[218:219], v[210:211], v[250:251] op_sel:[1,1,0] op_sel_hi:[1,0,1] neg_lo:[0,1,0]\n\tv_pk_fma_f32 v[252:253], v[222:223], v[210:211], v[252:253] op_sel:[1,1,0] op_sel_hi:[1,0,1] neg_lo:[0,1,0]\n\tv_pk_mul_f32 v[238:239], v[220:221], v[210:211] op_sel_hi:[0,1]\n\tv_pk_mul_f32 v[240:241], v[230:231], v[210:211] op_sel_hi:[0,1]\n\tv_pk_mul_f32 v[242:243], v[232:233], v[210:211] op_sel_hi:[0,1]\n\tv_pk_mul_f32 v[244:245], v[234:235], v[210:211] op_sel_hi:[0,1]\n\tv_cvt_pk_f16_f32 v200, v220, v221\n\tv_cvt_pk_f16_f32 v201, v230, v231\n\tv_cvt_pk_f16_f32 v202, v234, v235\n\tv_cvt_pk_f16_f32 v205, v236, v237\n\tv_cvt_pk_f16_f32 v206, v246, v247\n\tv_cvt_pk_f16_f32 v207, v248, v249\n\tv_cvt_pk_f16_f32 v203, v250, v251\n\tv_cvt_pk_f16_f32 v204, v252, v253\n\tv_pk_fma_f32 v[238:239], v[220:221], v[210:211], v[238:239] op_sel:[1,1,0] op_sel_hi:[1,0,1] neg_lo:[0,1,0]\n\tv_pk_fma_f32 v[240:241], v[230:231], v[210:211], v[240:241] op_sel:[1,1,0] op_sel_hi:[1,0,1] neg_lo:[0,1,0]\n\tv_pk_fma_f32 v[242:243], v[232:233], v[210:211], v[242:243] op_sel:[1,1,0] op_sel_hi:[1,0,1] neg_lo:[0,1,0]\n\tv_pk_fma_f32 v[244:245], v[234:235], v[210:211], v[244:245] op_sel:[1,1,0] op_sel_hi:[1,0,1] neg_lo:[0,1,0]\n\tds_write_addtid_b32 v200 offset:36960\n\tds_write_addtid_b32 v201 offset:43296\n\tds_write_addtid_b32 v202 offset:48576\n\tds_write_addtid_b32 v205 offset:50688\n\tv_pk_mul_f32 v[246:247], v[222:223], v[214:215] op_sel_hi:[0,1]\n\tv_pk_mul_f32 v[248:249], v[232:233], v[214:215] op_sel_hi:[0,1]\n\tv_pk_mul_f32 v[250:251], v[234:235], v[214:215] op_sel_hi:[0,1]\n\tv_pk_mul_f32 v[252:253], v[236:237], v[214:215] op_sel_hi:[0,1]\n\tds_write_addtid_b32 v206 offset:38016\n\tds_write_addtid_b32 v207 offset:39072\n\tds_write_addtid_b32 v203 offset:40128\n\tds_write_addtid_b32 v204 offset:44352\n\tv_cvt_pk_f16_f32 v200, v238, v239\n\tv_cvt_pk_f16_f32 v201, v240, v241\n\tv_cvt_pk_f16_f32 v202, v242, v243\n\tv_cvt_pk_f16_f32 v205, v244, v245\n\tv_pk_fma_f32 v[246:247], v[222:223], v[214:215], v[246:247] op_sel:[1,1,0] op_sel_hi:[1,0,1] neg_lo:[0,1,0]\n\tv_pk_fma_f32 v[248:249], v[232:233], v[214:215], v[248:249] op_sel:[1,1,0] op_sel_hi:[1,0,1] neg_lo:[0,1,0]\n\tv_pk_fma_f32 v[250:251], v[234:235], v[214:215], v[250:251] op_sel:[1,1,0] op_sel_hi:[1,0,1] neg_lo:[0,1,0]\n\tv_pk_fma_f32 v[252:253], v[236:237], v[214:215], v[252:253] op_sel:[1,1,0] op_sel_hi:[1,0,1] neg_lo:[0,1,0]\n\tds_write_addtid_b32 v200 offset:41184\n\tds_write_addtid_b32 v201 offset:45408\n\tds_write_addtid_b32 v202 offset:47520\n\tds_write_addtid_b32 v205 offset:49632\n\tv_cvt_pk_f16_f32 v206, v246, v247\n\tv_cvt_pk_f16_f32 v207, v248, v249\n\tv_cvt_pk_f16_f32 v203, v250, v251\n\tv_cvt_pk_f16_f32 v204, v252, v253\n\tds_write_addtid_b32 v206 offset:51744\n\tds_write_addtid_b32 v207 offset:52800\n\tds_write_addtid_b32 v203 offset:53856\n\tds_write_addtid_b32 v204 offset:54912"
               :: "{v[224:225]}"(W), "{v[226:227]}"(E1), "{v[228:229]}"(E256) : "v200", "v201", "v202", "v203", "v204", "v205", "v206", "v207", "v208", "v209", "v210", "v211", "v212", "v213", "v214", "v215", "v216", "v217", "v218", "v219", "v220", "v221", "v222", "v223", "v230", "v231", "v232", "v233", "v234", "v235", "v236", "v237", "v238", "v239", "v240", "v241", "v242", "v243", "v244", "v245", "v246", "v247", "v248", "v249", "v250", "v251", "v252", "v253", "memory");
}
__device__ __forceinline__ void lds_barrier() { asm volatile("s_waitcnt lgkmcnt(0)\n\ts_barrier" ::: "memory"); }
#define LDSP(T, off) ((__attribute__((address_space(3))) T*)((lds_ptr)smem + (off)))
#define MFMA16H(a, b, c) __builtin_amdgcn_mfma_f32_16x16x32_f16(__builtin_bit_cast(f16x8, a), __builtin_bit_cast(f16x8, b), c, 0, 0, 0)
__device__ __forceinline__ void glds16v(const void* sbase, u32 voff, unsigned lds_base) {
  asm volatile("s_mov_b32 m0, %2\n\ts_nop 0\n\tglobal_load_lds_dwordx4 %0, %1" :: "v"(voff), "s"(sbase), "s"(lds_base) : "memory");
}
__device__ __forceinline__ void reload16_sc1(u32x4& dst, const void* sbase, u32 voff, int imm) {
  asm volatile("global_load_dwordx4 %0, %1, %2 offset:%3 sc1" : "+v"(dst) : "v"(voff), "s"(sbase), "i"(imm) : "memory");
}
__device__ __forceinline__ void vm0() { asm volatile("s_waitcnt vmcnt(0)" ::: "memory"); }
template <int N> __device__ __forceinline__ void vmw() { asm volatile("s_waitcnt vmcnt(%0)" :: "i"(N) : "memory"); }
#define PIN(x) asm volatile("" : "+v"(x))
#define SB() __builtin_amdgcn_sched_barrier(0)
__device__ __forceinline__ u32x4 frag128(const char* smem, int base, int lbase, int phi, int ks, int t) {
  lds_cptr a = (lds_cptr)smem + base + lbase + 32 * (t ^ phi) + ks * 4096;
  const u32x2 t0 = tr_read(a), t1 = tr_read(a + 512);
  return u32x4{t0[0], t0[1], t1[0], t1[1]};
}
__device__ __forceinline__ void st16_wt(const __amdgpu_buffer_rsrc_t& r, int off, u32x4 v) { __builtin_amdgcn_raw_buffer_store_b128(v, r, off, 0, WT_AUX); }
__device__ __forceinline__ u32 cld32(const void* q) { return *(const __attribute__((address_space(4))) u32*)(uintptr_t)q; }
__device__ __forceinline__ float pow2_inv(float s) { return __uint_as_float(0x7F000000u - __float_as_uint(s)); }
__device__ __forceinline__ float rlane(u32 v, int l) { return __uint_as_float(__builtin_amdgcn_readlane(v, l)); }
#define LDS4(adr) (*(const __attribute__((address_space(3))) u32x4*)(adr))
#define LDS2(adr) (*(const __attribute__((address_space(3))) u32x2*)(adr))

__device__ __forceinline__ float sel4(const f32x4& a, int r) {
  const float lo = (r & 1) ? a[1] : a[0], hi = (r & 1) ? a[3] : a[2];
  return (r & 2) ? hi : lo;
}
__device__ __forceinline__ void ln_rows(const Params& p, u32x4 (&xr)[14], int row16, int lane, u32 tag) {
  const int col = lane & 15, g = lane >> 4;
  f32x4 ag = {0, 0, 0, 0}, as = {0, 0, 0, 0};
  const u32x4 ones = {0x3F803F80u, 0x3F803F80u, 0x3F803F80u, 0x3F803F80u};
#pragma unroll
  for (int s = 0; s < 14; ++s) { PIN4(xr[s]); ag = MFMA16(xr[s], xr[s], ag); as = MFMA16(xr[s], ones, as); }
  const float qq = sel4(ag, col), ss = sel4(as, col);
  const float mean = ss * (1.0f / D);
  const float var = fmaxf(qq * (1.0f / D) - mean * mean, 0.f);
  const float sd = __builtin_amdgcn_sqrtf(rnd(rnd(var) + 1e-5f));
  if (g == (col >> 2)) st_gran(p.ws + WS_SG, row16 + col, pkbf(mean, sd), tag);
}
template <int N> __device__ __forceinline__ void vmw_pin(u32x4& a) { asm volatile("s_waitcnt vmcnt(%1)" : "+v"(a) : "i"(N) : "memory"); }
template <int N> __device__ __forceinline__ void vmw_pin(u32x4& a, u32x4& b) { asm volatile("s_waitcnt vmcnt(%2)" : "+v"(a), "+v"(b) : "i"(N) : "memory"); }
#define LDS4U(adr) (*(const __attribute__((address_space(3))) u32x4 __attribute__((aligned(8)))*)(adr))
typedef u32x4 __attribute__((aligned(8))) u32x4_a8;

#define PINS(x) asm volatile("" : "+s"(x))
template <int OFF> __device__ __forceinline__ void lds_rd64(u32x2& dst, u32 adr) {
  asm volatile("ds_read_b64 %0, %1 offset:%2" : "=v"(dst) : "v"(adr), "i"(OFF) : "memory");
}
__device__ __forceinline__ void swap16(u32& a, u32& b) {
  const auto s = __builtin_amdgcn_permlane16_swap(a, b, false, false);
  a = s[0]; b = s[1];
}
#define X1_AUX 0

__device__ __forceinline__ void conv_main(const Params& p, int b, char* smem) {
  const int tid = threadIdx.x, lane = tid & 63;
  const int w = __builtin_amdgcn_readfirstlane(tid >> 6), ch = w >> 2, pq = w & 3;
  const int cp = (b & 7) * 28 + (b >> 3), d0 = 2 * cp, d = d0 + ch;
  const int col = lane & 15, g = lane >> 4;
  const unsigned lds0 = (unsigned)(uintptr_t)smem;
  SDECL(14); SST(0);
  const bool ln = w == 0 && b < 128;
  u32x4 xr[14];
  if (ln) {
    const char* xs = (const char*)(p.u + (size_t)(16 * b) * D);
    const u32 xo = (u32)(col * D + 8 * g) * 2;
#pragma unroll
    for (int s = 0; s < 14; ++s) gload16(xr[s], xs, xo, s * 64);
  }
  u32 pa[6];
  {
    const u32 po = (u32)(d * NS + pq * 64 + lane) * 2;
    const u16* const ps[6] = {p.A_re, p.A_im, p.B_re, p.B_im, p.C_re, p.C_im};
#pragma unroll
    for (int i = 0; i < 6; ++i) asm volatile("global_load_short_d16_hi %0, %1, %2" : "=v"(pa[i]) : "v"(po), "s"(ps[i]) : "memory");
  }
  const u32 ldt2 = cld32(p.log_dt + d0), dsk2 = cld32(p.Dskip + d0), tag = cld32(p.ws + WS_EP) + TAGC;
  asm volatile("s_barrier" ::: "memory");
  const int row0 = 256 * w + 16 * col + 4 * g;
  const u32 xo4 = (u32)row0 * (D * 2) + cp * 4;
  u32 xg[4];
  u32x4 gt[3];
#pragma unroll
  for (int i = 0; i < 3; ++i) gload16(gt[i], &gtab.v[ch][i][0][0], (u32)(tid & 255) * 16, 0);
  asm volatile("global_load_dword %0, %1, %2" : "=v"(xg[0]) : "v"(xo4), "s"(p.u) : "memory");
  asm volatile("global_load_dword %0, %1, %2 offset:896" : "=v"(xg[1]) : "v"(xo4), "s"(p.u) : "memory");
  const int wt = b < 196 ? b : b - 196, wta = wt / 7, wtb = wt - 7 * wta;
  const u16* wsrc0 = p.w_gate + (size_t)(64 * wtb + 8 * w) * F + 64 * wta;
  const u16* wsrc1 = p.w_up + (size_t)(64 * wtb + 8 * w) * F + 64 * wta;
  const u16* wsrc2 = p.w_down + (size_t)(64 * wta + 8 * w) * D + 64 * wtb;
  u32 vf, vd;
  {
    const int dchunk = (lane & 7) ^ ((((lane >> 4) & 1) | ((w & 1) << 1)) << 1);
    vf = (u32)((lane >> 3) * F + dchunk * 8) * 2; vd = (u32)((lane >> 3) * D + dchunk * 8) * 2;
  }
  PINS(wsrc0); PINS(wsrc1); PINS(wsrc2); PIN(vf); PIN(vd);
  if (tid < 60) *LDSP(u32x4, L_H + (tid >= 30 ? H_CH - 480 : 0) + tid * 16) = u32x4{0, 0, 0, 0};
  if (tid >= 128 && tid < 130) *LDSP(u16, L_KT + (tid & 1) * KT_CH + KT_Z) = 0;
  const int rt = pq & 1, ct = pq >> 1;
  u32 a_fq = lds0 + ch * T_CH + (16 * ct + col) * T_RS + 16 * g;
  u32 a_fp = lds0 + ch * T_CH + T_P + (16 * rt + col) * T_RS + 16 * g;
  u32 a_kt = lds0 + L_KT + ch * KT_CH + pq * 512 + lane * 8;
  u32 a_rw = lds0 + L_R + ch * R_CH + (tid & 255) * 16;
  u32 a_ar;
  {
    const int e0 = 2031 - col + 8 * g, c4 = e0 & 3;
    a_ar = lds0 + L_R + ch * R_CH + c4 * R_CP + 2 * (e0 - c4) - 64 * 63 - 64 * pq + 192;
  }
  u32 a_hw = lds0 + L_H + 480 + row0 * 2;
  u32 a_br = lds0 + L_H + ch * H_CH + 448 - 256 - 64 * pq + 32 * col + 16 * g;
  u32 a_pw = lds0 + w * 8192 + lane * 16, a_pr = lds0 + w * 1024 + lane * 16;
  u32 a_wf;
  {
    const int q4 = col >> 2, pp = lane & 3, phi = (q4 >> 1) | ((g & 1) << 1);
    a_wf = lds0 + L_W + (8 * g + q4) * 128 + 8 * pp + 32 * ((w & 3) ^ phi) + (w >> 2) * 4096;
  }
  u32 sgo = (u32)WS_SG + row0 * 8, mgo = (u32)WS_MG + (((lane & 3) == 3 ? 4 : (lane & 3)) * D + d0) * 8;
  u32 oo = (u32)(((2 * w + (col >> 3)) * D + d0 + (g & 1)) * 128 + 16 * (col & 7) + 8 * (g >> 1)) * 2 + (u32)WS_X1T;
  u32 wo0, wo1;
  {
    const int ks = 2 * wtb + (w >> 2), ft = 4 * wta + (w & 3), kk = 2 * wta + (w >> 2), w7 = kk / 7;
    wo0 = (u32)WS_WF + ((ft * 14 + ks) * 2) * 1024;
    wo1 = (u32)WS_WDF + (((wtb * 8 + w7) * 4 + (w & 3)) * 7 + (kk - 7 * w7)) * 1024;
  }
  PIN(a_fq); PIN(a_fp); PIN(a_kt); PIN(a_rw); PIN(a_ar); PIN(a_hw); PIN(a_br); PIN(a_pw); PIN(a_pr); PIN(a_wf); PIN(sgo); PIN(mgo); PIN(oo);
  PINS(wo0); PINS(wo1);
  float dt = rnd(expf(ch ? bfhi(ldt2) : bflo(ldt2)));
  PIN(dt);
  const u32 m0v = lds0 + ch * T_CH + pq * 256;
  SB();
  SST(1);
  if (ln) { vmw<11>(); ln_rows(p, xr, 16 * b, lane, tag); vmw<6>(); }
  else vmw<5>();
#pragma unroll
  for (int i = 0; i < 6; ++i) PIN(pa[i]);
  if (tid == 448) __hip_atomic_store((gu32*)(p.ws + WS_RD) + b, tag, __ATOMIC_RELAXED, __HIP_MEMORY_SCOPE_AGENT);
  SST(2);
  const float zr = dt * bfhi(pa[0]), zi = dt * bfhi(pa[1]);
  asm volatile("s_mov_b32 m0, %0" :: "s"(m0v) : "memory");
  {
    table_q(cexpm(zr, zi, 8.f));
  }
  asm volatile("global_load_dword %0, %1, %2 offset:1792" : "=v"(xg[2]) : "v"(xo4), "s"(p.u) : "memory");
  asm volatile("global_load_dword %0, %1, %2 offset:2688" : "=v"(xg[3]) : "v"(xo4), "s"(p.u) : "memory");
  SST(3);
  lds_barrier();
  u32x4 fq[16];
#pragma unroll
  for (int s = 0; s < 16; ++s) fq[s] = LDS4((lds_cptr)(uintptr_t)a_fq + 64 * s);
  SB();
  {
    const float br = dt * bfhi(pa[2]), bi = dt * bfhi(pa[3]), cr = bfhi(pa[4]), ci = bfhi(pa[5]);
    f32x2 W = {cr * br - ci * bi, -(cr * bi + ci * br)};
    const float S = wave_sum(fabsf(W.x) + fabsf(W.y));
    const f32x2 E1 = cexpm(zr, -zi, 1.f), E256 = cexpm(zr, -zi, 256.f);
    const float sw = pow2_scale(S, 14);
    W *= sw;
    if (lane == 0) *LDSP(f32x2, L_SC + w * 8) = f32x2{pow2_inv(sw), S};
    table_p(W, E1, E256);
  }
  SST(4);
  lds_barrier();
  float kt0, kt1;
  u32x4 mg, st0, st1;
  {
    u32x4 fp[16];
#pragma unroll
    for (int s = 0; s < 4; ++s)
#pragma unroll
      for (int c = 0; c < 4; ++c) fp[4 * c + s] = LDS4((lds_cptr)(uintptr_t)a_fp + 256 * c + 64 * s);
    const f32x4 s0 = *LDSP(f32x4, L_SC), s1 = *LDSP(f32x4, L_SC + 16), s2 = *LDSP(f32x4, L_SC + 32), s3 = *LDSP(f32x4, L_SC + 48);
    const f32x4 sa = *LDSP(f32x4, L_SC + ch * 32), sb = *LDSP(f32x4, L_SC + ch * 32 + 16);
    SB();
    f32x4 acc[4];
#pragma unroll
    for (int s = 0; s < 4; ++s)
#pragma unroll
      for (int c = 0; c < 4; ++c) acc[c] = MFMA16H(fp[4 * c + s], fq[4 * c + s], (s == 0 ? f32x4{0, 0, 0, 0} : acc[c]));
    kt0 = pow2_scale(s0[1] + s0[3] + s1[1] + s1[3], 14);
    kt1 = pow2_scale(s2[1] + s2[3] + s3[1] + s3[3], 14);
    const float kt = pow2_scale(sa[1] + sa[3] + sb[1] + sb[3], 14);
    const f32x4 kv = acc[0] * (sa[0] * kt) + acc[1] * (sa[2] * kt) + acc[2] * (sb[0] * kt) + acc[3] * (sb[2] * kt);
    *(__attribute__((address_space(3))) u32x2*)(uintptr_t)a_kt = u32x2{pkh(kv[0], kv[1]), pkh(kv[2], kv[3])};
  }
  SB();
  gload16_sc1(mg, p.ws, mgo, 0); gload16_sc1(st0, p.ws, sgo, 0); gload16_sc1(st1, p.ws, sgo, 16);
  glds16v(wsrc0, vf, lds0 + L_W + w * 1024);
  glds16v(wsrc1, vf, lds0 + L_W + 8192 + w * 1024);
  glds16v(wsrc2, vd, lds0 + L_W + 16384 + w * 1024);
  asm volatile("s_waitcnt vmcnt(10)" : "+v"(gt[0]), "+v"(gt[1]), "+v"(gt[2]) :: "memory");
  SST(5);
  lds_barrier();
  {
    u32 v[11];
#pragma unroll
    for (int j = 0; j < 11; ++j) v[j] = *(const __attribute__((address_space(3))) u16*)(uintptr_t)(gt[j >> 2][j & 3] + lds0);
    u32 e[5], o[5];
#pragma unroll
    for (int j = 0; j < 5; ++j) { e[j] = v[2 * j] | (v[2 * j + 1] << 16); o[j] = v[2 * j + 1] | (v[2 * j + 2] << 16); }
    lds_ptr rb = (lds_ptr)(uintptr_t)a_rw;
    *(__attribute__((address_space(3))) u32x4*)(rb) = u32x4{e[0], e[1], e[2], e[3]};
    *(__attribute__((address_space(3))) u32x4*)(rb + R_CP) = u32x4{o[0], o[1], o[2], o[3]};
    *(__attribute__((address_space(3))) u32x4*)(rb + 2 * R_CP) = u32x4{e[1], e[2], e[3], e[4]};
    *(__attribute__((address_space(3))) u32x4*)(rb + 3 * R_CP) = u32x4{o[1], o[2], o[3], o[4]};
    if ((tid & 255) < 8) *LDSP(u32x4, L_R + ch * R_CH + ((tid & 7) >> 1) * R_CP + 4096 + (tid & 1) * 16) = u32x4{0, 0, 0, 0};
  }
  SST(6);
  lds_barrier();
  u32x2 Al[2][8], Ah[2][8];
  {
    u32 a2 = a_ar + 2048;
    PIN(a2);
#define RDA(u, k) do { if (k < 4) { lds_rd64<64 * (28 - 4 * u - 8 * k) + 0 * (k < 4)>(Al[u][k], a2); lds_rd64<64 * (28 - 4 * u - 8 * k) + 8>(Ah[u][k], a2); } } while (0)
#define RDB(u, k) do { lds_rd64<64 * (60 - 4 * u - 8 * k)>(Al[u][k], a_ar); lds_rd64<64 * (60 - 4 * u - 8 * k) + 8>(Ah[u][k], a_ar); } while (0)
    RDA(0, 0); RDA(0, 1); RDA(0, 2); RDA(0, 3); RDB(0, 4); RDB(0, 5); RDB(0, 6); RDB(0, 7);
    RDA(1, 0); RDA(1, 1); RDA(1, 2); RDA(1, 3); RDB(1, 4); RDB(1, 5); RDB(1, 6); RDB(1, 7);
#undef RDA
#undef RDB
  }
  asm volatile("s_waitcnt vmcnt(3)" : "+v"(mg), "+v"(st0), "+v"(st1) :: "memory");
#define NE(x) __builtin_amdgcn_ballot_w64((x) != tag)
  unsigned long long bm = NE(mg[1]) | NE(mg[3]);
  if ((NE(st0[1]) | NE(st0[3]) | NE(st1[1]) | NE(st1[3]) | (bm & 3)) != 0) {
#pragma unroll 1
    for (unsigned spins = 0; spins < (1u << 20); ++spins) {
      __builtin_amdgcn_s_sleep(1);
      asm volatile("global_load_dwordx4 %0, %3, %5 sc1\n\tglobal_load_dwordx4 %1, %3, %5 offset:16 sc1\n\tglobal_load_dwordx4 %2, %4, %5 sc1\n\ts_waitcnt vmcnt(0)"
                   : "+v"(st0), "+v"(st1), "+v"(mg) : "v"(sgo), "v"(mgo), "s"(p.ws) : "memory");
      bm = NE(mg[1]) | NE(mg[3]);
      if ((NE(st0[1]) | NE(st0[3]) | NE(st1[1]) | NE(st1[3]) | (bm & 3)) == 0) break;
    }
  }
#pragma unroll
  for (int r = 0; r < 4; ++r) PIN(xg[r]);
  SST(7);
  f32x2 xf[2][2], h[2][2];
  float ihs[2], dks[2];
  {
    const u32 sw_[4] = {st0[0], st0[2], st1[0], st1[2]};
    f32x2 m2[2], r2[2];
#pragma unroll
    for (int hh = 0; hh < 2; ++hh) {
      m2[hh] = f32x2{bflo(sw_[2 * hh]), bflo(sw_[2 * hh + 1])};
      r2[hh] = f32x2{__builtin_amdgcn_rcpf(bfhi(sw_[2 * hh])), __builtin_amdgcn_rcpf(bfhi(sw_[2 * hh + 1]))};
      xf[0][hh] = f32x2{bflo(xg[2 * hh]), bflo(xg[2 * hh + 1])};
      xf[1][hh] = f32x2{bfhi(xg[2 * hh]), bfhi(xg[2 * hh + 1])};
    }
#pragma unroll
    for (int c = 0; c < 2; ++c) {
      const float sa = rlane(mg[2 * c], 0), ca = rnd(rlane(mg[2 * c], 1) + 1.0f);
      const float hs = pow2_scale(22.0f * fabsf(ca) + fabsf(sa), 14);
      ihs[c] = pow2_inv(hs);
      dks[c] = (c ? bfhi(dsk2) : bflo(dsk2)) * ihs[c];
      const float cas = ca * hs, sas = sa * hs;
#pragma unroll
      for (int hh = 0; hh < 2; ++hh) {
        const f32x2 xn = rnd2(rnd2(xf[c][hh] - m2[hh]) * r2[hh]);
        h[c][hh] = rnd2(rnd2(xn * cas) + sas);
      }
      *(__attribute__((address_space(3))) u32x2*)(uintptr_t)(a_hw + c * H_CH) = u32x2{pkh2(h[c][0]), pkh2(h[c][1])};
    }
  }
  const bool again = (bm & 12) != 0;
  vm0();
  if (again) reload16_sc1(mg, p.ws, mgo, 0);
  SST(8);
  lds_barrier();
  f32x2 hd[2][2];
  {
    u32x4 B[2][8], A[2][8];
#pragma unroll
    for (int u = 0; u < 2; ++u)
#pragma unroll
      for (int k = 0; k < 8; ++k) B[u][k] = LDS4((lds_cptr)(uintptr_t)a_br + 512 * k + 256 * (1 - u));
    u32x4 wfr[3];
#pragma unroll
    for (int i = 0; i < 3; ++i) {
      lds_cptr a = (lds_cptr)(uintptr_t)a_wf + i * 8192;
      const u32x2 t0 = tr_read(a), t1 = tr_read(a + 512);
      wfr[i] = u32x4{t0[0], t0[1], t1[0], t1[1]};
    }
#pragma unroll
    for (int u = 0; u < 2; ++u)
#pragma unroll
      for (int k = 0; k < 8; ++k) { PIN(Al[u][k]); PIN(Ah[u][k]); A[u][k] = u32x4{Al[u][k][0], Al[u][k][1], Ah[u][k][0], Ah[u][k][1]}; }
    SB();
#pragma unroll
    for (int c = 0; c < 2; ++c)
#pragma unroll
      for (int hh = 0; hh < 2; ++hh) { hd[c][hh] = rnd2(h[c][hh] * dks[c]); PIN(hd[c][hh]); }
    f32x4 acc[8];
#pragma unroll
    for (int jj = 0; jj < 8; ++jj)
#pragma unroll
      for (int k = 0; k + jj < 8; ++k) acc[k + jj] = MFMA16H(A[0][k], B[0][jj], (jj == 0 ? f32x4{0, 0, 0, 0} : acc[k + jj]));
    SB();
    if (b < 196) {
      const __amdgpu_buffer_rsrc_t wr = __builtin_amdgcn_make_buffer_rsrc(p.ws, 0, (int)WS_X1T, 0x00020000);
      __builtin_amdgcn_raw_buffer_store_b128(wfr[0], wr, lane * 16, wo0, WT_AUX);
      __builtin_amdgcn_raw_buffer_store_b128(wfr[1], wr, lane * 16 + 1024, wo0, WT_AUX);
      __builtin_amdgcn_raw_buffer_store_b128(wfr[2], wr, lane * 16, wo1, WT_AUX);
    }
    SB();
#pragma unroll
    for (int jj = 0; jj < 8; ++jj) {
#pragma unroll
      for (int k = 0; k + jj < 8; ++k) acc[k + jj] = MFMA16H(A[1][k], B[1][jj], acc[k + jj]);
      if (jj > 0) *(__attribute__((address_space(3))) f32x4*)(uintptr_t)(a_pw + (jj - 1) * 1024) = acc[jj - 1];
      SB();
    }
    *(__attribute__((address_space(3))) f32x4*)(uintptr_t)(a_pw + 7 * 1024) = acc[7];
  }
  SST(9);
  lds_barrier();
  SST(10);
  {
    f32x4 y[2];
#pragma unroll
    for (int c = 0; c < 2; ++c) {
      lds_cptr rb = (lds_cptr)(uintptr_t)a_pr + c * 32768;
      typedef const __attribute__((address_space(3))) f32x4* lf4;
      y[c] = (*(lf4)rb + *(lf4)(rb + 8192)) + (*(lf4)(rb + 16384) + *(lf4)(rb + 24576));
    }
    if (again) {
      if (b < 196) asm volatile("s_waitcnt vmcnt(3)" : "+v"(mg) :: "memory"); else asm volatile("s_waitcnt vmcnt(0)" : "+v"(mg) :: "memory");
#pragma unroll 1
      for (unsigned spins = 0; spins < (1u << 20); ++spins) {
        if (((NE(mg[1]) | NE(mg[3])) & 12) == 0) break;
        __builtin_amdgcn_s_sleep(1);
        asm volatile("global_load_dwordx4 %0, %1, %2 sc1\n\ts_waitcnt vmcnt(0)" : "+v"(mg) : "v"(mgo), "s"(p.ws) : "memory");
      }
    }
#undef NE
    u32 o1[2][2], oc[2][2];
#pragma unroll
    for (int c = 0; c < 2; ++c) {
      const float ga = rlane(mg[2 * c], 2), c1 = rnd(rlane(mg[2 * c], 3) + 1.0f);
      const float ysc = pow2_inv(c ? kt1 : kt0) * ihs[c];
#pragma unroll
      for (int hh = 0; hh < 2; ++hh) {
        const f32x2 yy = rnd2(f32x2{y[c][2 * hh], y[c][2 * hh + 1]} * ysc);
        const f32x2 t = rnd2(yy + hd[c][hh]);
        const f32x2 xs = xf[c][hh] + rnd2(t * ga);
        o1[c][hh] = pkbf(xs.x, xs.y);
        const f32x2 xc = f32x2{bflo(o1[c][hh]), bfhi(o1[c][hh])} * c1;
        oc[c][hh] = pkbf(xc.x, xc.y);
      }
    }
    swap16(o1[0][0], o1[1][0]); swap16(o1[0][1], o1[1][1]); swap16(oc[0][0], oc[1][0]); swap16(oc[0][1], oc[1][1]);
    const __amdgpu_buffer_rsrc_t xr_ = __builtin_amdgcn_make_buffer_rsrc(p.ws, 0, (int)WS_HH, 0x00020000);
    __builtin_amdgcn_raw_buffer_store_b128(u32x4{o1[0][0], o1[0][1], o1[1][0], o1[1][1]}, xr_, oo, 0, X1_AUX);
    __builtin_amdgcn_raw_buffer_store_b128(u32x4{oc[0][0], oc[0][1], oc[1][0], oc[1][1]}, xr_, oo, (int)(WS_X1C - WS_X1T), X1_AUX);
  }
  SSTE(11); SEND(0, 14, b);
}

#define G2_POS 1
constexpr int H_S = 0, H_CS = 1024, H_PART = 3072, H_G2 = 4096, H_WG = H_G2 + 57344, H_G1 = H_WG + 57344, H_END = H_G1 + 28672;
constexpr int AB_LDS = AB_MAIN_LDS > H_END ? AB_MAIN_LDS : H_END;

template <int V> struct IC { static constexpr int value = V; };
template <int I, int N, class Fn> __device__ __forceinline__ void sfor(Fn&& f) {
  if constexpr (I < N) { f(IC<I>{}); sfor<I + 1, N>(f); }
}
template <int SA, int LA>
__device__ __forceinline__ void hdma16(__amdgpu_buffer_rsrc_t r, u32 voff, u32 sbase, u32 lbase) {
  u32 t;
  asm volatile("s_add_i32 m0, %4, %6\n\ts_add_i32 %0, %3, %5\n\tbuffer_load_dwordx4 %1, %2, %0 offen lds"
               : "=&s"(t) : "v"(voff), "s"(r), "s"(sbase), "s"(lbase), "i"(SA), "i"(LA) : "memory", "scc");
}
__device__ __forceinline__ void helper(const Params& p, int j, char* smem) {
  const int tid = threadIdx.x, lane = tid & 63;
  const int w = __builtin_amdgcn_readfirstlane(tid >> 6);
  const int col = lane & 15, g = lane >> 4, q4 = col >> 2, pp = lane & 3;
  const unsigned lds0 = (unsigned)(uintptr_t)smem;
  SDECL(12); SST(0);
  u32 ep;
  asm volatile("s_load_dword %0, %1, 0x0" : "=s"(ep) : "s"(p.ws + WS_EP) : "memory");
  if (j >= 28) {
    asm volatile("s_waitcnt lgkmcnt(0)" : "+s"(ep) :: "memory");
    const u32 tag = ep + TAGC;
    if (tid == 448) __hip_atomic_store((gu32*)(p.ws + WS_RD) + AB_MAIN + j, tag, __ATOMIC_RELAXED, __HIP_MEMORY_SCOPE_AGENT);
    if (j == 28) ((u32x2*)(p.ws + WS_FLG))[tid] = u32x2{0, 0};
    if (j == 29) *(u32x4*)(p.ws + WS_STG + tid * 16) = u32x4{0, 0, 0, 0};
    if (j == 30) {
      if (tid < 256) {
#pragma unroll 1
        for (unsigned spins = 0; spins < (1u << 20); ++spins) {
          if (__hip_atomic_load((gu32*)(p.ws + WS_RD) + tid, __ATOMIC_RELAXED, __HIP_MEMORY_SCOPE_AGENT) == tag) break;
          __builtin_amdgcn_s_sleep(4);
        }
      }
      __syncthreads();
      if (tid == 0) *(u32*)(p.ws + WS_EP) = tag;
    }
    return;
  }
  const int lbase = (8 * g + q4) * 128 + 8 * pp, phi = (q4 >> 1) | ((g & 1) << 1);
  const int dchunk = (lane & 7) ^ ((((lane >> 4) & 1) | ((w & 1) << 1)) << 1);
  const int ks0 = w < 6 ? 2 * w : w + 6, nks = w < 6 ? 2 : 1;
  u32 cw;
  asm volatile("global_load_short_d16_hi %0, %1, %2" : "=v"(cw) : "v"((u32)min(32 * ks0 + lane, D - 1) * 2), "s"(p.c) : "memory");
  const __amdgpu_buffer_rsrc_t wm = __builtin_amdgcn_make_buffer_rsrc((void*)p.w_mod, 0, D * 6 * D * 2, 0x00020000);
  {
    const u32 go = (u32)((lane >> 2) * (6 * D) + ((lane & 3) ^ (((lane >> 5) & 1) << 1)) * 8) * 2;
    const u32 so = (u32)(32 * ks0 * 6 * D + 32 * j) * 2, lo = lds0 + H_G1 + 32 * ks0 * 64;
    hdma16<0, 0>(wm, go, so, lo); hdma16<16 * 6 * D * 2, 1024>(wm, go, so, lo);
    if (w < 6) { hdma16<32 * 6 * D * 2, 2048>(wm, go, so, lo); hdma16<48 * 6 * D * 2, 3072>(wm, go, so, lo); }
  }
  SST(6);
  {
    vm0();
    PIN(cw);
  {
    const u32 go = (u32)((lane >> 3) * (6 * D) + dchunk * 8) * 2;
    const u32 so = (u32)(8 * w * 6 * D + 2 * D + 64 * j) * 2, lo = lds0 + H_G2 + w * 1024;
    sfor<0, 7>([&](auto I) { hdma16<I.value * 64 * 6 * D * 2, I.value * 8192>(wm, go, so, lo); });
  }
    SST(8);
    const float c0 = bfhi(cw);
    if (lane < 32 * nks) *LDSP(u16, H_S + (32 * ks0 + lane) * 2) = (u16)pkbf(c0 / (1.0f + expf(-c0)), 0.f);
    f32x4 a0 = {0, 0, 0, 0}, a1 = {0, 0, 0, 0};
    const int lb64 = (8 * g + q4) * 64 + 8 * pp;
    for (int s = 0; s < nks; ++s) {
      const int ks = ks0 + s;
      const u32x4 sf = *LDSP(u32x4, H_S + (32 * ks + 8 * g) * 2);
      lds_cptr a = (lds_cptr)smem + H_G1 + lb64 + ks * 2048;
      const u32x2 t0 = tr_read(a + 32 * (g & 1)), t1 = tr_read(a + 32 * (g & 1) + 256), t2 = tr_read(a + 32 * ((g & 1) ^ 1)), t3 = tr_read(a + 32 * ((g & 1) ^ 1) + 256);
      a0 = MFMA16(sf, (u32x4{t0[0], t0[1], t1[0], t1[1]}), a0);
      a1 = MFMA16(sf, (u32x4{t2[0], t2[1], t3[0], t3[1]}), a1);
    }
    SST(9);
    if (lane < 16) { *LDSP(float, H_PART + (w * 32 + lane) * 4) = a0[0]; *LDSP(float, H_PART + (w * 32 + 16 + lane) * 4) = a1[0]; }
  }
  lds_barrier();
  SST(7);
  PINS(ep);
  const u32 tag = ep + TAGC;
  if (w == 0 && lane < 32) {
    float t = 0.f;
#pragma unroll
    for (int u = 0; u < 8; ++u) t += *LDSP(float, H_PART + (u * 32 + lane) * 4);
    st_gran(p.ws + WS_MG, 32 * j + lane, __float_as_uint(rnd(t)), tag);
  }
  if (tid == 448) __hip_atomic_store((gu32*)(p.ws + WS_RD) + AB_MAIN + j, tag, __ATOMIC_RELAXED, __HIP_MEMORY_SCOPE_AGENT);
  SST(1);
  const u32 vofff = (u32)((lane >> 3) * F + dchunk * 8) * 2;
#pragma unroll
  for (int i = 0; i < 7; ++i) glds16v(p.w_gate + (size_t)(8 * (w + 8 * i)) * F + 64 * j, vofff, lds0 + H_WG + (w + 8 * i) * 1024);
  vmw<7>();
  lds_barrier();
  SST(2);
  if (w < 4) {
    f32x4 a = {0, 0, 0, 0};
#pragma unroll
    for (int ks = 0; ks < 14; ++ks) a = MFMA16(*LDSP(u32x4, H_S + (32 * ks + 8 * g) * 2), frag128(smem, H_G2, lbase, phi, ks, w), a);
    if (lane < 16) {
      const float t = rnd(a[0]);
      const int mi = 2 * D + 64 * j + 16 * w + lane;
      st_gran(p.ws + WS_MG, mi, __float_as_uint(t), tag);
      ((float*)(p.ws + WS_MOD))[mi] = t;
    }
  }
  lds_barrier();
  SST(3);
#pragma unroll
  for (int i = 0; i < 7; ++i) glds16v(p.w_up + (size_t)(8 * (w + 8 * i)) * F + 64 * j, vofff, lds0 + H_G2 + (w + 8 * i) * 1024);
  if (tid < D) {
    unsigned long long a, c;
#pragma unroll 1
    for (unsigned spins = 0; spins < (1u << 20); ++spins) {
      a = __hip_atomic_load((gu64*)(p.ws + WS_MG) + 3 * D + tid, __ATOMIC_RELAXED, __HIP_MEMORY_SCOPE_AGENT);
      c = __hip_atomic_load((gu64*)(p.ws + WS_MG) + 4 * D + tid, __ATOMIC_RELAXED, __HIP_MEMORY_SCOPE_AGENT);
      if ((u32)(a >> 32) == tag && (u32)(c >> 32) == tag) break;
      __builtin_amdgcn_s_sleep(1);
    }
    *LDSP(u16, H_CS + tid * 2) = (u16)(pkbf(1.0f + __uint_as_float((u32)c), 0.f));
    *LDSP(u16, H_CS + 896 + tid * 2) = (u16)((u32)a >> 16);
  }
  vm0();
  lds_barrier();
  SST(4);
  {
    f32x4 a = {0, 0, 0, 0};
#pragma unroll
    for (int ks = 0; ks < 14; ++ks)
      a = MFMA16(*LDSP(u32x4, H_CS + (col & 1) * 896 + (32 * ks + 8 * g) * 2), frag128(smem, w < 4 ? H_WG : H_G2, lbase, phi, ks, w & 3), a);
    if (lane < 16) {
      float* uv = (float*)(p.ws + WS_UV) + (size_t)(w >> 2) * 2 * F + 64 * j + 16 * (w & 3) + lane;
      uv[0] = a[0]; uv[F] = a[1];
    }
  }
  SSTE(5); SEND(1, 12, 224 + j);
}

__global__ __launch_bounds__(512) void kAB(Params p) {
  extern __shared__ __attribute__((aligned(16))) char smem[];
  asm volatile("" :: "s"(p.u), "s"(p.A_re), "s"(p.A_im), "s"(p.B_re), "s"(p.B_im), "s"(p.C_re), "s"(p.C_im), "s"(p.log_dt), "s"(p.Dskip),
               "s"(p.w_gate), "s"(p.w_up), "s"(p.w_down), "s"(p.c), "s"(p.w_mod), "s"(p.ws));
  if (__builtin_expect(blockIdx.x >= 256 - AB_MAIN, 1)) conv_main(p, blockIdx.x - (256 - AB_MAIN), smem);
  else helper(p, blockIdx.x, smem);
}

constexpr int C_UV = 0, C_RES = 2048, C_XH1 = 10240, C_XH0 = C_XH1 + 65536, CD_LDS = C_XH0 + 65536;

__device__ __forceinline__ void stats_role(const Params& p, int j, char* smem) {
  const int tid = threadIdx.x, lane = tid & 63, w = tid >> 6;
  const int lc = tid & 7, dr = tid >> 3;
  const char* src = p.ws + WS_X1T + (size_t)(j >> 1) * D * 256 + (j & 1) * 128 + lc * 16;
  u32x4 v[7];
#pragma unroll
  for (int i = 0; i < 7; ++i) v[i] = *(const u32x4*)(src + (dr + 64 * i) * 256);
  float s[8] = {}, q[8] = {};
#pragma unroll
  for (int i = 0; i < 7; ++i)
#pragma unroll
    for (int k = 0; k < 4; ++k) {
      const float a = bflo(v[i][k]), b = bfhi(v[i][k]);
      s[2 * k] += a; q[2 * k] = fmaf(a, a, q[2 * k]); s[2 * k + 1] += b; q[2 * k + 1] = fmaf(b, b, q[2 * k + 1]);
    }
#pragma unroll
  for (int k = 0; k < 8; ++k) {
    s[k] += dppf<0x128>(s[k]); q[k] += dppf<0x128>(q[k]);
    s[k] = xrow16_sum(s[k]); q[k] = xrow16_sum(q[k]);
  }
  float* red = (float*)smem;
  if (lane < 8) {
    *(f32x4*)(red + (w * 2 + 0) * 64 + lane * 8) = f32x4{s[0], s[1], s[2], s[3]};
    *(f32x4*)(red + (w * 2 + 0) * 64 + lane * 8 + 4) = f32x4{s[4], s[5], s[6], s[7]};
    *(f32x4*)(red + (w * 2 + 1) * 64 + lane * 8) = f32x4{q[0], q[1], q[2], q[3]};
    *(f32x4*)(red + (w * 2 + 1) * 64 + lane * 8 + 4) = f32x4{q[4], q[5], q[6], q[7]};
  }
  __syncthreads();
  if (tid < 64) {
    float ss = 0.f, qq = 0.f;
#pragma unroll
    for (int u = 0; u < 8; ++u) { ss += red[(u * 2) * 64 + tid]; qq += red[(u * 2 + 1) * 64 + tid]; }
    const float mean = ss * (1.0f / D);
    const float var = fmaxf(qq * (1.0f / D) - mean * mean, 0.f);
    const float sd = sqrtf(bfr(bfr(var) + 1e-5f));
    const u32 word = pkbf(mean, sd);
    __hip_atomic_store((gu32*)(p.ws + WS_STG) + 64 * j + tid, word, __ATOMIC_RELAXED, __HIP_MEMORY_SCOPE_AGENT);
  }
}

#define C_LA 3
struct COp { int kind, arg; };
#define ROT 0
#define Y_LATE 1
constexpr COp c_opA(int ps, int ks, int g) {
  if (ps == 0) {
    if (g < 2 && ks + C_LA <= 13) return {1, 2 * (ks + C_LA) + g};
    if (ks == 0 && g >= 2) return {2, g};
    if (ks >= 1 && ks <= 3 && g == 2) return {2, ks + 3};
    if (ks == 1 && g == 3) return {5, 0};
    if (!Y_LATE && ks >= 7 && ks <= 10 && g == 2) return {6, ks - 7};
    if (Y_LATE && ks >= 12 && (g & 1) == 0) return {6, 2 * (ks - 12) + (g >> 1)};
  } else {
    if (g < 2) return {4, 2 * ks + g};
    if (ks <= 2 && g == 2) return {6, ks + 4};
    if (ks == 12 && g == 2) return {7, 0};
  }
  return {0, 0};
}
constexpr COp c_opB(int ps, int ks, int g) { return ks == (ps ? 10 : 11) ? COp{3, 4 * ps + g} : COp{0, 0}; }
struct CSched { int wait_x0, wait_w[14], wait_x3, wait_x7, wait_x13, wait_st0, wait_y3, wait_y7, wait_cnt; };
constexpr CSched c_make_sched() {
  CSched r{};
  int seqW[28] = {}, seqX[7] = {}, seqY[7] = {}, seqS[2] = {}, seqC = 0, n = 0;
  seqX[0] = seqX[1] = n++;
  for (int k = 0; k < 2 * C_LA; ++k) seqW[k] = n++;
  ++n;
  r.wait_x0 = n - 1 - seqX[1];
  for (int ps = 0; ps < 2; ++ps) {
    for (int ks = 0; ks < 14; ++ks) {
      if (ps == 0) {
        r.wait_w[ks] = n - 1 - seqW[2 * ks + 1];
        if (ks == 3) r.wait_x3 = n - 1 - seqX[3];
        if (ks == 7) r.wait_x7 = n - 1 - seqX[6];
        if (ks == 13) r.wait_x13 = n - 1 - seqY[3];
      } else if (ks == 7) r.wait_y7 = n - 1 - seqY[6];
      for (int g = 0; g < 4; ++g)
        for (int u = 0; u < 2; ++u) {
          const COp o = u ? c_opB(ps, ks, g) : c_opA(ps, ks, g);
          if (o.kind == 1) seqW[o.arg] = n;
          if (o.kind == 2) seqX[o.arg] = n;
          if (o.kind == 6) seqY[o.arg] = n;
          if (o.kind == 3) seqS[o.arg >> 2] = n;
          if (o.kind == 7) seqC = n;
          if (o.kind) ++n;
        }
    }
    if (ps == 0) { r.wait_st0 = n - 1 - seqS[0]; n += 4; r.wait_y3 = n - 1 - seqY[3]; }
  }
  r.wait_cnt = n - 1 - seqC;
  return r;
}
constexpr CSched C_SCHED = c_make_sched();
constexpr size_t WS_END = WS_HH + (size_t)L * F * 2;

typedef __amdgpu_buffer_rsrc_t rsrc_t;
__device__ __forceinline__ void bload16(u32x4& dst, rsrc_t r, u32 voff, u32 soff, int imm) {
  asm volatile("buffer_load_dwordx4 %0, %1, %2, %3 offen offset:%4" : "=v"(dst) : "v"(voff), "s"(r), "s"(soff), "i"(imm) : "memory");
}
__device__ __forceinline__ void bload16_sc1(u32x4& dst, rsrc_t r, u32 voff, u32 soff, int imm) {
  asm volatile("buffer_load_dwordx4 %0, %1, %2, %3 offen offset:%4 sc1" : "=v"(dst) : "v"(voff), "s"(r), "s"(soff), "i"(imm) : "memory");
}

__device__ __forceinline__ void pin(u32& x) { asm volatile("" : "+v"(x)); }
__device__ __forceinline__ void pin4(u32x4& x) { asm volatile("" : "+v"(x)); }
__device__ __forceinline__ void sbarrier() { asm volatile("s_barrier" ::: "memory"); }
__device__ __forceinline__ void bstore8_sc1(u32x2 v, rsrc_t r, u32 voff, u32 soff) {
  asm volatile("buffer_store_dwordx2 %0, %1, %2, %3 offen sc1" :: "v"(v), "v"(voff), "s"(r), "s"(soff) : "memory");
}
__device__ __forceinline__ void gload4_sc1(u32& dst, const void* sbase, u32 voff) {
  asm volatile("global_load_dword %0, %1, %2 sc1" : "=v"(dst) : "v"(voff), "s"(sbase) : "memory");
}
__global__ __launch_bounds__(512) void kCD(Params p) {
  extern __shared__ __attribute__((aligned(16))) char smem[];
  if (blockIdx.x < 32) { stats_role(p, blockIdx.x, smem); return; }
  const int bid = blockIdx.x - 32;
  const int tid = threadIdx.x, lane = tid & 63;
  const int w = __builtin_amdgcn_readfirstlane(tid >> 6);
  const int xcd = bid & 7, idx = bid >> 3, i7 = (idx * 37) >> 8, Q = idx - 7 * i7, sub = xcd & 1;
  const int Lt = 4 * (xcd >> 1) + i7, Fi = 7 * sub + Q;
  const unsigned lds0 = (unsigned)(uintptr_t)smem;
  const int g = lane >> 4, q4 = (lane & 15) >> 2, pp = lane & 3;
  const u32 l16 = lane * 16;
  const rsrc_t ws = __builtin_amdgcn_make_buffer_rsrc(p.ws, 0, (int)WS_END, 0x00020000);

  SDECL(14); SST(0);
  u32x4 wf[28], wd[28];
  const u32 dch = (lane & 7) ^ ((((lane >> 4) & 1) | ((w & 1) << 1)) << 1);
  const u32 xoff = (u32)(lane >> 3) * 256 + dch * 16;
  int off = ROT ? 2 * Q + ((7 * i7) >> 1) : 0;
  off -= off >= 14 ? 14 : 0;
  const int offw = off + (w >> 2);
  const u32 sx0 = (u32)WS_X1C + Lt * (D * 256) + (w & 3) * 2048, sl = lds0 + w * 1024;
  const u32 sw = (u32)WS_WF + (Fi * 8 + w) * (28 * 1024) + off * 2048;
  auto ld_w = [&](int n) { bload16(wf[n], ws, l16, sw - ((n >> 1) + off >= 14 ? 28 * 1024 : 0) + (n >> 2) * 4096, (n & 3) * 1024); };
  auto xsrc = [&](int o, int kp) { return sx0 + o * 8192 - (kp + o >= 14 ? D * 256 : 0); };
  if (w < 4) {
    sfor<0, 4>([&](auto I) { hdma16<I.value * 8192, C_XH0 + I.value * 4096>(ws, xoff, xsrc(off, I.value), sl); });
  }
#pragma unroll
  for (int n = 0; n < 2 * C_LA; ++n) ld_w(n);
  SB();
  if (lane < 16) glds16s(p.ws + WS_UV + ((size_t)(w >> 1) * F + 128 * Fi + 64 * (w & 1)) * 4, l16, lds0 + C_UV + w * 256);
  const u32 swd = (u32)WS_WDF + (Q * 8 + w) * (28 * 1024);
  const char* stsrc = p.ws + WS_STG + Lt * 512;
  gu32* cnt = (gu32*)(p.ws + WS_FLG) + Lt * 64;
  u32 stw[8], cn0;
  u32 jo[7], io[4];
#pragma unroll
  for (int j = 0; j < 7; ++j) { const int t = j + (ROT ? Q : 0); jo[j] = (u32)(t - (t >= 7 ? 7 : 0)) * 1024; PINS(jo[j]); }
#pragma unroll
  for (int i = 0; i < 4; ++i) { io[i] = swd + (u32)((i + (ROT ? i7 : 0)) & 3) * 7168; PINS(io[i]); }
  auto ld_wd = [&](int m) { bload16(wd[4 * (m % 7) + m / 7], ws, l16, io[m / 7] + jo[m % 7], 0); };
  auto ld_st = [&](int i) {
    gload4_sc1(stw[i], stsrc + (((i & 4) | ((i ^ w) & 3)) << 6), (u32)(lane & 15) * 4);
  };
  SST(1);
  f32x4 ag[4], au[4];
  unsigned ta[4];
  {
    const unsigned base = lds0 + C_XH0 + (unsigned)((8 * g + q4) * 128 + 8 * pp), phi = (q4 >> 1) | ((g & 1) << 1);
#pragma unroll
    for (int i = 0; i < 4; ++i) ta[i] = base + 32u * ((unsigned)(i ^ (w & 3)) ^ phi);
  }
  u32x4 xb[2][4];
  auto rd_tile = [&](int ks, int i, u32x4& dst) {
    lds_cptr a = (lds_cptr)(uintptr_t)ta[i] + ks * 4096;
    const u32x2 t0 = tr_read(a), t1 = tr_read(a + 512);
    dst = u32x4{t0[0], t0[1], t1[0], t1[1]};
  };
  const u32 hoff = (u32)((16 * (2 * (w & 1) + (g >> 1)) + (lane & 15)) * 16 + 8 * (g & 1));
  auto epilogue = [&](auto PS, auto&& hook) {
    constexpr int ps = PS.value;
#pragma unroll 1
    for (unsigned spins = 0; spins < (1u << 16); ++spins) {
      u32 mn = 0xffffffffu;
#pragma unroll
      for (int i = 0; i < 4; ++i) { pin(stw[4 * ps + i]); mn = min(mn, stw[4 * ps + i]); }
      if (__all(mn >= 0x10000u)) break;
#pragma unroll
      for (int i = 0; i < 4; ++i) ld_st(4 * ps + i);
      vmwait(0);
    }
    const float* uvl = (const float*)(smem + C_UV) + 16 * w + 4 * g;
    const f32x4 ug = *(const f32x4*)(uvl), uu = *(const f32x4*)(uvl + 256), vu = *(const f32x4*)(uvl + 384);
    const f32x4 vg = *(const f32x4*)(uvl + 128) * -1.4426950408889634f;
#pragma unroll
    for (int i = 0; i < 4; ++i) {
      const u32 so = (u32)WS_HH + ((Lt * 8 + 4 * ps + ((i ^ w) & 3)) * 56 + 4 * Fi + (w >> 1)) * 1024;
      const float m = bflo(stw[4 * ps + i]), rs = __builtin_amdgcn_rcpf(bfhi(stw[4 * ps + i]));
      const float rsg = rs * -1.4426950408889634f;
      f32x4 gv = (ag[i] - m * ug) * rsg + vg;
      f32x4 uv = (au[i] - m * uu) * rs + vu;
      f32x4 h;
#pragma unroll
      for (int r = 0; r < 4; ++r) h[r] = __builtin_amdgcn_rcpf(1.0f + __builtin_amdgcn_exp2f(gv[r])) * uv[r];
      const u32x2 o = {pkbf(h[0], h[1]), pkbf(h[2], h[3])};
      bstore8_sc1(o, ws, hoff, so);
      hook(i);
    }
  };
  vmw<C_SCHED.wait_x0>();
  asm volatile("s_barrier" ::: "memory");
#pragma unroll
  for (int i = 0; i < 4; ++i) rd_tile(0, i, xb[0][i]);
  sfor<0, 2>([&](auto PS) {
    constexpr int ps = PS.value;
    sfor<0, 14>([&](auto KS) {
      constexpr int ks = KS.value;
      if constexpr (ps == 0) { vmw<C_SCHED.wait_w[ks]>(); pin4(wf[2 * ks]); pin4(wf[2 * ks + 1]); }
      if constexpr (ps == 0 ? (ks == 3 || ks == 7 || (ks == 13 && !Y_LATE)) : ks == 7) {
        vmw<(ps ? C_SCHED.wait_y7 : ks == 3 ? C_SCHED.wait_x3 : ks == 7 ? C_SCHED.wait_x7 : C_SCHED.wait_x13)>();
        asm volatile("s_barrier" ::: "memory");
        if constexpr (ps == 1) { if (tid == 0) __hip_atomic_fetch_add(cnt, 1u, __ATOMIC_RELAXED, __HIP_MEMORY_SCOPE_AGENT); }
      }
      if constexpr (ps == 0 && ks == 13 && !Y_LATE) {
#pragma unroll
        for (int i = 0; i < 4; ++i) ta[i] -= (unsigned)(C_XH0 - C_XH1);
      }
      sfor<0, 4>([&](auto GI) {
        constexpr int i = GI.value;
        if constexpr (ks == 0) { mfma_first(ag[i], wf[0], xb[0][i]); mfma_first(au[i], wf[1], xb[0][i]); }
        else { mfma_acc(ag[i], wf[2 * ks], xb[ks & 1][i]); mfma_acc(au[i], wf[2 * ks + 1], xb[ks & 1][i]); }
        if constexpr ((ps == 0 && !Y_LATE) || ks < 13) rd_tile((ks + 1) % 14, i, xb[(ks + 1) & 1][i]);
        sfor<0, 2>([&](auto U) {
          constexpr COp o = U.value ? c_opB(ps, ks, i) : c_opA(ps, ks, i);
          if constexpr (o.kind == 1) ld_w(o.arg);
          if constexpr (o.kind == 2) hdma16<o.arg * 16384, C_XH0 + o.arg * 8192>(ws, xoff, xsrc(offw, 2 * o.arg), sl);
          if constexpr (o.kind == 6) hdma16<o.arg * 16384 + 128, C_XH1 + o.arg * 8192>(ws, xoff, xsrc(offw, 2 * o.arg), sl);
          if constexpr (o.kind == 3) ld_st(o.arg);
          if constexpr (o.kind == 4) ld_wd(o.arg);
          if constexpr (o.kind == 5)
            hdma16<0, C_RES>(ws, (u32)(lane >> 3) * 256 + (dch & 3) * 16 + (dch >> 2) * 128,
                             (u32)WS_X1T + (Lt * D + 64 * Q + 8 * w) * 256 + sub * 64, sl);
          if constexpr (o.kind == 7) gload4_sc1(cn0, (const void*)cnt, 0u);
        });
        __builtin_amdgcn_sched_barrier(0);
      });
    });
    mfma_fence();
    if constexpr (ps == 0) {
      SST(2);
      vmw<C_SCHED.wait_st0>();
      epilogue(PS, [&](int) {});
      if constexpr (Y_LATE) {
        vmw<C_SCHED.wait_y3>();
        sbarrier();
#pragma unroll
        for (int i = 0; i < 4; ++i) { ta[i] -= (unsigned)(C_XH0 - C_XH1); rd_tile(0, i, xb[0][i]); }
      }
      SST(3);
    }
  });
  SST(4);
  u32x4 hb[2][7], gbv;
  const int ilx = ROT ? Q & 1 : 0;
  u32 shl[2];
#pragma unroll
  for (int il = 0; il < 2; ++il) shl[il] = (u32)WS_HH + ((Lt * 8 + 2 * sub + (il ^ ilx)) * 56 + 7 * w) * 1024;
  auto ld_hq = [&](int ps, int q) {
    const int il = q / 7, j = q % 7;
    bload16_sc1(hb[il][j], ws, l16, shl[il] + jo[j], ps * 0);
  };
#define H_D 6
  auto gemm2 = [&](auto PS) {
    constexpr int ps = PS.value;
#pragma unroll
    for (int il = 0; il < 2; ++il) {
      f32x4 acc[4];
#pragma unroll
      for (int j = 0; j < 7; ++j) {
        const int q = 7 * il + j;
        if (ps == 0) vmwait(13 - q + (q < 4 ? 3 : q < 8 ? 2 : q < 11 ? 1 : 0));
        else vmwait((q + H_D - 1 < 13 ? H_D - 1 : 13 - q) + (q < H_D ? 1 : 0));
        pin4(hb[il][j]);
#pragma unroll
        for (int id = 0; id < 4; ++id) { if (j == 0) mfma_first(acc[id], wd[id], hb[il][0]); else mfma_acc(acc[id], wd[4 * j + id], hb[il][j]); }
        if (ps == 1 && q + H_D < 14) ld_hq(1, q + H_D);
        __builtin_amdgcn_sched_barrier(0);
      }
      mfma_fence();
#pragma unroll
      for (int id = 0; id < 4; ++id)
        *(f32x4*)(smem + (ps ? C_XH1 : C_XH0) + ((w * 2 + (il ^ ilx)) * 4 + ((id + (ROT ? i7 : 0)) & 3)) * 1024 + l16) = acc[id];
      __builtin_amdgcn_sched_barrier(0);
    }
  };
  const int dcol = 64 * Q + 16 * (w >> 1) + 4 * g;
  auto finish = [&](auto PS) {
    constexpr int ps = PS.value;
    f32x4 o = {0, 0, 0, 0};
    const char* rb = smem + (ps ? C_XH1 : C_XH0) + ((w & 1) * 4 + (w >> 1)) * 1024 + l16;
#pragma unroll
    for (int s = 0; s < 8; ++s) o += *(const f32x4*)(rb + s * 8192);
    const u32x2 xres = tr_read((lds_cptr)smem + C_RES + (16 * (w >> 1) + 4 * g + q4) * 128 + 32 * ((2 * ps + (w & 1)) ^ ((q4 >> 1) | ((g >> 1) << 1))) + 8 * pp);
    const f32x4 gb = __builtin_bit_cast(f32x4, gbv);
    float r[4];
#pragma unroll
    for (int k = 0; k < 4; ++k) r[k] = bfr(gb[k] * bfr(o[k]));
    const u32x2 ov = {pkbf(bflo(xres[0]) + r[0], bfhi(xres[0]) + r[1]), pkbf(bflo(xres[1]) + r[2], bfhi(xres[1]) + r[3])};
    const int lrow = 128 * Lt + 64 * ps + 32 * sub + 16 * (w & 1) + (lane & 15);
    *(u32x2*)(p.out + (size_t)lrow * D + dcol) = ov;
  };
  vmw<C_SCHED.wait_cnt>();
  PIN(cn0);
  if (__builtin_amdgcn_readfirstlane(cn0) < 14u) {
#pragma unroll 1
    for (unsigned spins = 0; spins < (1u << 20); ++spins) {
      if (__hip_atomic_load(cnt, __ATOMIC_RELAXED, __HIP_MEMORY_SCOPE_AGENT) >= 14u) break;
      __builtin_amdgcn_s_sleep(1);
    }
  }
  bload16(gbv, ws, (u32)dcol * 4, (u32)WS_MOD + 5 * D * 4, 0);
  epilogue(IC<1>{}, [&](int i) {
    const int q0 = i < 2 ? 4 * i : 8 + 3 * (i - 2), q1 = i < 2 ? q0 + 4 : q0 + 3;
#pragma unroll
    for (int q = q0; q < q1; ++q) ld_hq(0, q);
  });
  SST(5);
  vmw<3>();
  sbarrier();
  if (tid == 0) __hip_atomic_fetch_add(cnt + 32, 1u, __ATOMIC_RELAXED, __HIP_MEMORY_SCOPE_AGENT);
  SST(6);
#pragma unroll
  for (int n = 0; n < 28; ++n) PIN4(wd[n]);
  gemm2(IC<0>{});
  PIN4(gbv);
  SST(7);
  if (w == 0) {
#pragma unroll 1
    for (unsigned spins = 0; spins < (1u << 20); ++spins) {
      if (__hip_atomic_load(cnt + 32, __ATOMIC_RELAXED, __HIP_MEMORY_SCOPE_AGENT) >= 14u) break;
      __builtin_amdgcn_s_sleep(1);
    }
  }
  lds_barrier();
  SST(10);
  shl[0] += 4 * 56 * 1024; shl[1] += 4 * 56 * 1024;
#pragma unroll
  for (int q = 0; q < H_D; ++q) ld_hq(1, q);
  finish(IC<0>{});
  SST(9);
  gemm2(IC<1>{});
  SST(11);
  lds_barrier();
  finish(IC<1>{});
  SSTE(8); SEND(2, 14, bid);
}

extern "C" void kernel_launch(void* const* d_in, const int* in_sizes, int n_in, void* d_out, int out_size, void* d_ws,
                              size_t ws_size, hipStream_t stream) {
  Params p;
  p.u = (const u16*)d_in[0]; p.A_re = (const u16*)d_in[1]; p.A_im = (const u16*)d_in[2]; p.B_re = (const u16*)d_in[3];
  p.B_im = (const u16*)d_in[4]; p.C_re = (const u16*)d_in[5]; p.C_im = (const u16*)d_in[6]; p.log_dt = (const u16*)d_in[7];
  p.Dskip = (const u16*)d_in[8]; p.w_gate = (const u16*)d_in[10]; p.w_up = (const u16*)d_in[11]; p.w_down = (const u16*)d_in[12];
  p.c = (const u16*)d_in[13]; p.w_mod = (const u16*)d_in[14];
  p.ws = (char*)d_ws; p.out = (u16*)d_out;
  hipFuncSetAttribute((const void*)kAB, hipFuncAttributeMaxDynamicSharedMemorySize, AB_LDS);
  hipFuncSetAttribute((const void*)kCD, hipFuncAttributeMaxDynamicSharedMemorySize, CD_LDS);
  kAB<<<256, 512, AB_LDS, stream>>>(p);
  kCD<<<256, 512, CD_LDS, stream>>>(p);
}
```

```cpp
#define LAYOUT 2
#define Y_EPI 1
#define KA1 8
#define X1_AUX 16
#define EPI_PK 1
#define EPI_SB 1
#define LDS0 0u
#define PUB_ASM 1
#define YB -1
#define KARG_PF 1
#define CODE_PF 1
#include <hip/hip_runtime.h>
#include <hip/hip_bf16.h>
#include <stdint.h>

typedef unsigned short u16;
typedef unsigned int u32;
typedef __attribute__((ext_vector_type(8))) short s16x8;
typedef __attribute__((ext_vector_type(4))) short s16x4;
typedef __attribute__((ext_vector_type(8))) _Float16 f16x8;
typedef __attribute__((ext_vector_type(2))) _Float16 f16x2;
typedef __attribute__((ext_vector_type(2))) __bf16 bf16x2;
typedef __attribute__((ext_vector_type(8))) __bf16 bf16x8;
typedef __attribute__((ext_vector_type(2))) float f32x2;
typedef __attribute__((ext_vector_type(4))) float f32x4;
typedef __attribute__((ext_vector_type(16))) float f32x16;
typedef __attribute__((ext_vector_type(2))) unsigned u32x2;
typedef __attribute__((ext_vector_type(4))) unsigned u32x4;
typedef __attribute__((address_space(3))) char* lds_ptr;
typedef __attribute__((address_space(3))) const char* lds_cptr;

constexpr int L = 2048, D = 448, NS = 256, F = 1792;

constexpr size_t WS_EP = 0;
constexpr size_t WS_RD = 2048;
constexpr size_t WS_MG = 4096;
constexpr size_t WS_SG = 32768;
constexpr size_t WS_MOD = 49152;
constexpr size_t WS_FLG = 61440;
constexpr size_t WS_UV = 65536;
constexpr size_t WS_STG = 98304;
constexpr size_t WS_WDF = 131072;
constexpr size_t WS_WF = WS_WDF + (size_t)F * D * 2;
constexpr size_t WS_X1T = WS_WF + (size_t)2 * F * D * 2;
constexpr size_t WS_X1C = WS_X1T + (size_t)D * L * 2;
constexpr size_t WS_HH = WS_X1C + (size_t)D * L * 2;

struct Params {
  const u16* u; const u16* A_re; const u16* A_im; const u16* B_re; const u16* B_im; const u16* C_re; const u16* C_im;
  const u16* log_dt; const u16* Dskip; const u16* w_gate; const u16* w_up; const u16* w_down; const u16* c; const u16* w_mod;
  char* ws; u16* out;
};

#define SDECL(n) do {} while (0)
#define SST(i) do {} while (0)
#define SSTE(i) do {} while (0)
#define SEND(kid, n, bid) do {} while (0)
__device__ __forceinline__ float bf2f(u16 v) { return __uint_as_float((u32)v << 16); }
__device__ __forceinline__ float bflo(u32 v) { return __uint_as_float(v << 16); }
__device__ __forceinline__ float bfhi(u32 v) { return __uint_as_float(v & 0xffff0000u); }
__device__ __forceinline__ u32 pkbf(float lo, float hi) {
  f32x2 v = {lo, hi}; bf16x2 b = __builtin_convertvector(v, bf16x2); return __builtin_bit_cast(u32, b);
}
__device__ __forceinline__ float bfr(float x) { return bflo(pkbf(x, x)); }
__device__ __forceinline__ u32 pkh(float lo, float hi) {
  f32x2 v = {lo, hi}; f16x2 b = __builtin_convertvector(v, f16x2); return __builtin_bit_cast(u32, b);
}
template <int CTRL> __device__ __forceinline__ float dppf(float x) {
  return __builtin_bit_cast(float, __builtin_amdgcn_mov_dpp(__builtin_bit_cast(int, x), CTRL, 0xf, 0xf, true));
}
__device__ __forceinline__ float xrow16_sum(float x) {
  auto s = __builtin_amdgcn_permlane16_swap(__float_as_uint(x), __float_as_uint(x), false, false);
  x = __uint_as_float(s[0]) + __uint_as_float(s[1]);
  auto t = __builtin_amdgcn_permlane32_swap(__float_as_uint(x), __float_as_uint(x), false, false);
  return __uint_as_float(t[0]) + __uint_as_float(t[1]);
}
__device__ __forceinline__ float xrow16_max(float x) {
  auto s = __builtin_amdgcn_permlane16_swap(__float_as_uint(x), __float_as_uint(x), false, false);
  x = fmaxf(__uint_as_float(s[0]), __uint_as_float(s[1]));
  auto t = __builtin_amdgcn_permlane32_swap(__float_as_uint(x), __float_as_uint(x), false, false);
  return fmaxf(__uint_as_float(t[0]), __uint_as_float(t[1]));
}
__device__ __forceinline__ float wave_sum(float x) {
  x += dppf<0xB1>(x); x += dppf<0x4E>(x); x += dppf<0x141>(x); x += dppf<0x128>(x);
  return xrow16_sum(x);
}
__device__ __forceinline__ float wave_max(float x) {
  x = fmaxf(x, dppf<0xB1>(x)); x = fmaxf(x, dppf<0x4E>(x)); x = fmaxf(x, dppf<0x141>(x)); x = fmaxf(x, dppf<0x128>(x));
  return xrow16_max(x);
}
__device__ __forceinline__ float dot2bf(u32 a, u32 b, float acc) {
  return __builtin_amdgcn_fdot2_f32_bf16(__builtin_bit_cast(bf16x2, a), __builtin_bit_cast(bf16x2, b), acc, false);
}
typedef short v4i16_t __attribute__((ext_vector_type(4)));
__device__ __forceinline__ u32x2 tr_read(lds_cptr p) {
  return __builtin_bit_cast(u32x2, __builtin_amdgcn_ds_read_tr16_b64_v4i16((__attribute__((address_space(3))) v4i16_t*)p));
}
__device__ __forceinline__ void glds16(const void* g, unsigned lds_base) {
  unsigned sv;
  asm volatile("s_mov_b32 %0, m0\n\ts_mov_b32 m0, %2\n\ts_nop 0\n\tglobal_load_lds_dwordx4 %1, off\n\ts_mov_b32 m0, %0"
               : "=&s"(sv) : "v"(g), "s"(lds_base) : "memory");
}
__device__ __forceinline__ float pow2_scale(float x, int target) {
  const int e = (int)(__float_as_uint(x) >> 23);
  return __uint_as_float((u32)(127 + min(max(target + 127 - e, -100), 100)) << 23);
}
#define WT_AUX 16
__device__ __forceinline__ void gload16(u32x4& dst, const void* sbase, u32 voff, int imm) {
  asm volatile("global_load_dwordx4 %0, %1, %2 offset:%3" : "=v"(dst) : "v"(voff), "s"(sbase), "i"(imm) : "memory");
}
__device__ __forceinline__ void gload16_sc1(u32x4& dst, const void* sbase, u32 voff, int imm) {
  asm volatile("global_load_dwordx4 %0, %1, %2 offset:%3 sc1" : "=v"(dst) : "v"(voff), "s"(sbase), "i"(imm) : "memory");
}
__device__ __forceinline__ void glds16s(const void* sbase, u32 voff, unsigned lds_base) {
  asm volatile("s_mov_b32 m0, %2\n\ts_nop 0\n\tglobal_load_lds_dwordx4 %0, %1" :: "v"(voff), "s"(sbase), "s"(lds_base) : "memory");
}
#define MFMA16(a, b, c) __builtin_amdgcn_mfma_f32_16x16x32_bf16(__builtin_bit_cast(bf16x8, a), __builtin_bit_cast(bf16x8, b), c, 0, 0, 0)
__device__ __forceinline__ void mfma_acc(f32x4& c, const u32x4& a, const u32x4& b) {
  asm volatile("v_mfma_f32_16x16x32_bf16 %0, %1, %2, %0" : "+v"(c) : "v"(a), "v"(b));
}
__device__ __forceinline__ void mfma_first(f32x4& c, const u32x4& a, const u32x4& b) {
  asm volatile("v_mfma_f32_16x16x32_bf16 %0, %1, %2, 0" : "=&v"(c) : "v"(a), "v"(b));
}
__device__ __forceinline__ void mfma_fence() { asm volatile("s_nop 7\n\ts_nop 7" ::: "memory"); }
#define VMWAIT(n) case n: asm volatile("s_waitcnt vmcnt(" #n ")" ::: "memory"); break
__device__ __forceinline__ void vmwait(int n) {
  switch (n) {
    VMWAIT(0); VMWAIT(1); VMWAIT(2); VMWAIT(3); VMWAIT(4); VMWAIT(5); VMWAIT(6); VMWAIT(7); VMWAIT(8); VMWAIT(9);
    VMWAIT(10); VMWAIT(11); VMWAIT(12); VMWAIT(13); VMWAIT(14); VMWAIT(15); VMWAIT(16); VMWAIT(17); VMWAIT(18); VMWAIT(19);
    VMWAIT(20); VMWAIT(21); VMWAIT(22); VMWAIT(23); VMWAIT(24); VMWAIT(25); VMWAIT(26); VMWAIT(27); VMWAIT(28); VMWAIT(29);
    VMWAIT(30); VMWAIT(31); VMWAIT(32); VMWAIT(33); VMWAIT(34); VMWAIT(35); VMWAIT(36); VMWAIT(37); VMWAIT(38); VMWAIT(39);
    default: asm volatile("s_waitcnt vmcnt(0)" ::: "memory"); break;
  }
}
#define PIN4(x) asm volatile("" : "+v"(x))

typedef __attribute__((address_space(1))) unsigned gu32;
typedef __attribute__((address_space(1))) unsigned long long gu64;
constexpr u32 TAGC = 0x9E3779B9u;
constexpr int AB_MAIN = 224;
constexpr int T_RS = 1056, T_P = 32 * T_RS, T_CH = 53 * T_RS;
constexpr int L_R = 65536, R_CP = 4160, R_CH = 4 * R_CP;
constexpr int L_W = 2 * T_CH;
constexpr int L_KT = L_W + 24576, KT_Z = 2048, KT_CH = 2064;
constexpr int L_H = L_KT + 2 * KT_CH, H_CH = 4608;
constexpr int L_SC = (L_H + 2 * H_CH + 63) & ~63, AB_MAIN_LDS = L_SC + 64;

constexpr u32 kt_pos(int l) {
  if (l < 0) return KT_Z;
  int v = l;
  if (l >= 256) {
    const int s = l >= 1024 ? 3 : (l >= 512 ? 2 : 1), rem = l & ((1 << s) - 1), half = 1 << (s - 1);
    int q = l >> s;
    if (rem > half || (rem == half && (q & 1))) ++q;
    v = q << s;
  }
  int rr = 20, q = 0;
  if (v < 256) { rr = v & 7; q = v >> 3; }
  else if (v < 512) { rr = 8 + ((v >> 1) & 3); q = (v - 256) >> 3; }
  else if (v < 1024) { rr = 12 + 2 * ((v - 512) >> 8) + ((v >> 2) & 1); q = (v & 255) >> 3; }
  else if (v < 2048) { rr = 16 + ((v - 1024) >> 8); q = (v & 255) >> 3; }
  return (u32)(((rr >> 4) + 2 * (q >> 4)) * 512 + ((q & 15) + 16 * ((rr & 15) >> 2)) * 8 + 2 * (rr & 3));
}
struct GTab { u32 v[2][3][256][4]; };
constexpr GTab make_gtab() {
  GTab t{};
  for (int c = 0; c < 2; ++c)
    for (int i = 0; i < 256; ++i)
      for (int j = 0; j < 12; ++j) t.v[c][j >> 2][i][j & 3] = (u32)(L_KT + c * KT_CH) + kt_pos(2047 - 8 * i - j);
  return t;
}
static __device__ const GTab gtab = make_gtab();

__device__ __forceinline__ float rnd(float x) { return __uint_as_float(pkbf(0.f, x)); }
__device__ __forceinline__ f32x2 rnd2(f32x2 v) { return f32x2{rnd(v.x), rnd(v.y)}; }
__device__ __forceinline__ u32 pkh2(f32x2 v) { return __builtin_bit_cast(u32, __builtin_convertvector(v, f16x2)); }
struct cm { f32x2 v, n; };
__device__ __forceinline__ cm mkcm(f32x2 b) { return {b, f32x2{-b.y, b.x}}; }
__device__ __forceinline__ f32x2 cmul(f32x2 a, const cm& b) { return __builtin_elementwise_fma(a.yy, b.n, a.xx * b.v); }
__device__ __forceinline__ void cmul4(f32x2& o0, f32x2& o1, f32x2& o2, f32x2& o3, f32x2 a0, const cm& m0, f32x2 a1, const cm& m1, f32x2 a2, const cm& m2,
                                      f32x2 a3, const cm& m3) {
  f32x2 t0 = a0.xx * m0.v, t1 = a1.xx * m1.v, t2 = a2.xx * m2.v, t3 = a3.xx * m3.v;
  asm volatile("" : "+v"(t0), "+v"(t1), "+v"(t2), "+v"(t3));
  t0 = __builtin_elementwise_fma(a0.yy, m0.n, t0); t1 = __builtin_elementwise_fma(a1.yy, m1.n, t1);
  t2 = __builtin_elementwise_fma(a2.yy, m2.n, t2); t3 = __builtin_elementwise_fma(a3.yy, m3.n, t3);
  asm volatile("" : "+v"(t0), "+v"(t1), "+v"(t2), "+v"(t3));
  o0 = t0; o1 = t1; o2 = t2; o3 = t3;
}
__device__ __forceinline__ f32x2 cexpm(float zr, float zi, float m) {
  const float mag = __builtin_amdgcn_exp2f(zr * (m * 1.4426950408889634f));
  const float rev = __builtin_amdgcn_fractf(zi * (m * 0.15915494309189535f));
  return f32x2{mag * __builtin_amdgcn_cosf(rev), mag * __builtin_amdgcn_sinf(rev)};
}
__device__ __forceinline__ void st_gran(char* base, int idx, u32 val, u32 tag) {
  __hip_atomic_store((gu64*)base + idx, ((unsigned long long)tag << 32) | val, __ATOMIC_RELAXED, __HIP_MEMORY_SCOPE_AGENT);
}
template <int O0, int O1, int O2, int O3>
__device__ __forceinline__ void wr_rows4(u32 a, u32 b, u32 c, u32 d) {
  asm volatile("ds_write_addtid_b32 %0 offset:%4\n\tds_write_addtid_b32 %1 offset:%5\n\t"
               "ds_write_addtid_b32 %2 offset:%6\n\tds_write_addtid_b32 %3 offset:%7"
               :: "v"(a), "v"(b), "v"(c), "v"(d), "i"(O0), "i"(O1), "i"(O2), "i"(O3) : "memory");
}
template <int O0>
__device__ __forceinline__ void wr_rows1(u32 a) {
  asm volatile("ds_write_addtid_b32 %0 offset:%1" :: "v"(a), "i"(O0) : "memory");
}
__device__ __forceinline__ void table_q(f32x2 E8) {
  asm volatile("v_pk_mul_f32 v[232:233], v[226:227], v[226:227] op_sel_hi:[0,1]\n\tv_mov_b32 v240, 0x3c00\n\tv_cvt_pk_f16_f32 v241, v226, v227\n\tv_pk_fma_f32 v[228:229], v[226:227], v[226:227], v[232:233] op_sel:[1,1,0] op_sel_hi:[1,0,1] neg_lo:[0,1,0]\n\tds_write_addtid_b32 v240 offset:0\n\tds_write_addtid_b32 v241 offset:1056\n\tv_pk_mul_f32 v[232:233], v[228:229], v[226:227] op_sel_hi:[0,1]\n\tv_pk_mul_f32 v[234:235], v[228:229], v[228:229] op_sel_hi:[0,1]\n\tv_cvt_pk_f16_f32 v242, v228, v229\n\tv_pk_fma_f32 v[230:231], v[228:229], v[226:227], v[232:233] op_sel:[1,1,0] op_sel_hi:[1,0,1] neg_lo:[0,1,0]\n\tv_pk_fma_f32 v[244:245], v[228:229], v[228:229], v[234:235] op_sel:[1,1,0] op_sel_hi:[1,0,1] neg_lo:[0,1,0]\n\tds_write_addtid_b32 v242 offset:2112\n\tv_cvt_pk_f16_f32 v243, v230, v231\n\tv_pk_mul_f32 v[234:235], v[226:227], v[244:245] op_sel_hi:[0,1]\n\tv_pk_mul_f32 v[236:237], v[228:229], v[244:245] op_sel_hi:[0,1]\n\tv_pk_mul_f32 v[238:239], v[230:231], v[244:245] op_sel_hi:[0,1]\n\tds_write_addtid_b32 v243 offset:3168\n\tv_mov_b64 v[224:225], v[244:245]\n\tv_pk_fma_f32 v[226:227], v[226:227], v[244:245], v[234:235] op_sel:[1,1,0] op_sel_hi:[1,0,1] neg_lo:[0,1,0]\n\tv_pk_fma_f32 v[228:229], v[228:229], v[244:245], v[236:237] op_sel:[1,1,0] op_sel_hi:[1,0,1] neg_lo:[0,1,0]\n\tv_pk_fma_f32 v[230:231], v[230:231], v[244:245], v[238:239] op_sel:[1,1,0] op_sel_hi:[1,0,1] neg_lo:[0,1,0]\n\tv_cvt_pk_f16_f32 v240, v224, v225\n\tv_cvt_pk_f16_f32 v241, v226, v227\n\tv_cvt_pk_f16_f32 v242, v228, v229\n\tv_cvt_pk_f16_f32 v243, v230, v231\n\tv_pk_mul_f32 v[232:233], v[224:225], v[244:245] op_sel_hi:[0,1]\n\tv_pk_mul_f32 v[234:235], v[226:227], v[244:245] op_sel_hi:[0,1]\n\tv_pk_mul_f32 v[236:237], v[228:229], v[244:245] op_sel_hi:[0,1]\n\tv_pk_mul_f32 v[238:239], v[230:231], v[244:245] op_sel_hi:[0,1]\n\tds_write_addtid_b32 v240 offset:4224\n\tds_write_addtid_b32 v241 offset:5280\n\tds_write_addtid_b32 v242 offset:6336\n\tds_write_addtid_b32 v243 offset:7392\n\tv_pk_fma_f32 v[224:225], v[224:225], v[244:245], v[232:233] op_sel:[1,1,0] op_sel_hi:[1,0,1] neg_lo:[0,1,0]\n\tv_pk_fma_f32 v[226:227], v[226:227], v[244:245], v[234:235] op_sel:[1,1,0] op_sel_hi:[1,0,1] neg_lo:[0,1,0]\n\tv_pk_fma_f32 v[228:229], v[228:229], v[244:245], v[236:237] op_sel:[1,1,0] op_sel_hi:[1,0,1] neg_lo:[0,1,0]\n\tv_pk_fma_f32 v[230:231], v[230:231], v[244:245], v[238:239] op_sel:[1,1,0] op_sel_hi:[1,0,1] neg_lo:[0,1,0]\n\tv_cvt_pk_f16_f32 v240, v224, v225\n\tv_cvt_pk_f16_f32 v241, v226, v227\n\tv_cvt_pk_f16_f32 v242, v228, v229\n\tv_cvt_pk_f16_f32 v243, v230, v231\n\tv_pk_mul_f32 v[232:233], v[224:225], v[244:245] op_sel_hi:[0,1]\n\tv_pk_mul_f32 v[234:235], v[226:227], v[244:245] op_sel_hi:[0,1]\n\tv_pk_mul_f32 v[236:237], v[228:229], v[244:245] op_sel_hi:[0,1]\n\tv_pk_mul_f32 v[238:239], v[230:231], v[244:245] op_sel_hi:[0,1]\n\tds_write_addtid_b32 v240 offset:8448\n\tds_write_addtid_b32 v241 offset:9504\n\tds_write_addtid_b32 v242 offset:10560\n\tds_write_addtid_b32 v243 offset:11616\n\tv_pk_fma_f32 v[224:225], v[224:225], v[244:245], v[232:233] op_sel:[1,1,0] op_sel_hi:[1,0,1] neg_lo:[0,1,0]\n\tv_pk_fma_f32 v[226:227], v[226:227], v[244:245], v[234:235] op_sel:[1,1,0] op_sel_hi:[1,0,1] neg_lo:[0,1,0]\n\tv_pk_fma_f32 v[228:229], v[228:229], v[244:245], v[236:237] op_sel:[1,1,0] op_sel_hi:[1,0,1] neg_lo:[0,1,0]\n\tv_pk_fma_f32 v[230:231], v[230:231], v[244:245], v[238:239] op_sel:[1,1,0] op_sel_hi:[1,0,1] neg_lo:[0,1,0]\n\tv_cvt_pk_f16_f32 v240, v224, v225\n\tv_cvt_pk_f16_f32 v241, v226, v227\n\tv_cvt_pk_f16_f32 v242, v228, v229\n\tv_cvt_pk_f16_f32 v243, v230, v231\n\tv_pk_mul_f32 v[232:233], v[224:225], v[244:245] op_sel_hi:[0,1]\n\tv_pk_mul_f32 v[234:235], v[226:227], v[244:245] op_sel_hi:[0,1]\n\tv_pk_mul_f32 v[236:237], v[228:229], v[244:245] op_sel_hi:[0,1]\n\tv_pk_mul_f32 v[238:239], v[230:231], v[244:245] op_sel_hi:[0,1]\n\tds_write_addtid_b32 v240 offset:12672\n\tds_write_addtid_b32 v241 offset:13728\n\tds_write_addtid_b32 v242 offset:14784\n\tds_write_addtid_b32 v243 offset:15840\n\tv_pk_fma_f32 v[224:225], v[224:225], v[244:245], v[232:233] op_sel:[1,1,0] op_sel_hi:[1,0,1] neg_lo:[0,1,0]\n\tv_pk_fma_f32 v[226:227], v[226:227], v[244:245], v[234:235] op_sel:[1,1,0] op_sel_hi:[1,0,1] neg_lo:[0,1,0]\n\tv_pk_fma_f32 v[228:229], v[228:229], v[244:245], v[236:237] op_sel:[1,1,0] op_sel_hi:[1,0,1] neg_lo:[0,1,0]\n\tv_pk_fma_f32 v[230:231], v[230:231], v[244:245], v[238:239] op_sel:[1,1,0] op_sel_hi:[1,0,1] neg_lo:[0,1,0]\n\tv_cvt_pk_f16_f32 v240, v224, v225\n\tv_cvt_pk_f16_f32 v241, v226, v227\n\tv_cvt_pk_f16_f32 v242, v228, v229\n\tv_cvt_pk_f16_f32 v243, v230, v231\n\tv_pk_mul_f32 v[232:233], v[224:225], v[244:245] op_sel_hi:[0,1]\n\tv_pk_mul_f32 v[234:235], v[226:227], v[244:245] op_sel_hi:[0,1]\n\tv_pk_mul_f32 v[236:237], v[228:229], v[244:245] op_sel_hi:[0,1]\n\tv_pk_mul_f32 v[238:239], v[230:231], v[244:245] op_sel_hi:[0,1]\n\tds_write_addtid_b32 v240 offset:16896\n\tds_write_addtid_b32 v241 offset:17952\n\tds_write_addtid_b32 v242 offset:19008\n\tds_write_addtid_b32 v243 offset:20064\n\tv_pk_fma_f32 v[224:225], v[224:225], v[244:245], v[232:233] op_sel:[1,1,0] op_sel_hi:[1,0,1] neg_lo:[0,1,0]\n\tv_pk_fma_f32 v[226:227], v[226:227], v[244:245], v[234:235] op_sel:[1,1,0] op_sel_hi:[1,0,1] neg_lo:[0,1,0]\n\tv_pk_fma_f32 v[228:229], v[228:229], v[244:245], v[236:237] op_sel:[1,1,0] op_sel_hi:[1,0,1] neg_lo:[0,1,0]\n\tv_pk_fma_f32 v[230:231], v[230:231], v[244:245], v[238:239] op_sel:[1,1,0] op_sel_hi:[1,0,1] neg_lo:[0,1,0]\n\tv_cvt_pk_f16_f32 v240, v224, v225\n\tv_cvt_pk_f16_f32 v241, v226, v227\n\tv_cvt_pk_f16_f32 v242, v228, v229\n\tv_cvt_pk_f16_f32 v243, v230, v231\n\tv_pk_mul_f32 v[232:233], v[224:225], v[244:245] op_sel_hi:[0,1]\n\tv_pk_mul_f32 v[234:235], v[226:227], v[244:245] op_sel_hi:[0,1]\n\tv_pk_mul_f32 v[236:237], v[228:229], v[244:245] op_sel_hi:[0,1]\n\tv_pk_mul_f32 v[238:239], v[230:231], v[244:245] op_sel_hi:[0,1]\n\tds_write_addtid_b32 v240 offset:21120\n\tds_write_addtid_b32 v241 offset:22176\n\tds_write_addtid_b32 v242 offset:23232\n\tds_write_addtid_b32 v243 offset:24288\n\tv_pk_fma_f32 v[224:225], v[224:225], v[244:245], v[232:233] op_sel:[1,1,0] op_sel_hi:[1,0,1] neg_lo:[0,1,0]\n\tv_pk_fma_f32 v[226:227], v[226:227], v[244:245], v[234:235] op_sel:[1,1,0] op_sel_hi:[1,0,1] neg_lo:[0,1,0]\n\tv_pk_fma_f32 v[228:229], v[228:229], v[244:245], v[236:237] op_sel:[1,1,0] op_sel_hi:[1,0,1] neg_lo:[0,1,0]\n\tv_pk_fma_f32 v[230:231], v[230:231], v[244:245], v[238:239] op_sel:[1,1,0] op_sel_hi:[1,0,1] neg_lo:[0,1,0]\n\tv_cvt_pk_f16_f32 v240, v224, v225\n\tv_cvt_pk_f16_f32 v241, v226, v227\n\tv_cvt_pk_f16_f32 v242, v228, v229\n\tv_cvt_pk_f16_f32 v243, v230, v231\n\tv_pk_mul_f32 v[232:233], v[224:225], v[244:245] op_sel_hi:[0,1]\n\tv_pk_mul_f32 v[234:235], v[226:227], v[244:245] op_sel_hi:[0,1]\n\tv_pk_mul_f32 v[236:237], v[228:229], v[244:245] op_sel_hi:[0,1]\n\tv_pk_mul_f32 v[238:239], v[230:231], v[244:245] op_sel_hi:[0,1]\n\tds_write_addtid_b32 v240 offset:25344\n\tds_write_addtid_b32 v241 offset:26400\n\tds_write_addtid_b32 v242 offset:27456\n\tds_write_addtid_b32 v243 offset:28512\n\tv_pk_fma_f32 v[224:225], v[224:225], v[244:245], v[232:233] op_sel:[1,1,0] op_sel_hi:[1,0,1] neg_lo:[0,1,0]\n\tv_pk_fma_f32 v[226:227], v[226:227], v[244:245], v[234:235] op_sel:[1,1,0] op_sel_hi:[1,0,1] neg_lo:[0,1,0]\n\tv_pk_fma_f32 v[228:229], v[228:229], v[244:245], v[236:237] op_sel:[1,1,0] op_sel_hi:[1,0,1] neg_lo:[0,1,0]\n\tv_pk_fma_f32 v[230:231], v[230:231], v[244:245], v[238:239] op_sel:[1,1,0] op_sel_hi:[1,0,1] neg_lo:[0,1,0]\n\tv_cvt_pk_f16_f32 v240, v224, v225\n\tv_cvt_pk_f16_f32 v241, v226, v227\n\tv_cvt_pk_f16_f32 v242, v228, v229\n\tv_cvt_pk_f16_f32 v243, v230, v231\n\tds_write_addtid_b32 v240 offset:29568\n\tds_write_addtid_b32 v241 offset:30624\n\tds_write_addtid_b32 v242 offset:31680\n\tds_write_addtid_b32 v243 offset:32736"
               : "+{v[226:227]}"(E8) :: "v224", "v225", "v228", "v229", "v230", "v231", "v232", "v233", "v234", "v235", "v236", "v237", "v238", "v239", "v240", "v241", "v242", "v243", "v244", "v245", "memory");
}
__device__ __forceinline__ void table_p(f32x2 W, f32x2 E1, f32x2 E256) {
  asm volatile("v_pk_mul_f32 v[238:239], v[226:227], v[226:227] op_sel_hi:[0,1]\n\tv_pk_mul_f32 v[240:241], v[228:229], v[228:229] op_sel_hi:[0,1]\n\tv_pk_mul_f32 v[242:243], v[224:225], v[226:227] op_sel_hi:[0,1]\n\tv_pk_mul_f32 v[244:245], v[224:225], v[228:229] op_sel_hi:[0,1]\n\tv_cvt_pk_f16_f32 v200, v224, v225\n\tv_pk_fma_f32 v[208:209], v[226:227], v[226:227], v[238:239] op_sel:[1,1,0] op_sel_hi:[1,0,1] neg_lo:[0,1,0]\n\tv_pk_fma_f32 v[212:213], v[228:229], v[228:229], v[240:241] op_sel:[1,1,0] op_sel_hi:[1,0,1] neg_lo:[0,1,0]\n\tv_pk_fma_f32 v[216:217], v[224:225], v[226:227], v[242:243] op_sel:[1,1,0] op_sel_hi:[1,0,1] neg_lo:[0,1,0]\n\tv_pk_fma_f32 v[222:223], v[224:225], v[228:229], v[244:245] op_sel:[1,1,0] op_sel_hi:[1,0,1] neg_lo:[0,1,0]\n\tds_write_addtid_b32 v200 offset:33792\n\tv_pk_mul_f32 v[238:239], v[208:209], v[208:209] op_sel_hi:[0,1]\n\tv_pk_mul_f32 v[240:241], v[212:213], v[212:213] op_sel_hi:[0,1]\n\tv_pk_mul_f32 v[242:243], v[224:225], v[208:209] op_sel_hi:[0,1]\n\tv_pk_mul_f32 v[244:245], v[224:225], v[212:213] op_sel_hi:[0,1]\n\tv_cvt_pk_f16_f32 v201, v216, v217\n\tv_cvt_pk_f16_f32 v202, v222, v223\n\tv_pk_fma_f32 v[210:211], v[208:209], v[208:209], v[238:239] op_sel:[1,1,0] op_sel_hi:[1,0,1] neg_lo:[0,1,0]\n\tv_pk_fma_f32 v[214:215], v[212:213], v[212:213], v[240:241] op_sel:[1,1,0] op_sel_hi:[1,0,1] neg_lo:[0,1,0]\n\tv_pk_fma_f32 v[218:219], v[224:225], v[208:209], v[242:243] op_sel:[1,1,0] op_sel_hi:[1,0,1] neg_lo:[0,1,0]\n\tv_pk_fma_f32 v[232:233], v[224:225], v[212:213], v[244:245] op_sel:[1,1,0] op_sel_hi:[1,0,1] neg_lo:[0,1,0]\n\tds_write_addtid_b32 v201 offset:34848\n\tds_write_addtid_b32 v202 offset:42240\n\tv_pk_mul_f32 v[238:239], v[216:217], v[208:209] op_sel_hi:[0,1]\n\tv_pk_mul_f32 v[240:241], v[222:223], v[208:209] op_sel_hi:[0,1]\n\tv_pk_mul_f32 v[242:243], v[222:223], v[212:213] op_sel_hi:[0,1]\n\tv_pk_mul_f32 v[244:245], v[224:225], v[214:215] op_sel_hi:[0,1]\n\tv_cvt_pk_f16_f32 v203, v218, v219\n\tv_cvt_pk_f16_f32 v204, v232, v233\n\tv_pk_mul_f32 v[246:247], v[224:225], v[210:211] op_sel_hi:[0,1]\n\tv_pk_mul_f32 v[248:249], v[216:217], v[210:211] op_sel_hi:[0,1]\n\tv_pk_mul_f32 v[250:251], v[218:219], v[210:211] op_sel_hi:[0,1]\n\tv_pk_mul_f32 v[252:253], v[222:223], v[210:211] op_sel_hi:[0,1]\n\tv_pk_fma_f32 v[220:221], v[216:217], v[208:209], v[238:239] op_sel:[1,1,0] op_sel_hi:[1,0,1] neg_lo:[0,1,0]\n\tv_pk_fma_f32 v[230:231], v[222:223], v[208:209], v[240:241] op_sel:[1,1,0] op_sel_hi:[1,0,1] neg_lo:[0,1,0]\n\tv_pk_fma_f32 v[234:235], v[222:223], v[212:213], v[242:243] op_sel:[1,1,0] op_sel_hi:[1,0,1] neg_lo:[0,1,0]\n\tv_pk_fma_f32 v[236:237], v[224:225], v[214:215], v[244:245] op_sel:[1,1,0] op_sel_hi:[1,0,1] neg_lo:[0,1,0]\n\tds_write_addtid_b32 v203 offset:35904\n\tds_write_addtid_b32 v204 offset:46464\n\tv_pk_fma_f32 v[246:247], v[224:225], v[210:211], v[246:247] op_sel:[1,1,0] op_sel_hi:[1,0,1] neg_lo:[0,1,0]\n\tv_pk_fma_f32 v[248:249], v[216:217], v[210:211], v[248:249] op_sel:[1,1,0] op_sel_hi:[1,0,1] neg_lo:[0,1,0]\n\tv_pk_fma_f32 v[250:251], v[218:219], v[210:211], v[250:251] op_sel:[1,1,0] op_sel_hi:[1,0,1] neg_lo:[0,1,0]\n\tv_pk_fma_f32 v[252:253], v[222:223], v[210:211], v[252:253] op_sel:[1,1,0] op_sel_hi:[1,0,1] neg_lo:[0,1,0]\n\tv_pk_mul_f32 v[238:239], v[220:221], v[210:211] op_sel_hi:[0,1]\n\tv_pk_mul_f32 v[240:241], v[230:231], v[210:211] op_sel_hi:[0,1]\n\tv_pk_mul_f32 v[242:243], v[232:233], v[210:211] op_sel_hi:[0,1]\n\tv_pk_mul_f32 v[244:245], v[234:235], v[210:211] op_sel_hi:[0,1]\n\tv_cvt_pk_f16_f32 v200, v220, v221\n\tv_cvt_pk_f16_f32 v201, v230, v231\n\tv_cvt_pk_f16_f32 v202, v234, v235\n\tv_cvt_pk_f16_f32 v205, v236, v237\n\tv_cvt_pk_f16_f32 v206, v246, v247\n\tv_cvt_pk_f16_f32 v207, v248, v249\n\tv_cvt_pk_f16_f32 v203, v250, v251\n\tv_cvt_pk_f16_f32 v204, v252, v253\n\tv_pk_fma_f32 v[238:239], v[220:221], v[210:211], v[238:239] op_sel:[1,1,0] op_sel_hi:[1,0,1] neg_lo:[0,1,0]\n\tv_pk_fma_f32 v[240:241], v[230:231], v[210:211], v[240:241] op_sel:[1,1,0] op_sel_hi:[1,0,1] neg_lo:[0,1,0]\n\tv_pk_fma_f32 v[242:243], v[232:233], v[210:211], v[242:243] op_sel:[1,1,0] op_sel_hi:[1,0,1] neg_lo:[0,1,0]\n\tv_pk_fma_f32 v[244:245], v[234:235], v[210:211], v[244:245] op_sel:[1,1,0] op_sel_hi:[1,0,1] neg_lo:[0,1,0]\n\tds_write_addtid_b32 v200 offset:36960\n\tds_write_addtid_b32 v201 offset:43296\n\tds_write_addtid_b32 v202 offset:48576\n\tds_write_addtid_b32 v205 offset:50688\n\tv_pk_mul_f32 v[246:247], v[222:223], v[214:215] op_sel_hi:[0,1]\n\tv_pk_mul_f32 v[248:249], v[232:233], v[214:215] op_sel_hi:[0,1]\n\tv_pk_mul_f32 v[250:251], v[234:235], v[214:215] op_sel_hi:[0,1]\n\tv_pk_mul_f32 v[252:253], v[236:237], v[214:215] op_sel_hi:[0,1]\n\tds_write_addtid_b32 v206 offset:38016\n\tds_write_addtid_b32 v207 offset:39072\n\tds_write_addtid_b32 v203 offset:40128\n\tds_write_addtid_b32 v204 offset:44352\n\tv_cvt_pk_f16_f32 v200, v238, v239\n\tv_cvt_pk_f16_f32 v201, v240, v241\n\tv_cvt_pk_f16_f32 v202, v242, v243\n\tv_cvt_pk_f16_f32 v205, v244, v245\n\tv_pk_fma_f32 v[246:247], v[222:223], v[214:215], v[246:247] op_sel:[1,1,0] op_sel_hi:[1,0,1] neg_lo:[0,1,0]\n\tv_pk_fma_f32 v[248:249], v[232:233], v[214:215], v[248:249] op_sel:[1,1,0] op_sel_hi:[1,0,1] neg_lo:[0,1,0]\n\tv_pk_fma_f32 v[250:251], v[234:235], v[214:215], v[250:251] op_sel:[1,1,0] op_sel_hi:[1,0,1] neg_lo:[0,1,0]\n\tv_pk_fma_f32 v[252:253], v[236:237], v[214:215], v[252:253] op_sel:[1,1,0] op_sel_hi:[1,0,1] neg_lo:[0,1,0]\n\tds_write_addtid_b32 v200 offset:41184\n\tds_write_addtid_b32 v201 offset:45408\n\tds_write_addtid_b32 v202 offset:47520\n\tds_write_addtid_b32 v205 offset:49632\n\tv_cvt_pk_f16_f32 v206, v246, v247\n\tv_cvt_pk_f16_f32 v207, v248, v249\n\tv_cvt_pk_f16_f32 v203, v250, v251\n\tv_cvt_pk_f16_f32 v204, v252, v253\n\tds_write_addtid_b32 v206 offset:51744\n\tds_write_addtid_b32 v207 offset:52800\n\tds_write_addtid_b32 v203 offset:53856\n\tds_write_addtid_b32 v204 offset:54912"
               :: "{v[224:225]}"(W), "{v[226:227]}"(E1), "{v[228:229]}"(E256) : "v200", "v201", "v202", "v203", "v204", "v205", "v206", "v207", "v208", "v209", "v210", "v211", "v212", "v213", "v214", "v215", "v216", "v217", "v218", "v219", "v220", "v221", "v222", "v223", "v230", "v231", "v232", "v233", "v234", "v235", "v236", "v237", "v238", "v239", "v240", "v241", "v242", "v243", "v244", "v245", "v246", "v247", "v248", "v249", "v250", "v251", "v252", "v253", "memory");
}
__device__ __forceinline__ void lds_barrier() { asm volatile("s_waitcnt lgkmcnt(0)\n\ts_barrier" ::: "memory"); }
#define LDSP(T, off) ((__attribute__((address_space(3))) T*)((lds_ptr)smem + (off)))
#define MFMA16H(a, b, c) __builtin_amdgcn_mfma_f32_16x16x32_f16(__builtin_bit_cast(f16x8, a), __builtin_bit_cast(f16x8, b), c, 0, 0, 0)
__device__ __forceinline__ void glds16v(const void* sbase, u32 voff, unsigned lds_base) {
  asm volatile("s_mov_b32 m0, %2\n\ts_nop 0\n\tglobal_load_lds_dwordx4 %0, %1" :: "v"(voff), "s"(sbase), "s"(lds_base) : "memory");
}
__device__ __forceinline__ void reload16_sc1(u32x4& dst, const void* sbase, u32 voff, int imm) {
  asm volatile("global_load_dwordx4 %0, %1, %2 offset:%3 sc1" : "+v"(dst) : "v"(voff), "s"(sbase), "i"(imm) : "memory");
}
__device__ __forceinline__ void vm0() { asm volatile("s_waitcnt vmcnt(0)" ::: "memory"); }
template <int N> __device__ __forceinline__ void vmw() { asm volatile("s_waitcnt vmcnt(%0)" :: "i"(N) : "memory"); }
#define PIN(x) asm volatile("" : "+v"(x))
#define SB() __builtin_amdgcn_sched_barrier(0)
__device__ __forceinline__ u32x4 frag128(const char* smem, int base, int lbase, int phi, int ks, int t) {
  lds_cptr a = (lds_cptr)smem + base + lbase + 32 * (t ^ phi) + ks * 4096;
  const u32x2 t0 = tr_read(a), t1 = tr_read(a + 512);
  return u32x4{t0[0], t0[1], t1[0], t1[1]};
}
__device__ __forceinline__ void st16_wt(const __amdgpu_buffer_rsrc_t& r, int off, u32x4 v) { __builtin_amdgcn_raw_buffer_store_b128(v, r, off, 0, WT_AUX); }
__device__ __forceinline__ u32 cld32(const void* q) { return *(const __attribute__((address_space(4))) u32*)(uintptr_t)q; }
__device__ __forceinline__ float pow2_inv(float s) { return __uint_as_float(0x7F000000u - __float_as_uint(s)); }
__device__ __forceinline__ float rlane(u32 v, int l) { return __uint_as_float(__builtin_amdgcn_readlane(v, l)); }
#define LDS4(adr) (*(const __attribute__((address_space(3))) u32x4*)(adr))
#define LDS2(adr) (*(const __attribute__((address_space(3))) u32x2*)(adr))

__device__ __forceinline__ float sel4(const f32x4& a, int r) {
  const float lo = (r & 1) ? a[1] : a[0], hi = (r & 1) ? a[3] : a[2];
  return (r & 2) ? hi : lo;
}
__device__ __forceinline__ void ln_rows(const Params& p, u32x4 (&xr)[14], int row16, int lane, u32 tag) {
  const int col = lane & 15, g = lane >> 4;
  f32x4 ag = {0, 0, 0, 0}, as = {0, 0, 0, 0};
  const u32x4 ones = {0x3F803F80u, 0x3F803F80u, 0x3F803F80u, 0x3F803F80u};
#pragma unroll
  for (int s = 0; s < 14; ++s) { PIN4(xr[s]); ag = MFMA16(xr[s], xr[s], ag); as = MFMA16(xr[s], ones, as); }
  const float qq = sel4(ag, col), ss = sel4(as, col);
  const float mean = ss * (1.0f / D);
  const float var = fmaxf(qq * (1.0f / D) - mean * mean, 0.f);
  const float sd = __builtin_amdgcn_sqrtf(rnd(rnd(var) + 1e-5f));
  if (g == (col >> 2)) st_gran(p.ws + WS_SG, row16 + col, pkbf(mean, sd), tag);
}
template <int N> __device__ __forceinline__ void vmw_pin(u32x4& a) { asm volatile("s_waitcnt vmcnt(%1)" : "+v"(a) : "i"(N) : "memory"); }
template <int N> __device__ __forceinline__ void vmw_pin(u32x4& a, u32x4& b) { asm volatile("s_waitcnt vmcnt(%2)" : "+v"(a), "+v"(b) : "i"(N) : "memory"); }
#define LDS4U(adr) (*(const __attribute__((address_space(3))) u32x4 __attribute__((aligned(8)))*)(adr))
typedef u32x4 __attribute__((aligned(8))) u32x4_a8;

#define PINS(x) asm volatile("" : "+s"(x))
template <int OFF> __device__ __forceinline__ void lds_rd64(u32x2& dst, u32 adr) {
  asm volatile("ds_read_b64 %0, %1 offset:%2" : "=v"(dst) : "v"(adr), "i"(OFF) : "memory");
}
__device__ __forceinline__ void swap16(u32& a, u32& b) {
  const auto s = __builtin_amdgcn_permlane16_swap(a, b, false, false);
  a = s[0]; b = s[1];
}
template <int V> struct IC { static constexpr int value = V; };
template <int I, int N, class Fn> __device__ __forceinline__ void sfor(Fn&& f) {
  if constexpr (I < N) { f(IC<I>{}); sfor<I + 1, N>(f); }
}
__device__ __forceinline__ void pin(u32& x) { asm volatile("" : "+v"(x)); }
__device__ __forceinline__ void pin4(u32x4& x) { asm volatile("" : "+v"(x)); }
__device__ __forceinline__ void pin2(u32x2& x) { asm volatile("" : "+v"(x)); }
template <int OFF> __device__ __forceinline__ void lds_r128(u32x4& dst, u32 adr) {
  asm volatile("ds_read_b128 %0, %1 offset:%2" : "=v"(dst) : "v"(adr), "i"(OFF) : "memory");
}
template <int OFF> __device__ __forceinline__ void lds_rtr(u32x2& dst, u32 adr) {
  asm volatile("ds_read_b64_tr_b16 %0, %1 offset:%2" : "=v"(dst) : "v"(adr), "i"(OFF) : "memory");
}
template <int N> __device__ __forceinline__ void lgw() { asm volatile("s_waitcnt lgkmcnt(%0)" :: "i"(N > 15 ? 15 : N) : "memory"); }
#define XG_POL ""
#define W_ST 4

__device__ __forceinline__ void conv_main(const Params& p, int b, char* smem) {
  const int tid = threadIdx.x, lane = tid & 63;
  const int w = __builtin_amdgcn_readfirstlane(tid >> 6), ch = w >> 2, pq = w & 3;
  const int cp = (b & 7) * 28 + (b >> 3), d0 = 2 * cp, d = d0 + ch;
  const int col = lane & 15, g = lane >> 4;
  const unsigned lds0 = LDS0;
  SDECL(14); SST(0);
  const bool ln = w == 0 && b < 128;
  u32x4 xr[14];
  if (ln) {
    const char* xs = (const char*)(p.u + (size_t)(16 * b) * D);
    const u32 xo = (u32)(col * D + 8 * g) * 2;
#pragma unroll
    for (int s = 0; s < 14; ++s) gload16(xr[s], xs, xo, s * 64);
  }
  u32 pa[6];
  {
    const u32 po = (u32)(d * NS + pq * 64 + lane) * 2;
    const u16* const ps[6] = {p.A_re, p.A_im, p.B_re, p.B_im, p.C_re, p.C_im};
#pragma unroll
    for (int i = 0; i < 6; ++i) asm volatile("global_load_short_d16_hi %0, %1, %2" : "=v"(pa[i]) : "v"(po), "s"(ps[i]) : "memory");
  }
  const u32 ldt2 = cld32(p.log_dt + d0), dsk2 = cld32(p.Dskip + d0), tag = cld32(p.ws + WS_EP) + TAGC;
  asm volatile("s_barrier" ::: "memory");
  const int row0 = 256 * w + 16 * col + 4 * g;
  const u32 xo4 = (u32)row0 * (D * 2) + cp * 4;
  u32 xg[4];
  u32x4 gt[3];
#pragma unroll
  for (int i = 0; i < 3; ++i) gload16(gt[i], &gtab.v[ch][i][0][0], (u32)(tid & 255) * 16, 0);
  asm volatile("global_load_dword %0, %1, %2" XG_POL : "=v"(xg[0]) : "v"(xo4), "s"(p.u) : "memory");
  asm volatile("global_load_dword %0, %1, %2 offset:896" XG_POL : "=v"(xg[1]) : "v"(xo4), "s"(p.u) : "memory");
  const int wt = b < 196 ? b : b - 196, wta = wt / 7, wtb = wt - 7 * wta;
  const u16* wsrc0 = p.w_gate + (size_t)(64 * wtb + 8 * w) * F + 64 * wta;
  const u16* wsrc1 = p.w_up + (size_t)(64 * wtb + 8 * w) * F + 64 * wta;
  const u16* wsrc2 = p.w_down + (size_t)(64 * wta + 8 * w) * D + 64 * wtb;
  u32 vf, vd;
  {
    const int dchunk = (lane & 7) ^ ((((lane >> 4) & 1) | ((w & 1) << 1)) << 1);
    vf = (u32)((lane >> 3) * F + dchunk * 8) * 2; vd = (u32)((lane >> 3) * D + dchunk * 8) * 2;
  }
  PINS(wsrc0); PINS(wsrc1); PINS(wsrc2); PIN(vf); PIN(vd);
  if (tid < 60) *LDSP(u32x4, L_H + (tid >= 30 ? H_CH - 480 : 0) + tid * 16) = u32x4{0, 0, 0, 0};
  if (tid >= 128 && tid < 130) *LDSP(u16, L_KT + (tid & 1) * KT_CH + KT_Z) = 0;
  const int rt = pq & 1, ct = pq >> 1;
  u32 a_fq = lds0 + ch * T_CH + (16 * ct + col) * T_RS + 16 * g;
  u32 a_fp = lds0 + ch * T_CH + T_P + (16 * rt + col) * T_RS + 16 * g;
  u32 a_kt = lds0 + L_KT + ch * KT_CH + pq * 512 + lane * 8;
  u32 a_sc = lds0 + L_SC + ch * 32;
  u32 a_rw = lds0 + L_R + ch * R_CH + (tid & 255) * 16;
  u32 a_ar;
  {
    const int e0 = 2031 - col + 8 * g, c4 = e0 & 3;
    a_ar = lds0 + L_R + ch * R_CH + c4 * R_CP + 2 * (e0 - c4) - 64 * 63 - 64 * pq + 192;
  }
  u32 a_hw = lds0 + L_H + 480 + row0 * 2;
  u32 a_br = lds0 + L_H + ch * H_CH + 448 - 256 - 64 * pq + 32 * col + 16 * g;
  u32 a_pw = lds0 + w * 8192 + lane * 16, a_pr = lds0 + w * 1024 + lane * 16;
  u32 a_wf;
  {
    const int q4 = col >> 2, pp = lane & 3, phi = (q4 >> 1) | ((g & 1) << 1);
    a_wf = lds0 + L_W + (8 * g + q4) * 128 + 8 * pp + 32 * ((w & 3) ^ phi) + (w >> 2) * 4096;
  }
  u32 sgo = (u32)WS_SG + row0 * 8, mgo = (u32)WS_MG + ((lane & 1) * D + d0) * 8;
  u32 mgo2 = (u32)WS_MG + (col < 4 ? 4 * D + 64 * wtb + 32 * (w >> 2) + 8 * g + 2 * col : 2 * D + d0) * 8;
  u32 oo = (u32)(((2 * w + (col >> 3)) * D + d0 + (g & 1)) * 128 + 16 * (col & 7) + 8 * (g >> 1)) * 2 + (u32)WS_X1T;
  u32 wo0, wo1;
  {
    const int ks = 2 * wtb + (w >> 2), ft = 4 * wta + (w & 3), kk = 2 * wta + (w >> 2), w7 = kk / 7;
    wo0 = (u32)WS_WF + ((ft * 14 + ks) * 2) * 1024;
    wo1 = (u32)WS_WDF + (((wtb * 8 + w7) * 4 + (w & 3)) * 7 + (kk - 7 * w7)) * 1024;
  }
  PIN(a_fq); PIN(a_fp); PIN(a_kt); PIN(a_sc); PIN(a_rw); PIN(a_ar); PIN(a_hw); PIN(a_br); PIN(a_pw); PIN(a_pr); PIN(a_wf); PIN(sgo); PIN(mgo); PIN(mgo2); PIN(oo);
  PINS(wo0); PINS(wo1);
  float dt = rnd(expf(ch ? bfhi(ldt2) : bflo(ldt2)));
  PIN(dt);
  const u32 m0v = lds0 + ch * T_CH + pq * 256;
  SB();
  SST(1);
  if (ln) { vmw<11>(); ln_rows(p, xr, 16 * b, lane, tag); vmw<6>(); }
  else vmw<5>();
#pragma unroll
  for (int i = 0; i < 6; ++i) PIN(pa[i]);
  if (tid == 448) __hip_atomic_store((gu32*)(p.ws + WS_RD) + b, tag, __ATOMIC_RELAXED, __HIP_MEMORY_SCOPE_AGENT);
  SST(2);
  const float zr = dt * bfhi(pa[0]), zi = dt * bfhi(pa[1]);
  asm volatile("s_mov_b32 m0, %0" :: "s"(m0v) : "memory");
  {
    table_q(cexpm(zr, zi, 8.f));
  }
  asm volatile("global_load_dword %0, %1, %2 offset:1792" XG_POL : "=v"(xg[2]) : "v"(xo4), "s"(p.u) : "memory");
  asm volatile("global_load_dword %0, %1, %2 offset:2688" XG_POL : "=v"(xg[3]) : "v"(xo4), "s"(p.u) : "memory");
  SST(3);
  lds_barrier();
  u32x4 fq[16];
  sfor<0, 16>([&](auto I) { lds_r128<64 * I.value>(fq[I.value], a_fq); });
  SB();
  {
    const float br = dt * bfhi(pa[2]), bi = dt * bfhi(pa[3]), cr = bfhi(pa[4]), ci = bfhi(pa[5]);
    f32x2 W = {cr * br - ci * bi, -(cr * bi + ci * br)};
    const float S = wave_sum(fabsf(W.x) + fabsf(W.y));
    const f32x2 E1 = cexpm(zr, -zi, 1.f), E256 = cexpm(zr, -zi, 256.f);
    const float sw = pow2_scale(S, 14);
    W *= sw;
    if (lane == 0) *LDSP(f32x2, L_SC + w * 8) = f32x2{pow2_inv(sw), S};
    table_p(W, E1, E256);
  }
  SST(4);
  lds_barrier();
  float kto, ktx;
  u32x4 mg, st0, st1;
  {
    u32x4 fp[16], sa_, sb_, oa_, ob_;
    const u32 a_so = a_sc ^ 32;
    lds_r128<0>(sa_, a_sc); lds_r128<16>(sb_, a_sc); lds_r128<0>(oa_, a_so); lds_r128<16>(ob_, a_so);
    sfor<0, 16>([&](auto I) { constexpr int s_ = I.value >> 2, c_ = I.value & 3; lds_r128<256 * c_ + 64 * s_>(fp[4 * c_ + s_], a_fp); });
    f32x4 acc[4];
    float kt, k0, k1, k2, k3;
    sfor<0, 16>([&](auto I) {
      constexpr int s_ = I.value >> 2, c_ = I.value & 3;
      lgw<15 - I.value>();
      pin4(fp[4 * c_ + s_]);
      acc[c_] = MFMA16H(fp[4 * c_ + s_], fq[4 * c_ + s_], (s_ == 0 ? f32x4{0, 0, 0, 0} : acc[c_]));
      if constexpr (I.value == 3) {
        pin4(sa_); pin4(sb_);
        const f32x4 sa = __builtin_bit_cast(f32x4, sa_), sb = __builtin_bit_cast(f32x4, sb_);
        kt = pow2_scale(sa[1] + sa[3] + sb[1] + sb[3], 14);
        k0 = sa[0] * kt; k1 = sa[2] * kt; k2 = sb[0] * kt; k3 = sb[2] * kt;
        PIN(k0); PIN(k1); PIN(k2); PIN(k3);
      }
      if constexpr (I.value == 7) {
        pin4(oa_); pin4(ob_);
        const f32x4 oa = __builtin_bit_cast(f32x4, oa_), ob = __builtin_bit_cast(f32x4, ob_);
        ktx = pow2_scale(oa[1] + oa[3] + ob[1] + ob[3], 14);
        PIN(ktx);
      }
      SB();
    });
    kto = kt;
    const f32x4 kv = acc[0] * k0 + acc[1] * k1 + acc[2] * k2 + acc[3] * k3;
    *(__attribute__((address_space(3))) u32x2*)(uintptr_t)a_kt = u32x2{pkh(kv[0], kv[1]), pkh(kv[2], kv[3])};
  }
  SB();
  gload16_sc1(mg, p.ws, mgo, 0); gload16_sc1(st0, p.ws, sgo, 0); gload16_sc1(st1, p.ws, sgo, 16);
  glds16v(wsrc0, vf, lds0 + L_W + w * 1024);
  glds16v(wsrc1, vf, lds0 + L_W + 8192 + w * 1024);
  glds16v(wsrc2, vd, lds0 + L_W + 16384 + w * 1024);
  asm volatile("s_waitcnt vmcnt(10)" : "+v"(gt[0]), "+v"(gt[1]), "+v"(gt[2]) :: "memory");
  SST(5);
  lds_barrier();
  {
    u32 v[11];
#pragma unroll
    for (int j = 0; j < 11; ++j) v[j] = *(const __attribute__((address_space(3))) u16*)(uintptr_t)(gt[j >> 2][j & 3] + lds0);
    u32 e[5], o[5];
#pragma unroll
    for (int j = 0; j < 5; ++j) { e[j] = v[2 * j] | (v[2 * j + 1] << 16); o[j] = v[2 * j + 1] | (v[2 * j + 2] << 16); }
    lds_ptr rb = (lds_ptr)(uintptr_t)a_rw;
    *(__attribute__((address_space(3))) u32x4*)(rb) = u32x4{e[0], e[1], e[2], e[3]};
    *(__attribute__((address_space(3))) u32x4*)(rb + R_CP) = u32x4{o[0], o[1], o[2], o[3]};
    *(__attribute__((address_space(3))) u32x4*)(rb + 2 * R_CP) = u32x4{e[1], e[2], e[3], e[4]};
    *(__attribute__((address_space(3))) u32x4*)(rb + 3 * R_CP) = u32x4{o[1], o[2], o[3], o[4]};
    if ((tid & 255) < 8) *LDSP(u32x4, L_R + ch * R_CH + ((tid & 7) >> 1) * R_CP + 4096 + (tid & 1) * 16) = u32x4{0, 0, 0, 0};
  }
  SST(6);
  lds_barrier();
  u32x4 mg2;
  gload16_sc1(mg2, p.ws, mgo2, 0);
  u32x2 Al[2][8], Ah[2][8];
  u32 a_ar2 = a_ar + 2048;
  PIN(a_ar2);
  auto rd_a = [&](auto U, auto K) {
    constexpr int u = U.value, k = K.value;
    if constexpr (k < 4) { lds_rd64<64 * (28 - 4 * u - 8 * k)>(Al[u][k], a_ar2); lds_rd64<64 * (28 - 4 * u - 8 * k) + 8>(Ah[u][k], a_ar2); }
    else { lds_rd64<64 * (60 - 4 * u - 8 * k)>(Al[u][k], a_ar); lds_rd64<64 * (60 - 4 * u - 8 * k) + 8>(Ah[u][k], a_ar); }
  };
  sfor<0, 7>([&](auto K) { rd_a(IC<0>{}, K); });
  asm volatile("s_waitcnt vmcnt(4)" : "+v"(mg), "+v"(st0), "+v"(st1) :: "memory");
#define NE(x) __builtin_amdgcn_ballot_w64((x) != tag)
  unsigned long long bm = NE(mg[1]) | NE(mg[3]);
  if ((NE(st0[1]) | NE(st0[3]) | NE(st1[1]) | NE(st1[3]) | (bm & 3)) != 0) {
#pragma unroll 1
    for (unsigned spins = 0; spins < (1u << 20); ++spins) {
      __builtin_amdgcn_s_sleep(1);
      asm volatile("global_load_dwordx4 %0, %3, %5 sc1\n\tglobal_load_dwordx4 %1, %3, %5 offset:16 sc1\n\tglobal_load_dwordx4 %2, %4, %5 sc1\n\ts_waitcnt vmcnt(0)"
                   : "+v"(st0), "+v"(st1), "+v"(mg) : "v"(sgo), "v"(mgo), "s"(p.ws) : "memory");
      bm = NE(mg[1]) | NE(mg[3]);
      if ((NE(st0[1]) | NE(st0[3]) | NE(st1[1]) | NE(st1[3]) | (bm & 3)) == 0) break;
    }
  }
#pragma unroll
  for (int r = 0; r < 4; ++r) PIN(xg[r]);
  rd_a(IC<0>{}, IC<7>{});
  SST(7);
  f32x2 xf[2][2], h[2][2];
  float ihs[2], dks[2];
  {
    const u32 sw_[4] = {st0[0], st0[2], st1[0], st1[2]};
    f32x2 m2[2], r2[2];
#pragma unroll
    for (int hh = 0; hh < 2; ++hh) {
      m2[hh] = f32x2{bflo(sw_[2 * hh]), bflo(sw_[2 * hh + 1])};
      r2[hh] = f32x2{__builtin_amdgcn_rcpf(bfhi(sw_[2 * hh])), __builtin_amdgcn_rcpf(bfhi(sw_[2 * hh + 1]))};
      xf[0][hh] = f32x2{bflo(xg[2 * hh]), bflo(xg[2 * hh + 1])};
      xf[1][hh] = f32x2{bfhi(xg[2 * hh]), bfhi(xg[2 * hh + 1])};
    }
#pragma unroll
    for (int c = 0; c < 2; ++c) {
      if (c == 1) { SB(); sfor<0, KA1>([&](auto K) { rd_a(IC<1>{}, K); }); SB(); }
      const float sa = rlane(mg[2 * c], 0), ca = rnd(rlane(mg[2 * c], 1) + 1.0f);
      const float hs = pow2_scale(22.0f * fabsf(ca) + fabsf(sa), 14);
      ihs[c] = pow2_inv(hs);
      dks[c] = (c ? bfhi(dsk2) : bflo(dsk2)) * ihs[c];
      const float cas = ca * hs, sas = sa * hs;
#pragma unroll
      for (int hh = 0; hh < 2; ++hh) {
        const f32x2 xn = rnd2(rnd2(xf[c][hh] - m2[hh]) * r2[hh]);
        h[c][hh] = rnd2(rnd2(xn * cas) + sas);
      }
      *(__attribute__((address_space(3))) u32x2*)(uintptr_t)(a_hw + c * H_CH) = u32x2{pkh2(h[c][0]), pkh2(h[c][1])};
    }
  }
  vmw<1>();
  SST(8);
  lds_barrier();
  f32x2 hd[2][2];
  float ysc[2];
  {
    u32x4 B[2][8], A[2][8];
    u32x2 wt_[3][2];
    sfor<0, 8>([&](auto K) { lds_r128<512 * K.value + 256>(B[0][K.value], a_br); });
    constexpr int NA = 2 * (8 - KA1), NL = NA + 14;
    auto later = [&](auto I) {
      constexpr int i = I.value;
      if constexpr (i < NA) { if constexpr ((i & 1) == 0) rd_a(IC<1>{}, IC<KA1 + i / 2>{}); }
      else if constexpr (i < NA + 4) lds_r128<512 * (i - NA)>(B[1][i - NA], a_br);
      else if constexpr (i < NA + 10) lds_rtr<((i - NA - 4) >> 1) * 8192 + ((i - NA - 4) & 1) * 512>(wt_[(i - NA - 4) >> 1][(i - NA - 4) & 1], a_wf);
      else lds_r128<512 * (i - NA - 6)>(B[1][i - NA - 6], a_br);
    };
    constexpr auto lat0 = [](int j) constexpr -> int {
      if (KA1 == 8) return j <= 1 ? 0 : j == 2 ? 4 : j == 3 ? 10 : 14;
      if (KA1 == 4) return j <= 0 ? 0 : j <= 2 ? 8 : j == 3 ? 12 : j == 4 ? 18 : 22;
      return j <= 0 ? 0 : j == 1 ? 8 : j == 2 ? 12 : j == 3 ? 16 : j == 4 ? 20 : j == 5 ? 26 : 30;
    };
#pragma unroll
    for (int k = 0; k < 8; ++k) { pin2(Al[0][k]); pin2(Ah[0][k]); A[0][k] = u32x4{Al[0][k][0], Al[0][k][1], Ah[0][k][0], Ah[0][k][1]}; }
    f32x4 acc[8];
    u32 c1w[4];
    sfor<0, 8>([&](auto J) {
      constexpr int jj = J.value;
      lgw<7 - jj + lat0(jj)>();
      pin4(B[0][jj]);
#pragma unroll
      for (int k = 0; k + jj < 8; ++k) acc[k + jj] = MFMA16H(A[0][k], B[0][jj], (jj == 0 ? f32x4{0, 0, 0, 0} : acc[k + jj]));
      if constexpr (jj == 1) {
#pragma unroll
        for (int c = 0; c < 2; ++c)
#pragma unroll
          for (int hh = 0; hh < 2; ++hh) { hd[c][hh] = rnd2(h[c][hh] * dks[c]); PIN(hd[c][hh]); }
      }
      if constexpr (jj == 2) {
        ysc[0] = pow2_inv(ch ? ktx : kto) * ihs[0]; ysc[1] = pow2_inv(ch ? kto : ktx) * ihs[1];
        PIN(ysc[0]); PIN(ysc[1]);
      }
      SB();
      sfor<lat0(jj), lat0(jj + 1)>(later);
      if constexpr (jj == 5) {
        SB();
        asm volatile("s_waitcnt vmcnt(0)" : "+v"(mg2) :: "memory");
        if ((NE(mg2[1]) | NE(mg2[3])) != 0) {
#pragma unroll 1
          for (unsigned spins = 0; spins < (1u << 20); ++spins) {
            __builtin_amdgcn_s_sleep(1);
            asm volatile("global_load_dwordx4 %0, %1, %2 sc1\n\ts_waitcnt vmcnt(0)" : "+v"(mg2) : "v"(mgo2), "s"(p.ws) : "memory");
            if ((NE(mg2[1]) | NE(mg2[3])) == 0) break;
          }
        }
        const u32 c1p = pkbf(__uint_as_float(mg2[0]) + 1.0f, __uint_as_float(mg2[2]) + 1.0f);
        c1w[0] = __builtin_amdgcn_mov_dpp(c1p, 0x150, 0xf, 0xf, true); c1w[1] = __builtin_amdgcn_mov_dpp(c1p, 0x151, 0xf, 0xf, true);
        c1w[2] = __builtin_amdgcn_mov_dpp(c1p, 0x152, 0xf, 0xf, true); c1w[3] = __builtin_amdgcn_mov_dpp(c1p, 0x153, 0xf, 0xf, true);
      }
    });
    SB();
    const __amdgpu_buffer_rsrc_t wr = __builtin_amdgcn_make_buffer_rsrc(p.ws, 0, (int)WS_X1T, 0x00020000);
    u32x4 wfr[3];
    sfor<0, 8>([&](auto J) {
      constexpr int jj = J.value;
      if constexpr (jj == 0) {
        lgw<10>();
#pragma unroll
        for (int k = 0; k < 8; ++k) { pin2(Al[1][k]); pin2(Ah[1][k]); A[1][k] = u32x4{Al[1][k][0], Al[1][k][1], Ah[1][k][0], Ah[1][k][1]}; }
#pragma unroll
        for (int k = 0; k < 4; ++k) pin4(B[1][k]);
      }
      if constexpr (jj == 1) {
        lgw<4>();
#pragma unroll
        for (int i = 0; i < 3; ++i) { pin2(wt_[i][0]); pin2(wt_[i][1]); wfr[i] = u32x4{wt_[i][0][0], wt_[i][0][1], wt_[i][1][0], wt_[i][1][1]}; }
        if (b < 196) __builtin_amdgcn_raw_buffer_store_b128(wfr[2], wr, lane * 16, wo1, WT_AUX);
      }
      if constexpr (jj == 2) {
        lgw<1>();
#pragma unroll
        for (int k = 4; k < 8; ++k) pin4(B[1][k]);
      }
#pragma unroll
      for (int k = 0; k + jj < 8; ++k) acc[k + jj] = MFMA16H(A[1][k], B[1][jj], acc[k + jj]);
      if (jj > 0) *(__attribute__((address_space(3))) f32x4*)(uintptr_t)(a_pw + (jj - 1) * 1024) = acc[jj - 1];
      if constexpr (jj >= W_ST - 3 && jj <= W_ST) {
        constexpr int t = jj - (W_ST - 3);
        const f32x2 cf = {bflo(c1w[t]), bfhi(c1w[t])};
#pragma unroll
        for (int i = 0; i < 2; ++i) {
          const f32x2 v = f32x2{bflo(wfr[i][t]), bfhi(wfr[i][t])} * cf;
          u32 o = pkbf(v.x, v.y);
          PIN(o);
          wfr[i][t] = o;
        }
      }
      if (jj == W_ST && b < 196) {
        __builtin_amdgcn_raw_buffer_store_b128(wfr[0], wr, lane * 16, wo0, WT_AUX);
        __builtin_amdgcn_raw_buffer_store_b128(wfr[1], wr, lane * 16 + 1024, wo0, WT_AUX);
      }
      SB();
    });
    *(__attribute__((address_space(3))) f32x4*)(uintptr_t)(a_pw + 7 * 1024) = acc[7];
  }
  SST(9);
  lds_barrier();
  SST(10);
  {
    u32x4 pr[8];
    sfor<0, 8>([&](auto I) { lds_r128<(I.value >> 2) * 32768 + (I.value & 3) * 8192>(pr[I.value], a_pr); });
    f32x4 y[2];
#pragma unroll
    for (int c = 0; c < 2; ++c) {
      if (c == 0) lgw<4>(); else lgw<0>();
#pragma unroll
      for (int i = 0; i < 4; ++i) pin4(pr[4 * c + i]);
      y[c] = (__builtin_bit_cast(f32x4, pr[4 * c]) + __builtin_bit_cast(f32x4, pr[4 * c + 1])) +
             (__builtin_bit_cast(f32x4, pr[4 * c + 2]) + __builtin_bit_cast(f32x4, pr[4 * c + 3]));
    }
#undef NE
    u32 o1[2][2];
#pragma unroll
    for (int c = 0; c < 2; ++c) {
      const float ga = rlane(mg2[2 * c], 4);
#pragma unroll
      for (int hh = 0; hh < 2; ++hh) {
        const f32x2 yy = rnd2(f32x2{y[c][2 * hh], y[c][2 * hh + 1]} * ysc[c]);
        const f32x2 t = rnd2(yy + hd[c][hh]);
        const f32x2 xs = xf[c][hh] + rnd2(t * ga);
        o1[c][hh] = pkbf(xs.x, xs.y);
      }
    }
    swap16(o1[0][0], o1[1][0]); swap16(o1[0][1], o1[1][1]);
    const __amdgpu_buffer_rsrc_t xr_ = __builtin_amdgcn_make_buffer_rsrc(p.ws, 0, (int)WS_HH, 0x00020000);
    __builtin_amdgcn_raw_buffer_store_b128(u32x4{o1[0][0], o1[0][1], o1[1][0], o1[1][1]}, xr_, oo, 0, X1_AUX);
  }
  SSTE(11); SEND(0, 14, b);
}

#define G2_POS 1
constexpr int H_S = 0, H_CS = 1024, H_PART = 3072, H_G2 = 4096, H_WG = H_G2 + 57344, H_G1 = H_WG + 57344, H_END = H_G1 + 28672;
constexpr int AB_LDS = AB_MAIN_LDS > H_END ? AB_MAIN_LDS : H_END;

template <int SA, int LA>
__device__ __forceinline__ void hdma16(__amdgpu_buffer_rsrc_t r, u32 voff, u32 sbase, u32 lbase) {
  u32 t;
  asm volatile("s_add_i32 m0, %4, %6\n\ts_add_i32 %0, %3, %5\n\tbuffer_load_dwordx4 %1, %2, %0 offen lds"
               : "=&s"(t) : "v"(voff), "s"(r), "s"(sbase), "s"(lbase), "i"(SA), "i"(LA) : "memory", "scc");
}
__device__ __forceinline__ void helper(const Params& p, int j, char* smem) {
  const int tid = threadIdx.x, lane = tid & 63;
  const int w = __builtin_amdgcn_readfirstlane(tid >> 6);
  const int col = lane & 15, g = lane >> 4, q4 = col >> 2, pp = lane & 3;
  const unsigned lds0 = LDS0;
  SDECL(12); SST(0);
  u32 ep;
  asm volatile("s_load_dword %0, %1, 0x0" : "=s"(ep) : "s"(p.ws + WS_EP) : "memory");
  if (j >= 28) {
    asm volatile("s_waitcnt lgkmcnt(0)" : "+s"(ep) :: "memory");
    const u32 tag = ep + TAGC;
    if (tid == 448) __hip_atomic_store((gu32*)(p.ws + WS_RD) + AB_MAIN + j, tag, __ATOMIC_RELAXED, __HIP_MEMORY_SCOPE_AGENT);
    if (j == 28) ((u32x2*)(p.ws + WS_FLG))[tid] = u32x2{0, 0};
    if (j == 29) *(u32x4*)(p.ws + WS_STG + tid * 16) = u32x4{0, 0, 0, 0};
    if (j == 30) {
      if (tid < 256) {
#pragma unroll 1
        for (unsigned spins = 0; spins < (1u << 20); ++spins) {
          if (__hip_atomic_load((gu32*)(p.ws + WS_RD) + tid, __ATOMIC_RELAXED, __HIP_MEMORY_SCOPE_AGENT) == tag) break;
          __builtin_amdgcn_s_sleep(4);
        }
      }
      __syncthreads();
      if (tid == 0) *(u32*)(p.ws + WS_EP) = tag;
    }
    return;
  }
  const int lbase = (8 * g + q4) * 128 + 8 * pp, phi = (q4 >> 1) | ((g & 1) << 1);
  const int dchunk = (lane & 7) ^ ((((lane >> 4) & 1) | ((w & 1) << 1)) << 1);
  const int ks0 = w < 6 ? 2 * w : w + 6, nks = w < 6 ? 2 : 1;
  u32 cw;
  asm volatile("global_load_short_d16_hi %0, %1, %2" : "=v"(cw) : "v"((u32)min(32 * ks0 + lane, D - 1) * 2), "s"(p.c) : "memory");
  const __amdgpu_buffer_rsrc_t wm = __builtin_amdgcn_make_buffer_rsrc((void*)p.w_mod, 0, D * 6 * D * 2, 0x00020000);
  {
    const u32 go = (u32)((lane >> 2) * (6 * D) + ((lane & 3) ^ (((lane >> 5) & 1) << 1)) * 8) * 2;
    const u32 so = (u32)(32 * ks0 * 6 * D + 32 * j) * 2, lo = lds0 + H_G1 + 32 * ks0 * 64;
    hdma16<0, 0>(wm, go, so, lo); hdma16<16 * 6 * D * 2, 1024>(wm, go, so, lo);
    if (w < 6) { hdma16<32 * 6 * D * 2, 2048>(wm, go, so, lo); hdma16<48 * 6 * D * 2, 3072>(wm, go, so, lo); }
  }
  SST(6);
  {
    vm0();
    PIN(cw);
  {
    const u32 go = (u32)((lane >> 3) * (6 * D) + dchunk * 8) * 2;
    const u32 so = (u32)(8 * w * 6 * D + 2 * D + 64 * j) * 2, lo = lds0 + H_G2 + w * 1024;
    sfor<0, 7>([&](auto I) { hdma16<I.value * 64 * 6 * D * 2, I.value * 8192>(wm, go, so, lo); });
  }
    SST(8);
    const float c0 = bfhi(cw);
    if (lane < 32 * nks) *LDSP(u16, H_S + (32 * ks0 + lane) * 2) = (u16)pkbf(c0 / (1.0f + expf(-c0)), 0.f);
    f32x4 a0 = {0, 0, 0, 0}, a1 = {0, 0, 0, 0};
    const int lb64 = (8 * g + q4) * 64 + 8 * pp;
    for (int s = 0; s < nks; ++s) {
      const int ks = ks0 + s;
      const u32x4 sf = *LDSP(u32x4, H_S + (32 * ks + 8 * g) * 2);
      lds_cptr a = (lds_cptr)smem + H_G1 + lb64 + ks * 2048;
      const u32x2 t0 = tr_read(a + 32 * (g & 1)), t1 = tr_read(a + 32 * (g & 1) + 256), t2 = tr_read(a + 32 * ((g & 1) ^ 1)), t3 = tr_read(a + 32 * ((g & 1) ^ 1) + 256);
      a0 = MFMA16(sf, (u32x4{t0[0], t0[1], t1[0], t1[1]}), a0);
      a1 = MFMA16(sf, (u32x4{t2[0], t2[1], t3[0], t3[1]}), a1);
    }
    SST(9);
    if (lane < 16) { *LDSP(float, H_PART + (w * 32 + lane) * 4) = a0[0]; *LDSP(float, H_PART + (w * 32 + 16 + lane) * 4) = a1[0]; }
  }
  lds_barrier();
  SST(7);
  PINS(ep);
  const u32 tag = ep + TAGC;
  if (w == 0 && lane < 32) {
    float t = 0.f;
#pragma unroll
    for (int u = 0; u < 8; ++u) t += *LDSP(float, H_PART + (u * 32 + lane) * 4);
    st_gran(p.ws + WS_MG, 32 * j + lane, __float_as_uint(rnd(t)), tag);
  }
  if (tid == 448) __hip_atomic_store((gu32*)(p.ws + WS_RD) + AB_MAIN + j, tag, __ATOMIC_RELAXED, __HIP_MEMORY_SCOPE_AGENT);
  SST(1);
  const u32 vofff = (u32)((lane >> 3) * F + dchunk * 8) * 2;
#pragma unroll
  for (int i = 0; i < 7; ++i) glds16v(p.w_gate + (size_t)(8 * (w + 8 * i)) * F + 64 * j, vofff, lds0 + H_WG + (w + 8 * i) * 1024);
  vmw<7>();
  lds_barrier();
  SST(2);
  if (w < 4) {
    f32x4 a = {0, 0, 0, 0};
#pragma unroll
    for (int ks = 0; ks < 14; ++ks) a = MFMA16(*LDSP(u32x4, H_S + (32 * ks + 8 * g) * 2), frag128(smem, H_G2, lbase, phi, ks, w), a);
    if (lane < 16) {
      const float t = rnd(a[0]);
      const int mi = 2 * D + 64 * j + 16 * w + lane;
      st_gran(p.ws + WS_MG, mi, __float_as_uint(t), tag);
      ((float*)(p.ws + WS_MOD))[mi] = t;
    }
  }
  lds_barrier();
  SST(3);
#pragma unroll
  for (int i = 0; i < 7; ++i) glds16v(p.w_up + (size_t)(8 * (w + 8 * i)) * F + 64 * j, vofff, lds0 + H_G2 + (w + 8 * i) * 1024);
  if (tid < D) {
    unsigned long long a, c;
#pragma unroll 1
    for (unsigned spins = 0; spins < (1u << 20); ++spins) {
      a = __hip_atomic_load((gu64*)(p.ws + WS_MG) + 3 * D + tid, __ATOMIC_RELAXED, __HIP_MEMORY_SCOPE_AGENT);
      c = __hip_atomic_load((gu64*)(p.ws + WS_MG) + 4 * D + tid, __ATOMIC_RELAXED, __HIP_MEMORY_SCOPE_AGENT);
      if ((u32)(a >> 32) == tag && (u32)(c >> 32) == tag) break;
      __builtin_amdgcn_s_sleep(1);
    }
    *LDSP(u16, H_CS + tid * 2) = (u16)(pkbf(1.0f + __uint_as_float((u32)c), 0.f));
    *LDSP(u16, H_CS + 896 + tid * 2) = (u16)((u32)a >> 16);
  }
  vm0();
  lds_barrier();
  SST(4);
  {
    f32x4 a = {0, 0, 0, 0};
#pragma unroll
    for (int ks = 0; ks < 14; ++ks)
      a = MFMA16(*LDSP(u32x4, H_CS + (col & 1) * 896 + (32 * ks + 8 * g) * 2), frag128(smem, w < 4 ? H_WG : H_G2, lbase, phi, ks, w & 3), a);
    if (lane < 16) {
      float* uv = (float*)(p.ws + WS_UV) + (size_t)(w >> 2) * 2 * F + 64 * j + 16 * (w & 3) + lane;
      uv[0] = a[0]; uv[F] = a[1];
    }
  }
  if (w == 7 && lane < 32) {
    const unsigned long long ka = (unsigned long long)(uintptr_t)__builtin_amdgcn_kernarg_segment_ptr() & ~4095ull;
    asm volatile("s_mov_b32 m0, %2\n\ts_nop 0\n\tglobal_load_lds_dword %0, %1" :: "v"((u32)lane * 128), "s"(ka), "i"(H_S) : "memory");
  }
  SSTE(5); SEND(1, 12, 224 + j);
}

__global__ __launch_bounds__(512) void kAB(Params p) {
  extern __shared__ __attribute__((aligned(16))) char smem[];
  {
    u32 vo, t; unsigned long long pc;
    asm volatile("v_readfirstlane_b32 %[t], %[tid]\n\t"
                 "s_cmp_lt_u32 %[bid], 32\n\t"
                 "s_cselect_b32 %[t], %[t], 0\n\t"
                 "s_cmp_lt_u32 %[t], 448\n\t"
                 "s_cbranch_scc1 .Lcpf_skip%=\n\t"
                 "s_mov_b32 m0, %[lo]\n\t"
                 "s_getpc_b64 %[pc]\n"
                 ".Lcpf_here%=:\n\t"
                 "s_lshr_b32 %[t], %[bid], 3\n\t"
                 "s_lshl_b32 %[t], %[t], 13\n\t"
                 "s_sub_u32 %[t], %[t], 57344\n\t"
                 "v_lshlrev_b32 %[vo], 7, %[tid]\n\t"
                 "v_add_u32 %[vo], %[t], %[vo]\n\t"
                 "v_min_u32 %[vo], cd_code_end-.Lcpf_here%=-4, %[vo]\n\t"
                 "global_load_lds_dword %[vo], %[pc]\n"
                 ".Lcpf_skip%=:"
                 : [vo] "=&v"(vo), [t] "=&s"(t), [pc] "=&s"(pc) : [tid] "v"(threadIdx.x), [bid] "s"(blockIdx.x), [lo] "i"(H_WG) : "scc", "memory");
  }
  asm volatile("" :: "s"(p.u), "s"(p.A_re), "s"(p.A_im), "s"(p.B_re), "s"(p.B_im), "s"(p.C_re), "s"(p.C_im), "s"(p.log_dt), "s"(p.Dskip),
               "s"(p.w_gate), "s"(p.w_up), "s"(p.w_down), "s"(p.c), "s"(p.w_mod), "s"(p.ws));
  if (blockIdx.x < 256 - AB_MAIN) helper(p, blockIdx.x, smem);
  else conv_main(p, blockIdx.x - (256 - AB_MAIN), smem);
}

constexpr int C_UV = 0, C_RES = 2048, C_XH1 = 10240, C_XH0 = C_XH1 + 65536, C_CTR = C_XH0 + 65536, CD_LDS = C_CTR + 16;

__device__ __forceinline__ void stats_role(const Params& p, int j, char* smem) {
  const int tid = threadIdx.x, lane = tid & 63, w = tid >> 6;
  const int lc = tid & 7, dr = tid >> 3;
  const char* src = p.ws + WS_X1T + (size_t)(j >> 1) * D * 256 + (j & 1) * 128 + lc * 16;
  u32x4 v[7];
#define PFS_W 0
#define PFS_D 0
#pragma unroll
  for (int i = 0; i < 7; ++i) v[i] = *(const u32x4*)(src + (dr + 64 * i) * 256);
  float s[8] = {}, q[8] = {};
#pragma unroll
  for (int i = 0; i < 7; ++i)
#pragma unroll
    for (int k = 0; k < 4; ++k) {
      const float a = bflo(v[i][k]), b = bfhi(v[i][k]);
      s[2 * k] += a; q[2 * k] = fmaf(a, a, q[2 * k]); s[2 * k + 1] += b; q[2 * k + 1] = fmaf(b, b, q[2 * k + 1]);
    }
#pragma unroll
  for (int k = 0; k < 8; ++k) {
    s[k] += dppf<0x128>(s[k]); q[k] += dppf<0x128>(q[k]);
    s[k] = xrow16_sum(s[k]); q[k] = xrow16_sum(q[k]);
  }
  float* red = (float*)smem;
  if (lane < 8) {
    *(f32x4*)(red + (w * 2 + 0) * 64 + lane * 8) = f32x4{s[0], s[1], s[2], s[3]};
    *(f32x4*)(red + (w * 2 + 0) * 64 + lane * 8 + 4) = f32x4{s[4], s[5], s[6], s[7]};
    *(f32x4*)(red + (w * 2 + 1) * 64 + lane * 8) = f32x4{q[0], q[1], q[2], q[3]};
    *(f32x4*)(red + (w * 2 + 1) * 64 + lane * 8 + 4) = f32x4{q[4], q[5], q[6], q[7]};
  }
  __syncthreads();
  if (tid < 64) {
    float ss = 0.f, qq = 0.f;
#pragma unroll
    for (int u = 0; u < 8; ++u) { ss += red[(u * 2) * 64 + tid]; qq += red[(u * 2 + 1) * 64 + tid]; }
    const float mean = ss * (1.0f / D);
    const float var = fmaxf(qq * (1.0f / D) - mean * mean, 0.f);
    const float sd = sqrtf(bfr(bfr(var) + 1e-5f));
    const u32 word = pkbf(mean, sd);
    __hip_atomic_store((gu32*)(p.ws + WS_STG) + 64 * j + tid, word, __ATOMIC_RELAXED, __HIP_MEMORY_SCOPE_AGENT);
  }
  asm volatile("cd_code_end:");
}

#define C_LA 3
struct COp { int kind, arg; };
#define ROT 0
#define Y_LATE 1
#define C_KS 13
#define PRIO 2
#define NB1 7
#define PUB 1
#define WD_PRE 0
#define PF_W 0
#define PF_D 0
#define WD_LATE 0
constexpr int WD_REM = 28 - WD_PRE, WD_X = WD_REM > 14 ? WD_REM - 14 : 0;
constexpr COp c_opA(int ps, int ks, int g) {
  if (ps == 0) {
    if (g < 2 && ks + C_LA <= 13) return {1, 2 * (ks + C_LA) + g};
    if (ks == 0 && g >= 2) return {2, g};
    if (ks >= 1 && ks <= 3 && g == 2) return {2, ks + 3};
    if (ks == 1 && g == 3) return {5, 0};
    if (!Y_LATE && ks >= 7 && ks <= 10 && g == 2) return {6, ks - 7};
    if (Y_LATE && ks >= 12 && (g & 1) == 0) return {6, 2 * (ks - 12) + (g >> 1)};
    if (PF_D && ks == PF_D && g == 3) return {8, 0};
  } else {
    if (WD_LATE) {
      if (g == 0 && WD_PRE + ks + (ks > 14 - WD_X ? ks - (14 - WD_X) : 0) < 28) return {4, WD_PRE + ks + (ks > 14 - WD_X ? ks - (14 - WD_X) : 0)};
      if (g == 1 && ks >= 14 - WD_X) return {4, WD_PRE + ks + (ks - (14 - WD_X)) + 1};
    } else {
      if (g == 0 && WD_PRE + ks + (ks < WD_X ? ks : WD_X) < 28) return {4, WD_PRE + ks + (ks < WD_X ? ks : WD_X)};
      if (g == 1 && ks < WD_X) return {4, WD_PRE + 2 * ks + 1};
    }
    if (!Y_EPI && ks <= 2 && g == 2) return {6, ks + 4};
    if (ks == C_KS && g == 2) return {7, 0};
  }
  return {0, 0};
}
constexpr COp c_opB(int ps, int ks, int g) { return ks == (ps ? 10 : 11) ? COp{3, 4 * ps + g} : COp{0, 0}; }
struct CSched { int wait_x0, wait_w[14], wait_x3, wait_x7, wait_x13, wait_st0, wait_y3, wait_y7, wait_st1; };
constexpr CSched c_make_sched() {
  CSched r{};
  int seqW[28] = {}, seqX[7] = {}, seqY[7] = {}, seqS[2] = {}, n = 0;
  seqX[0] = seqX[1] = n++;
  for (int k = 0; k < 2 * C_LA; ++k) seqW[k] = n++;
  ++n;
  if (PF_W) ++n;
  r.wait_x0 = n - 1 - seqX[1];
  for (int ps = 0; ps < 2; ++ps) {
    for (int ks = 0; ks < 14; ++ks) {
      if (ps == 0) {
        r.wait_w[ks] = n - 1 - seqW[2 * ks + 1];
        if (ks == 3) r.wait_x3 = n - 1 - seqX[3];
        if (ks == 7) r.wait_x7 = n - 1 - seqX[6];
        if (ks == 13) r.wait_x13 = n - 1 - seqY[3];
      } else if (ks == 7) r.wait_y7 = n - 1 - seqY[6];
      for (int g = 0; g < 4; ++g)
        for (int u = 0; u < 2; ++u) {
          const COp o = u ? c_opB(ps, ks, g) : c_opA(ps, ks, g);
          if (o.kind == 1) seqW[o.arg] = n;
          if (o.kind == 2) seqX[o.arg] = n;
          if (o.kind == 6) seqY[o.arg] = n;
          if (o.kind == 3) seqS[o.arg >> 2] = n;
          if (o.kind) ++n;
        }
    }
    if (ps == 0) {
      r.wait_st0 = n - 1 - seqS[0];
      for (int i = 0; i < 4; ++i) { n += WD_PRE / 4; if (Y_EPI && i < 3) seqY[4 + i] = n++; ++n; }
      r.wait_y3 = n - 1 - seqY[3];
    }
  }
  r.wait_st1 = n - 1 - seqS[1];
  return r;
}
constexpr CSched C_SCHED = c_make_sched();
constexpr size_t WS_END = WS_HH + (size_t)L * F * 2;

__device__ __forceinline__ f32x2 half2of(const f32x4& v, int r) { return r ? f32x2{v[2], v[3]} : f32x2{v[0], v[1]}; }
__device__ __forceinline__ f32x2 pkfma(f32x2 a, f32x2 b, f32x2 c) { return __builtin_elementwise_fma(a, b, c); }
__device__ __forceinline__ f32x2 pkadd1(f32x2 a) { return a + f32x2{1.0f, 1.0f}; }
typedef __amdgpu_buffer_rsrc_t rsrc_t;
__device__ __forceinline__ void bload16(u32x4& dst, rsrc_t r, u32 voff, u32 soff, int imm) {
  asm volatile("buffer_load_dwordx4 %0, %1, %2, %3 offen offset:%4" : "=v"(dst) : "v"(voff), "s"(r), "s"(soff), "i"(imm) : "memory");
}
__device__ __forceinline__ void bload16_sc1(u32x4& dst, rsrc_t r, u32 voff, u32 soff, int imm) {
  asm volatile("buffer_load_dwordx4 %0, %1, %2, %3 offen offset:%4 sc1" : "=v"(dst) : "v"(voff), "s"(r), "s"(soff), "i"(imm) : "memory");
}

__device__ __forceinline__ void bload4(u32& dst, rsrc_t r, u32 voff, u32 soff) {
  asm volatile("buffer_load_dword %0, %1, %2, %3 offen" : "=v"(dst) : "v"(voff), "s"(r), "s"(soff) : "memory");
}
template <int OFF> __device__ __forceinline__ void lds_rd128(u32x4& dst, u32 adr) {
  asm volatile("ds_read_b128 %0, %1 offset:%2" : "=v"(dst) : "v"(adr), "i"(OFF) : "memory");
}
__device__ __forceinline__ void gstore8(const void* sbase, u32 voff, u32x2 v) {
#define OUT_POL ""
  asm volatile("global_store_dwordx2 %0, %1, %2 " OUT_POL :: "v"(voff), "v"(v), "s"(sbase) : "memory");
}
__device__ __forceinline__ void lds_wait8(u32x4 (&pr)[8]) {
  asm volatile("s_waitcnt lgkmcnt(0)" : "+v"(pr[0]), "+v"(pr[1]), "+v"(pr[2]), "+v"(pr[3]), "+v"(pr[4]), "+v"(pr[5]), "+v"(pr[6]), "+v"(pr[7]) :: "memory");
}
__device__ __forceinline__ void sbarrier() { asm volatile("s_barrier" ::: "memory"); }
__device__ __forceinline__ void bstore8_sc1(u32x2 v, rsrc_t r, u32 voff, u32 soff) {
  asm volatile("buffer_store_dwordx2 %0, %1, %2, %3 offen sc1" :: "v"(v), "v"(voff), "s"(r), "s"(soff) : "memory");
}
__device__ __forceinline__ void gload4_sc1(u32& dst, const void* sbase, u32 voff) {
  asm volatile("global_load_dword %0, %1, %2 sc1" : "=v"(dst) : "v"(voff), "s"(sbase) : "memory");
}
__global__ __launch_bounds__(512) void kCD(Params p) {
  extern __shared__ __attribute__((aligned(16))) char smem[];
  if (blockIdx.x < 32) { stats_role(p, blockIdx.x, smem); return; }
  const int bid = blockIdx.x - 32;
  const int tid = threadIdx.x, lane = tid & 63;
  const int w = __builtin_amdgcn_readfirstlane(tid >> 6);
  int wh = w >> 1;
  PINS(wh);
  const int xcd = bid & 7, idx = bid >> 3, i7 = (idx * 37) >> 8, Q = idx - 7 * i7, sub = xcd & 1;
  const int Lt = 4 * (xcd >> 1) + i7, Fi = 7 * sub + Q;
  const unsigned lds0 = LDS0;
  const int g = lane >> 4, q4 = (lane & 15) >> 2, pp = lane & 3;
  const u32 l16 = lane * 16;
  const rsrc_t ws = __builtin_amdgcn_make_buffer_rsrc(p.ws, 0, (int)WS_END, 0x00020000);

  SDECL(14); SST(0);
  u32x4 wf[28], wd[28];
  const u32 dch = (lane & 7) ^ ((((lane >> 4) & 1) | ((w & 1) << 1)) << 1);
  const u32 xoff = (u32)(lane >> 3) * 256 + dch * 16;
  int off = ROT ? 2 * Q + ((7 * i7) >> 1) : 0;
  off -= off >= 14 ? 14 : 0;
  const int offw = off + (w >> 2);
  const u32 sx0 = (u32)WS_X1T + Lt * (D * 256) + (w & 3) * 2048, sl = lds0 + w * 1024;
  const u32 sw = (u32)WS_WF + (Fi * 8 + w) * (28 * 1024) + off * 2048;
  auto ld_w = [&](int n) { bload16(wf[n], ws, l16, sw - ((n >> 1) + off >= 14 ? 28 * 1024 : 0) + (n >> 2) * 4096, (n & 3) * 1024); };
  auto xsrc = [&](int o, int kp) { return sx0 + o * 8192 - (kp + o >= 14 ? D * 256 : 0); };
  if (w < 4) {
    sfor<0, 4>([&](auto I) { hdma16<I.value * 8192, C_XH0 + I.value * 4096>(ws, xoff, xsrc(off, I.value), sl); });
  }
#pragma unroll
  for (int n = 0; n < 2 * C_LA; ++n) ld_w(n);
  SB();
  if (tid == 0) *LDSP(u32, C_CTR) = 0;
  if (lane < 16) glds16s(p.ws + WS_UV + ((size_t)wh * F + 128 * Fi + 64 * (w & 1)) * 4, l16, lds0 + C_UV + w * 256);
  u32 pfw = 0, pfd = 0;
  if constexpr (PF_W) bload4(pfw, ws, (u32)min(lane, 43) * 128, sw + 6144 + i7 * 5632);
  const u32 swd = (u32)WS_WDF + (Q * 8 + w) * (28 * 1024);
  const char* stsrc = p.ws + WS_STG + Lt * 512;
  gu32* cnt = (gu32*)(p.ws + WS_FLG) + Lt * 64;
  u32 stw[8], cn0;
  u32 jo[7], io[4];
#pragma unroll
  for (int j = 0; j < 7; ++j) { const int t = j + (ROT ? Q : 0); jo[j] = (u32)(t - (t >= 7 ? 7 : 0)) * 1024; PINS(jo[j]); }
#pragma unroll
  for (int i = 0; i < 4; ++i) { io[i] = swd + (u32)((i + (ROT ? i7 : 0)) & 3) * 7168; PINS(io[i]); }
  auto ld_wd = [&](int m) { bload16(wd[4 * (m % 7) + m / 7], ws, l16, io[m / 7] + jo[m % 7], 0); };
  auto ld_st = [&](int i) {
    gload4_sc1(stw[i], stsrc + (((i & 4) | ((i ^ w) & 3)) << 6), (u32)(lane & 15) * 4);
  };
  SST(1);
  f32x4 ag[4], au[4];
  unsigned ta[4];
  {
    const unsigned base = lds0 + C_XH0 + (unsigned)((8 * g + q4) * 128 + 8 * pp), phi = (q4 >> 1) | ((g & 1) << 1);
#pragma unroll
    for (int i = 0; i < 4; ++i) ta[i] = base + 32u * ((unsigned)(i ^ (w & 3)) ^ phi);
  }
  u32x4 xb[2][4];
  auto rd_tile = [&](int ks, int i, u32x4& dst) {
    lds_cptr a = (lds_cptr)(uintptr_t)ta[i] + ks * 4096;
    const u32x2 t0 = tr_read(a), t1 = tr_read(a + 512);
    dst = u32x4{t0[0], t0[1], t1[0], t1[1]};
  };
  const u32 hoff = (u32)((16 * (2 * (w & 1) + (g >> 1)) + (lane & 15)) * 16 + 8 * (g & 1));
  auto epilogue = [&](auto PS, auto&& pre, auto&& hook) {
    constexpr int ps = PS.value;
#pragma unroll 1
    for (unsigned spins = 0; spins < (1u << 16); ++spins) {
      u32 mn = 0xffffffffu;
#pragma unroll
      for (int i = 0; i < 4; ++i) { pin(stw[4 * ps + i]); mn = min(mn, stw[4 * ps + i]); }
      if (__all(mn >= 0x10000u)) break;
#pragma unroll
      for (int i = 0; i < 4; ++i) ld_st(4 * ps + i);
      vmwait(0);
    }
    const float* uvl = (const float*)(smem + C_UV) + 16 * w + 4 * g;
    const f32x4 ug = *(const f32x4*)(uvl), uu = *(const f32x4*)(uvl + 256), vu = *(const f32x4*)(uvl + 384);
    const f32x4 vg = *(const f32x4*)(uvl + 128) * -1.4426950408889634f;
#pragma unroll
    for (int i = 0; i < 4; ++i) {
      pre(i);
      const u32 so = (u32)WS_HH + ((Lt * 8 + 4 * ps + ((i ^ w) & 3)) * 56 + 4 * Fi + wh) * 1024;
      const float m = bflo(stw[4 * ps + i]), rs = __builtin_amdgcn_rcpf(bfhi(stw[4 * ps + i]));
      const float rsg = rs * -1.4426950408889634f;
      const f32x2 nm = {-m, -m}, rs2 = {rs, rs}, rsg2 = {rsg, rsg};
      u32x2 o;
#pragma unroll
      for (int r = 0; r < 2; ++r) {
        const f32x2 gv = pkfma(pkfma(nm, half2of(ug, r), half2of(ag[i], r)), rsg2, half2of(vg, r));
        const f32x2 uv = pkfma(pkfma(nm, half2of(uu, r), half2of(au[i], r)), rs2, half2of(vu, r));
        const f32x2 e = pkadd1(f32x2{__builtin_amdgcn_exp2f(gv.x), __builtin_amdgcn_exp2f(gv.y)});
        const f32x2 h = f32x2{__builtin_amdgcn_rcpf(e.x), __builtin_amdgcn_rcpf(e.y)} * uv;
        o[r] = pkbf(h.x, h.y);
      }
      bstore8_sc1(o, ws, hoff, so);
      hook(i);
      __builtin_amdgcn_sched_barrier(0);
    }
  };
  vmw<C_SCHED.wait_x0>();
  asm volatile("s_barrier" ::: "memory");
#pragma unroll
  for (int i = 0; i < 4; ++i) rd_tile(0, i, xb[0][i]);
  sfor<0, 2>([&](auto PS) {
    constexpr int ps = PS.value;
    sfor<0, 14>([&](auto KS) {
      constexpr int ks = KS.value;
      if constexpr (ps == 0) { vmw<C_SCHED.wait_w[ks]>(); pin4(wf[2 * ks]); pin4(wf[2 * ks + 1]); }
      if constexpr (ps == 0 ? (ks == 3 || ks == 7 || (ks == 13 && !Y_LATE)) : ks == 7) {
        vmw<(ps ? C_SCHED.wait_y7 : ks == 3 ? C_SCHED.wait_x3 : ks == 7 ? C_SCHED.wait_x7 : C_SCHED.wait_x13)>();
        asm volatile("s_barrier" ::: "memory");
        if constexpr (ps == 1 && PF_D) pin(pfd);
        if constexpr (ps == 1) {
          if (w == 0) {
            u32 t0, t1;
            asm volatile("s_mov_b64 exec, 1\n\tv_mov_b32 %0, 0\n\tv_mov_b32 %1, 1\n\tglobal_atomic_add %0, %1, %2\n\ts_mov_b64 exec, -1" : "=&v"(t0), "=&v"(t1) : "s"(cnt) : "memory");
          }
        }
      }
      if constexpr (ps == 0 && ks == 13 && !Y_LATE) {
#pragma unroll
        for (int i = 0; i < 4; ++i) ta[i] -= (unsigned)(C_XH0 - C_XH1);
      }
      sfor<0, 4>([&](auto GI) {
        constexpr int i = GI.value;
        if constexpr (ks == 0) { mfma_first(ag[i], wf[0], xb[0][i]); mfma_first(au[i], wf[1], xb[0][i]); }
        else { mfma_acc(ag[i], wf[2 * ks], xb[ks & 1][i]); mfma_acc(au[i], wf[2 * ks + 1], xb[ks & 1][i]); }
        if constexpr ((ps == 0 && !Y_LATE) || ks < 13) rd_tile((ks + 1) % 14, i, xb[(ks + 1) & 1][i]);
        sfor<0, 2>([&](auto U) {
          constexpr COp o = U.value ? c_opB(ps, ks, i) : c_opA(ps, ks, i);
          if constexpr (o.kind == 1) ld_w(o.arg);
          if constexpr (o.kind == 2) hdma16<o.arg * 16384, C_XH0 + o.arg * 8192>(ws, xoff, xsrc(offw, 2 * o.arg), sl);
          if constexpr (o.kind == 6) hdma16<o.arg * 16384 + 128, C_XH1 + o.arg * 8192>(ws, xoff, xsrc(offw, 2 * o.arg), sl);
          if constexpr (o.kind == 3) ld_st(o.arg);
          if constexpr (o.kind == 4) ld_wd(o.arg);
          if constexpr (o.kind == 5)
            hdma16<0, C_RES>(ws, (u32)(lane >> 3) * 256 + (dch & 3) * 16 + (dch >> 2) * 128,
                             (u32)WS_X1T + (Lt * D + 64 * Q + 8 * w) * 256 + sub * 64, sl);
          if constexpr (o.kind == 7) gload4_sc1(cn0, (const void*)cnt, 0u);
          if constexpr (o.kind == 8) bload4(pfd, ws, (u32)min(lane, 55) * 128, swd + i7 * 7168);
        });
        __builtin_amdgcn_sched_barrier(0);
      });
    });
    mfma_fence();
    if constexpr (ps == 0) {
      SST(2);
      vmw<C_SCHED.wait_st0>();
      epilogue(PS, [&](int i) {
#pragma unroll
        for (int t = 0; t < WD_PRE / 4; ++t) ld_wd(i * (WD_PRE / 4) + t);
        if (Y_EPI && i < 3) {
          if (i == 0) hdma16<4 * 16384 + 128, C_XH1 + 4 * 8192>(ws, xoff, xsrc(offw, 8), sl);
          if (i == 1) hdma16<5 * 16384 + 128, C_XH1 + 5 * 8192>(ws, xoff, xsrc(offw, 10), sl);
          if (i == 2) hdma16<6 * 16384 + 128, C_XH1 + 6 * 8192>(ws, xoff, xsrc(offw, 12), sl);
        }
        if (WD_PRE || Y_EPI) __builtin_amdgcn_sched_barrier(0);
      }, [&](int i) {
      });
      if constexpr (Y_LATE) {
        vmw<C_SCHED.wait_y3>();
        if constexpr (PF_W) pin(pfw);
        sbarrier();
#pragma unroll
        for (int i = 0; i < 4; ++i) { ta[i] -= (unsigned)(C_XH0 - C_XH1); rd_tile(0, i, xb[0][i]); }
      }
      SST(3);
    }
  });
  SST(4);
  u32x4 hb[2][7], gbv;
  const int ilx = ROT ? Q & 1 : 0;
  u32 shl[2];
#pragma unroll
  for (int il = 0; il < 2; ++il) { shl[il] = (u32)WS_HH + ((Lt * 8 + 2 * sub + (il ^ ilx)) * 56 + 7 * w) * 1024; PINS(shl[il]); }
  auto ld_hq = [&](int ps, int q) {
    const int il = q / 7, j = q % 7;
    bload16_sc1(hb[il][j], ws, l16, shl[il] + jo[j], ps * 0);
  };
#define H_D 6
  auto gemm2 = [&](auto PS) {
    constexpr int ps = PS.value;
#pragma unroll
    for (int il = 0; il < 2; ++il) {
      f32x4 acc[4];
#pragma unroll
      for (int j = 0; j < 7; ++j) {
        const int q = 7 * il + j;
        if (ps == 0) vmwait(13 - q);
        else vmwait((q + H_D - 1 < 13 ? H_D - 1 : 13 - q) + (q < H_D ? 1 : 0));
        pin4(hb[il][j]);
#pragma unroll
        for (int id = 0; id < 4; ++id) { if (j == 0) mfma_first(acc[id], wd[id], hb[il][0]); else mfma_acc(acc[id], wd[4 * j + id], hb[il][j]); }
        if (ps == 1 && q + H_D < 14) ld_hq(1, q + H_D);
        __builtin_amdgcn_sched_barrier(0);
      }
      mfma_fence();
#pragma unroll
      for (int id = 0; id < 4; ++id)
        *(f32x4*)(smem + (ps ? C_XH1 : C_XH0) + ((w * 2 + (il ^ ilx)) * 4 + ((id + (ROT ? i7 : 0)) & 3)) * 1024 + l16) = acc[id];
      __builtin_amdgcn_sched_barrier(0);
    }
  };
  const int dcol = 64 * Q + 16 * wh + 4 * g;
  u32 gbo = (u32)dcol * 4;
  PIN(gbo);
  u32 a_f0 = lds0 + C_XH0 + ((w & 1) * 4 + wh) * 1024 + l16, a_f1 = a_f0 - (C_XH0 - C_XH1);
  const u32 a_res = lds0 + C_RES + (16 * wh + 4 * g + q4) * 128 + 8 * pp;
  const u32 r_phi = (u32)((q4 >> 1) | ((g >> 1) << 1));
  u32 a_r0 = a_res + 32 * ((u32)(w & 1) ^ r_phi), a_r1 = a_res + 32 * ((u32)(2 + (w & 1)) ^ r_phi);
  u32 o_off0 = (u32)((128 * Lt + 32 * sub + 16 * (w & 1) + (lane & 15)) * D + dcol) * 2, o_off1 = o_off0 + 64 * D * 2;
  auto finish = [&](auto PS) {
    constexpr int ps = PS.value;
    u32x4 pr[8];
    const u32 af = ps ? a_f1 : a_f0;
    sfor<0, 8>([&](auto S) { lds_rd128<S.value * 8192>(pr[S.value], af); });
    u32x2 xres;
    lds_rtr<0>(xres, ps ? a_r1 : a_r0);
    lgw<7>();
    pin4(pr[0]); pin4(pr[1]);
    f32x4 o = __builtin_bit_cast(f32x4, pr[0]) + __builtin_bit_cast(f32x4, pr[1]);
    sfor<2, 8>([&](auto S) {
      lgw<8 - S.value>();
      pin4(pr[S.value]);
      o += __builtin_bit_cast(f32x4, pr[S.value]);
    });
    const f32x4 gb = __builtin_bit_cast(f32x4, gbv);
    const f32x2 r0 = rnd2(half2of(gb, 0) * rnd2(half2of(o, 0))), r1 = rnd2(half2of(gb, 1) * rnd2(half2of(o, 1)));
    lgw<0>();
    pin2(xres);
    const f32x2 s0 = f32x2{bflo(xres[0]), bfhi(xres[0])} + r0, s1 = f32x2{bflo(xres[1]), bfhi(xres[1])} + r1;
    const u32x2 ov = {pkbf(s0.x, s0.y), pkbf(s1.x, s1.y)};
    gstore8(p.out, ps ? o_off1 : o_off0, ov);
  };
  vmw<C_SCHED.wait_st1>();
  SST(12);
  if (w >= 4) __builtin_amdgcn_s_setprio(PRIO);
  epilogue(IC<1>{}, [&](int) {}, [&](int i) { if (i == 0) SST(13); });
  __builtin_amdgcn_s_setprio(0);
  SST(5);
  vmw<4>();
  PIN(cn0);
  if (__builtin_amdgcn_readfirstlane(cn0) < 14u) {
#pragma unroll 1
    for (unsigned spins = 0; spins < (1u << 20); ++spins) {
      if (__hip_atomic_load(cnt, __ATOMIC_RELAXED, __HIP_MEMORY_SCOPE_AGENT) >= 14u) break;
      __builtin_amdgcn_s_sleep(1);
    }
  }
  bload16(gbv, ws, gbo, (u32)WS_MOD + 5 * D * 4, 0);
#pragma unroll
  for (int q = 0; q < NB1; ++q) ld_hq(0, q);
  vmw<NB1 + 1>();
  if (PUB == 0) {
    sbarrier();
    if (tid == 0) __hip_atomic_fetch_add(cnt + 32, 1u, __ATOMIC_RELAXED, __HIP_MEMORY_SCOPE_AGENT);
  } else {
    u32 t0, t1, t2;
    asm volatile("s_mov_b64 exec, 1\n\tv_mov_b32 %0, %3\n\tv_mov_b32 %1, 1\n\tds_add_rtn_u32 %2, %0, %1\n\tv_mov_b32 %0, 0\n\ts_waitcnt lgkmcnt(0)\n\t"
                 "v_cmp_eq_u32 vcc, 7, %2\n\ts_cbranch_vccz .Lpub%=\n\tglobal_atomic_add %0, %1, %4 offset:128\n.Lpub%=:\n\ts_mov_b64 exec, -1"
                 : "=&v"(t0), "=&v"(t1), "=&v"(t2) : "s"(lds0 + C_CTR), "s"(cnt) : "vcc", "memory");
  }
#pragma unroll
  for (int q = NB1; q < 14; ++q) ld_hq(0, q);
  SST(6);
#pragma unroll
  for (int n = 0; n < 28; ++n) PIN4(wd[n]);
  gemm2(IC<0>{});
  PIN4(gbv);
  SST(7);
  if (w == 0) {
#pragma unroll 1
    for (unsigned spins = 0; spins < (1u << 20); ++spins) {
      if (__hip_atomic_load(cnt + 32, __ATOMIC_RELAXED, __HIP_MEMORY_SCOPE_AGENT) >= 14u) break;
      __builtin_amdgcn_s_sleep(1);
    }
  }
  lds_barrier();
  SST(10);
  shl[0] += 4 * 56 * 1024; shl[1] += 4 * 56 * 1024;
#pragma unroll
  for (int q = 0; q < H_D; ++q) ld_hq(1, q);
  finish(IC<0>{});
  SST(9);
  gemm2(IC<1>{});
  SST(11);
  lds_barrier();
  finish(IC<1>{});
  SSTE(8); SEND(2, 14, bid);
}

extern "C" void kernel_launch(void* const* d_in, const int* in_sizes, int n_in, void* d_out, int out_size, void* d_ws,
                              size_t ws_size, hipStream_t stream) {
  Params p;
  p.u = (const u16*)d_in[0]; p.A_re = (const u16*)d_in[1]; p.A_im = (const u16*)d_in[2]; p.B_re = (const u16*)d_in[3];
  p.B_im = (const u16*)d_in[4]; p.C_re = (const u16*)d_in[5]; p.C_im = (const u16*)d_in[6]; p.log_dt = (const u16*)d_in[7];
  p.Dskip = (const u16*)d_in[8]; p.w_gate = (const u16*)d_in[10]; p.w_up = (const u16*)d_in[11]; p.w_down = (const u16*)d_in[12];
  p.c = (const u16*)d_in[13]; p.w_mod = (const u16*)d_in[14];
  p.ws = (char*)d_ws; p.out = (u16*)d_out;
  hipFuncSetAttribute((const void*)kAB, hipFuncAttributeMaxDynamicSharedMemorySize, AB_LDS);
  hipFuncSetAttribute((const void*)kCD, hipFuncAttributeMaxDynamicSharedMemorySize, CD_LDS);
  kAB<<<256, 512, AB_LDS, stream>>>(p);
  kCD<<<256, 512, CD_LDS, stream>>>(p);
}
```
